```python
import torch

def _softmax(x, axis=-1):
    return torch.softmax(x, dim=axis)
def _gelu(x):
    return torch.nn.functional.gelu(x, approximate="tanh")
def _silu(x):
    return torch.nn.functional.silu(x)
def _sigmoid(x):
    return torch.sigmoid(x)
def _layernorm(x, axis=-1, eps=1e-5):
    m = x.mean(dim=axis, keepdim=True)
    v = x.var(dim=axis, keepdim=True, unbiased=False)
    return (x - m) / torch.sqrt(v + eps)
def _scatter_add(tgt, idx, src, axis=0):
    return tgt.clone().index_add_(axis, idx, src)
def _segment_sum(src, seg, n):
    out = torch.zeros((n,) + src.shape[1:], dtype=src.dtype)
    return out.index_add_(0, seg, src)
def _pad(x, pw):
    flat = tuple(v for pair in reversed(pw) for v in pair)
    return torch.nn.functional.pad(x, flat)

kernel_name = 'gated_mix__ssm_conv_h3__compressed_kv_cla__prompt'

def setup_inputs():

    _w = (lambda a: (lambda t: t.to(torch.bfloat16) if t.is_floating_point() else t)(torch.as_tensor(a)))
    def _A_setup():
        B, S, D = 1, 2048, 448
        ks = 4
        return {
            "q": _w((lambda *s: torch.randn(*s, dtype=torch.bfloat16) * 0.05)(B, S, D)),
            "k": _w((lambda *s: torch.randn(*s, dtype=torch.bfloat16) * 0.4)(B, S, D)),
            "v": _w((lambda *s: torch.randn(*s, dtype=torch.bfloat16) * 0.4)(B, S, D)),
            "shift_k": _w((lambda *s: torch.randn(*s, dtype=torch.bfloat16) * 0.4)(D, ks) * 0.5),
            "ssm_k": _w((lambda *s: torch.randn(*s, dtype=torch.bfloat16) * 0.8)(D, S) * (1.0 / S ** 0.5)),
            "ssm_D": _w((lambda *s: torch.randn(*s, dtype=torch.bfloat16) * 0.4)(D)),
        }
    _A_in = _A_setup()

    def _B_setup():
        B, H, Sq, Sk, D, M = 1, 16, 2048, 2048, 256, 448
        Dc = 4 * D
        return {
            "x": _w((lambda *s: torch.randn(*s, dtype=torch.bfloat16) * 0.05)(B, Sq, M)),
            "c_kv": _w((lambda *s: torch.randn(*s, dtype=torch.bfloat16) * 0.4)(B, Sk, Dc)),
            "w_q": _w((lambda *s: torch.randn(*s, dtype=torch.bfloat16) * 6.4)(M, H, D) * (M ** -0.5)),
            "w_uk": _w((lambda *s: torch.randn(*s, dtype=torch.bfloat16) * 6.4)(Dc, H, D) * (Dc ** -0.5)),
            "w_uv": _w((lambda *s: torch.randn(*s, dtype=torch.bfloat16) * 0.4)(Dc, H, D) * (Dc ** -0.5)),
            "w_o": _w((lambda *s: torch.randn(*s, dtype=torch.bfloat16) * 0.4)(H, D, M) * ((H * D) ** -0.5)),
        }
    _B_in = _B_setup()

    return {
        "A_q": _A_in["q"],
        "A_k": _A_in["k"],
        "A_v": _A_in["v"],
        "A_shift_k": _A_in["shift_k"],
        "A_ssm_k": _A_in["ssm_k"],
        "A_ssm_D": _A_in["ssm_D"],
        "B_x": _B_in["x"],
        "B_c_kv": _B_in["c_kv"],
        "B_w_q": _B_in["w_q"],
        "B_w_uk": _B_in["w_uk"],
        "B_w_uv": _B_in["w_uv"],
        "B_w_o": _B_in["w_o"],
        "gate_w": _w((lambda *s: torch.randn(*s, dtype=torch.bfloat16) * 0.05)(448) * 16.0),
    }

def reference(A_q, A_k, A_v, A_shift_k, A_ssm_k, A_ssm_D, B_x, B_c_kv, B_w_q, B_w_uk, B_w_uv, B_w_o, gate_w):
    def _A_ref(q, k, v, shift_k, ssm_k, ssm_D):
        B, S, D = q.shape
        ks = shift_k.shape[-1]
        k_pad = _pad(k, ((0, 0), (ks - 1, 0), (0, 0)))
        k_shift = torch.zeros_like(k)
        for t in range(ks):
            k_shift = k_shift + k_pad[:, t:t + S, :] * shift_k[:, ks - 1 - t][None, None, :]
        kv = k_shift * v
        L = 2 * S
        kv_pad = _pad(kv, ((0, 0), (0, S), (0, 0)))
        kf_pad = _pad(ssm_k, ((0, 0), (0, S)))
        kv_f = (lambda x, axes=(-2,-1): torch.fft.rfft2(x.float(), dim=axes))(kv_pad[:, None, :, :], axes=(1, 2))
        kf_f = (lambda x, axes=(-2,-1): torch.fft.rfft2(x.float(), dim=axes))(kf_pad[:, None, :], axes=(1, 2))
        kf_f = (lambda x, *d: x.permute(*d))(kf_f, 1, 2, 0)
        y_f = kv_f * kf_f[None, :, :, :]
        y = (lambda x, s, axes=(-2,-1): torch.fft.irfft2(x, s=s, dim=axes).to(torch.bfloat16))(y_f, (1, L), axes=(1, 2))[:, 0, :S, :]
        y = y + kv * ssm_D[None, None, :]
        return q * y

    def _B_ref(x, c_kv, w_q, w_uk, w_uv, w_o):
        B, Sq, M = x.shape[0], x.shape[1], x.shape[2]
        Sk = c_kv.shape[1]
        H, D = w_q.shape[1], w_q.shape[2]
        q = torch.einsum("bqm,mhd->bhqd", x, w_q)
        k = torch.einsum("bkc,chd->bhkd", c_kv, w_uk)
        v = torch.einsum("bkc,chd->bhkd", c_kv, w_uv)
        scores = torch.einsum("bhqd,bhkd->bhqk", q, k) * (D ** -0.5)
        off = Sk - Sq
        qi = torch.arange(Sq)[:, None]
        kj = torch.arange(Sk)[None, :]
        cmask = kj <= (qi + off)
        scores = torch.where(cmask[None, None], scores, -1e9)
        p = _softmax(scores, axis=-1)
        ctx = torch.einsum("bhqk,bhkd->bhqd", p, v)
        return torch.einsum("bhqd,hdm->bqm", ctx, w_o)

    x = A_q
    g = _sigmoid((x * gate_w).sum(-1, keepdims=True))
    a = _A_ref(x, A_k, A_v, A_shift_k, A_ssm_k, A_ssm_D)
    b = _B_ref(x, B_c_kv, B_w_q, B_w_uk, B_w_uv, B_w_o)
    return g * a + (1 - g) * b
```

```cpp
#include <hip/hip_runtime.h>

typedef __attribute__((ext_vector_type(8))) __bf16 bf16x8;
typedef __attribute__((ext_vector_type(4))) float f32x4;
typedef __attribute__((ext_vector_type(4))) unsigned u32x4;
typedef __attribute__((ext_vector_type(2))) unsigned u32x2;
typedef unsigned short u16;
#define WS_WOF (64u << 20)
#define WS_ZT  (68u << 20)
#define WS_KVT (72u << 20)
#define WS_FLAG (76u << 20)
#define WS_G   (70u << 20)
#define WS_CTXB (48u << 20)
__device__ __forceinline__ float bf2f(unsigned h) { return __uint_as_float(h << 16); }
typedef __attribute__((ext_vector_type(2))) __bf16 bf16x2;
typedef __attribute__((ext_vector_type(2))) float f32x2;
__device__ __forceinline__ unsigned pk2(float a, float b) {
  f32x2 v = {a, b};
  bf16x2 r = __builtin_convertvector(v, bf16x2);
  return __builtin_bit_cast(unsigned, r);
}
__device__ __forceinline__ unsigned f2bf(float f) { return pk2(f, 0.f) & 0xffffu; }
__device__ __forceinline__ float rbf(float f) { return __uint_as_float(pk2(f, f) << 16); }
__device__ __forceinline__ void st16(void* p, u32x4 v) { asm volatile("global_store_dwordx4 %0, %1, off sc1\n s_nop 1" :: "v"(p), "v"(v) : "memory"); }
__device__ __forceinline__ void st8(void* p, u32x2 v) { asm volatile("global_store_dwordx2 %0, %1, off sc1\n s_nop 1" :: "v"(p), "v"(v) : "memory"); }
__device__ __forceinline__ void st4(void* p, float v) { asm volatile("global_store_dword %0, %1, off sc1\n s_nop 1" :: "v"(p), "v"(v) : "memory"); }
__device__ __forceinline__ float lo16(unsigned d) { return __uint_as_float(d << 16); }
__device__ __forceinline__ float hi16(unsigned d) { return __uint_as_float(d & 0xffff0000u); }

#define CV_CS 4112
#define CV_U  (8 * CV_CS)
#define CV_CH (CV_U + 286 * 16)
__device__ __forceinline__ void conv_job(int cg, char* lds, const u16* ssk, const u16* ssd, char* ws) {
  const int tid = threadIdx.x, lane = tid & 63, w = tid >> 6;
  const int ch = cg * 4 + w;
  const u16* hp = ssk + (size_t)ch * 2048;
  const auto rs = __builtin_amdgcn_make_buffer_rsrc(ws, 0, 80u << 20, 0x00020000);
  u32x4 hc[4], hm[4], uc[4];
#pragma unroll
  for (int i = 0; i < 4; i++) {
    int m = lane + 64 * i;
    hc[i] = *(const u32x4*)(hp + 8 * m);
    hm[i] = *(const u32x4*)(hp + 8 * (m > 0 ? m - 1 : 0));
    uc[i] = __builtin_amdgcn_raw_buffer_load_b128(rs, WS_KVT + (unsigned)ch * 4096 + 16 * m, 0, 16);
  }
  const float dd = bf2f(ssd[ch]);
  char* my = lds + w * CV_CH;
  {
    u32x4 z = {0u, 0u, 0u, 0u};
    if (lane == 0) hm[0] = z;
    if (lane < 30) *(u32x4*)(my + CV_U + lane * 16) = z;
    if (lane < 8) *(u32x4*)(my + lane * CV_CS) = z;
  }
#pragma unroll
  for (int i = 0; i < 4; i++) {
    int m = lane + 64 * i;
    unsigned d[8] = {hm[i][0], hm[i][1], hm[i][2], hm[i][3], hc[i][0], hc[i][1], hc[i][2], hc[i][3]};
#pragma unroll
    for (int sg = 0; sg < 8; sg++) {
      u32x4 o;
#pragma unroll
      for (int q = 0; q < 4; q++) {
        if (sg & 1) { unsigned x = d[(7 + sg) / 2 - q]; o[q] = (x >> 16) | (x << 16); }
        else { int a = (8 + sg) / 2 - q; o[q] = (d[a] & 0xffffu) | (d[a - 1] & 0xffff0000u); }
      }
      *(u32x4*)(my + sg * CV_CS + (m + 1) * 16) = o;
    }
    *(u32x4*)(my + CV_U + (30 + m) * 16) = uc[i];
  }
  __builtin_amdgcn_fence(__ATOMIC_RELEASE, "wavefront");
  __builtin_amdgcn_wave_barrier();
  __builtin_amdgcn_fence(__ATOMIC_ACQUIRE, "wavefront");
  const int r = lane & 15, g = lane >> 4;
  const char* pa = my + (r & 7) * CV_CS + (g + (r >> 3)) * 16;
  const char* pb = my + CV_U + (30 + 2 * r + 1 - g) * 16;
  f32x4 acc[8];
#pragma unroll
  for (int n = 0; n < 8; n++) acc[n] = (f32x4){0.f, 0.f, 0.f, 0.f};
#pragma unroll
  for (int rho = 0; rho < 8; rho++) {
    bf16x8 A[8], B[8];
#pragma unroll
    for (int u = 0; u < 8; u++) {
      A[u] = *(const bf16x8*)(pa + (8 * u + rho) * 64);
      B[u] = *(const bf16x8*)(pb + (8 * u - rho) * 64);
    }
#pragma unroll
    for (int mu = 0; mu < 8; mu++)
#pragma unroll
      for (int nu = 0; nu + mu < 8; nu++)
        acc[mu + nu] = __builtin_amdgcn_mfma_f32_16x16x32_bf16(A[mu], B[nu], acc[mu + nu], 0, 0, 0);
  }
#pragma unroll
  for (int n = 0; n < 8; n++) {
    int t = 256 * n + 16 * r + 4 * g;
    u32x2 kv = *(const u32x2*)(my + CV_U + 480 + t * 2);
    unsigned y0 = pk2(acc[n][0], acc[n][1]), y1 = pk2(acc[n][2], acc[n][3]);
    unsigned e0 = pk2(lo16(kv[0]) * dd, hi16(kv[0]) * dd), e1 = pk2(lo16(kv[1]) * dd, hi16(kv[1]) * dd);
    u32x2 o = {pk2(lo16(y0) + lo16(e0), hi16(y0) + hi16(e0)), pk2(lo16(y1) + lo16(e1), hi16(y1) + hi16(e1))};
    *(u32x2*)(my + t * 2) = o;
  }
  __syncthreads();
#pragma unroll
  for (int n = 0; n < 4; n++) {
    int t = 512 * n + 2 * tid;
    unsigned c0 = *(const unsigned*)(lds + t * 2), c1 = *(const unsigned*)(lds + CV_CH + t * 2);
    unsigned c2 = *(const unsigned*)(lds + 2 * CV_CH + t * 2), c3 = *(const unsigned*)(lds + 3 * CV_CH + t * 2);
    u32x4 o = {(c0 & 0xffffu) | (c1 << 16), (c2 & 0xffffu) | (c3 << 16), (c0 >> 16) | (c1 & 0xffff0000u), (c2 >> 16) | (c3 & 0xffff0000u)};
    st16(ws + WS_ZT + ((size_t)cg * 2048 + t) * 8, o);
  }
  asm volatile("s_waitcnt vmcnt(0)" ::: "memory");
  __syncthreads();
  if (tid == 0) st4((float*)(ws + WS_FLAG + 1024u) + cg, 1.0f);
}


#define FL_C (WS_FLAG + 1024u)
#define FL_G (WS_FLAG + 1536u)
#define FL_X (WS_FLAG + 3072u)
__device__ __forceinline__ void gate_job(int k, const u16* x, const u16* gw, char* ws) {
  const int tid = threadIdx.x, lane = tid & 63, w = tid >> 6;
  const int c = lane & 15, g = lane >> 4;
  u32x4 gv[4];
#pragma unroll
  for (int i = 0; i < 4; i++) { int ch = c + 16 * i; ch = ch < 56 ? ch : 55; gv[i] = *(const u32x4*)(gw + ch * 8); }
  u32x4 xv[8][4];
#pragma unroll
  for (int ps = 0; ps < 8; ps++)
#pragma unroll
    for (int i = 0; i < 4; i++) {
      int ch = c + 16 * i; ch = ch < 56 ? ch : 55;
      xv[ps][i] = *(const u32x4*)(x + (size_t)(k * 128 + w * 32 + 8 * g + ps) * 448 + ch * 8);
    }
  float sel = 0.f;
#pragma unroll
  for (int ps = 0; ps < 8; ps++) {
    float s = 0.f;
#pragma unroll
    for (int i = 0; i < 4; i++) {
      float u = 0.f;
#pragma unroll
      for (int q = 0; q < 4; q++) {
        unsigned pr = pk2(lo16(xv[ps][i][q]) * lo16(gv[i][q]), hi16(xv[ps][i][q]) * hi16(gv[i][q]));
        u += lo16(pr) + hi16(pr);
      }
      s += (c + 16 * i < 56) ? u : 0.f;
    }
#pragma unroll
    for (int o = 8; o >= 1; o >>= 1) s += __shfl_xor(s, o);
    sel = (c == ps) ? s : sel;
  }
  sel = rbf(sel);
  const float gg = rbf(1.0f / (1.0f + __expf(-sel)));
  if (c < 8) st4((float*)(ws + WS_G) + k * 128 + w * 32 + 8 * g + c, gg);
  asm volatile("s_waitcnt vmcnt(0)" ::: "memory");
  __syncthreads();
  if (tid == 0) st4((float*)(ws + FL_G) + k, 1.0f);
}
__device__ __forceinline__ void otile_job(int bid, char* lds, const u16* x, const u16* gw, char* ws, u16* out) {
  const int tid = threadIdx.x, lane = tid & 63, w = __builtin_amdgcn_readfirstlane(tid >> 6);
  const int xcd = bid & 7, idx = bid >> 3, i14 = (idx & 15) - 2;
  if (i14 < 0) {
    if (idx < 2) gate_job(2 * xcd + idx, x, gw, ws);
    return;
  }
  const int rb = (idx & 16) + 2 * xcd + (i14 >= 7 ? 1 : 0), cb = i14 >= 7 ? i14 - 7 : i14;
  const int c = lane & 15, g = lane >> 4;
  const int q = rb * 64 + w * 16 + c;
  const unsigned oc = WS_CTXB + ((unsigned)(rb * 4) * 128 + w * 32) * 1024;
  const unsigned ow = WS_WOF + ((unsigned)cb * 128 + w * 32) * 4096;
  const unsigned ofx = FL_X + ((unsigned)(rb >> 1) * 16 + 4 * w) * 4;
  const unsigned ofc = g == 0 ? FL_C + (unsigned)cb * 64 + 4 * c : FL_G + (unsigned)(rb >> 1) * 4;
  const unsigned oz = WS_ZT + ((unsigned)(cb * 16 + g) * 2048 + q) * 8;
  const size_t oo = (size_t)q * 448 + cb * 64 + 4 * g;
  u32x2 z0, z1, z2, z3, x0, x1, x2, x3;
  float gg;
  asm volatile(R"ASM(
s_mov_b64 s[24:25], %9
s_and_b32 s25, s25, 0xffff
s_mov_b32 s26, 83886080
s_mov_b32 s27, 0x20000
s_mov_b32 s28, %10
s_mov_b32 s29, %11
s_mov_b32 s30, %12
v_mov_b32 v32, %14
v_add_u32 v33, 131072, %14
v_add_u32 v34, 262144, %14
v_add_u32 v35, 393216, %14
v_lshrrev_b32 v36, 2, %14
v_and_b32 v38, 12, v36
buffer_load_dword v37, v38, s[24:27], s30 offen sc1
buffer_load_dwordx4 v[40:43], v32, s[24:27], s29 offen offset:0 sc1
buffer_load_dwordx4 v[44:47], v32, s[24:27], s29 offen offset:1024 sc1
buffer_load_dwordx4 v[48:51], v32, s[24:27], s29 offen offset:2048 sc1
buffer_load_dwordx4 v[52:55], v32, s[24:27], s29 offen offset:3072 sc1
s_add_u32 s29, s29, 4096
buffer_load_dwordx4 v[56:59], v32, s[24:27], s29 offen offset:0 sc1
buffer_load_dwordx4 v[60:63], v32, s[24:27], s29 offen offset:1024 sc1
buffer_load_dwordx4 v[64:67], v32, s[24:27], s29 offen offset:2048 sc1
buffer_load_dwordx4 v[68:71], v32, s[24:27], s29 offen offset:3072 sc1
s_add_u32 s29, s29, 4096
buffer_load_dwordx4 v[72:75], v32, s[24:27], s29 offen offset:0 sc1
buffer_load_dwordx4 v[76:79], v32, s[24:27], s29 offen offset:1024 sc1
buffer_load_dwordx4 v[80:83], v32, s[24:27], s29 offen offset:2048 sc1
buffer_load_dwordx4 v[84:87], v32, s[24:27], s29 offen offset:3072 sc1
s_add_u32 s29, s29, 4096
buffer_load_dwordx4 v[88:91], v32, s[24:27], s29 offen offset:0 sc1
buffer_load_dwordx4 v[92:95], v32, s[24:27], s29 offen offset:1024 sc1
buffer_load_dwordx4 v[96:99], v32, s[24:27], s29 offen offset:2048 sc1
buffer_load_dwordx4 v[100:103], v32, s[24:27], s29 offen offset:3072 sc1
s_add_u32 s29, s29, 4096
buffer_load_dwordx4 v[104:107], v32, s[24:27], s29 offen offset:0 sc1
buffer_load_dwordx4 v[108:111], v32, s[24:27], s29 offen offset:1024 sc1
buffer_load_dwordx4 v[112:115], v32, s[24:27], s29 offen offset:2048 sc1
buffer_load_dwordx4 v[116:119], v32, s[24:27], s29 offen offset:3072 sc1
s_add_u32 s29, s29, 4096
buffer_load_dwordx4 v[120:123], v32, s[24:27], s29 offen offset:0 sc1
buffer_load_dwordx4 v[124:127], v32, s[24:27], s29 offen offset:1024 sc1
buffer_load_dwordx4 v[128:131], v32, s[24:27], s29 offen offset:2048 sc1
buffer_load_dwordx4 v[132:135], v32, s[24:27], s29 offen offset:3072 sc1
s_add_u32 s29, s29, 4096
buffer_load_dwordx4 v[136:139], v32, s[24:27], s29 offen offset:0 sc1
buffer_load_dwordx4 v[140:143], v32, s[24:27], s29 offen offset:1024 sc1
buffer_load_dwordx4 v[144:147], v32, s[24:27], s29 offen offset:2048 sc1
buffer_load_dwordx4 v[148:151], v32, s[24:27], s29 offen offset:3072 sc1
s_add_u32 s29, s29, 4096
buffer_load_dwordx4 v[152:155], v32, s[24:27], s29 offen offset:0 sc1
buffer_load_dwordx4 v[156:159], v32, s[24:27], s29 offen offset:1024 sc1
buffer_load_dwordx4 v[160:163], v32, s[24:27], s29 offen offset:2048 sc1
buffer_load_dwordx4 v[164:167], v32, s[24:27], s29 offen offset:3072 sc1
s_add_u32 s29, s29, 4096
buffer_load_dwordx4 v[168:171], v32, s[24:27], s29 offen offset:0 sc1
buffer_load_dwordx4 v[172:175], v32, s[24:27], s29 offen offset:1024 sc1
buffer_load_dwordx4 v[176:179], v32, s[24:27], s29 offen offset:2048 sc1
buffer_load_dwordx4 v[180:183], v32, s[24:27], s29 offen offset:3072 sc1
s_add_u32 s29, s29, 4096
buffer_load_dwordx4 v[184:187], v32, s[24:27], s29 offen offset:0 sc1
buffer_load_dwordx4 v[188:191], v32, s[24:27], s29 offen offset:1024 sc1
buffer_load_dwordx4 v[192:195], v32, s[24:27], s29 offen offset:2048 sc1
buffer_load_dwordx4 v[196:199], v32, s[24:27], s29 offen offset:3072 sc1
s_add_u32 s29, s29, 4096
buffer_load_dwordx4 v[200:203], v32, s[24:27], s29 offen offset:0 sc1
buffer_load_dwordx4 v[204:207], v32, s[24:27], s29 offen offset:1024 sc1
buffer_load_dwordx4 v[208:211], v32, s[24:27], s29 offen offset:2048 sc1
buffer_load_dwordx4 v[212:215], v32, s[24:27], s29 offen offset:3072 sc1
s_add_u32 s29, s29, 4096
buffer_load_dwordx4 v[216:219], v32, s[24:27], s29 offen offset:0 sc1
buffer_load_dwordx4 v[220:223], v32, s[24:27], s29 offen offset:1024 sc1
buffer_load_dwordx4 v[224:227], v32, s[24:27], s29 offen offset:2048 sc1
buffer_load_dwordx4 v[228:231], v32, s[24:27], s29 offen offset:3072 sc1
s_add_u32 s29, s29, 4096
buffer_load_dwordx4 v[232:235], v32, s[24:27], s29 offen offset:0 sc1
buffer_load_dwordx4 v[236:239], v32, s[24:27], s29 offen offset:1024 sc1
buffer_load_dwordx4 v[240:243], v32, s[24:27], s29 offen offset:2048 sc1
buffer_load_dwordx4 v[244:247], v32, s[24:27], s29 offen offset:3072 sc1
s_add_u32 s29, s29, 4096
buffer_load_dwordx4 v[248:251], v32, s[24:27], s29 offen offset:0 sc1
buffer_load_dwordx4 v[252:255], v32, s[24:27], s29 offen offset:1024 sc1
buffer_load_dwordx4 a[64:67], v32, s[24:27], s29 offen offset:2048 sc1
buffer_load_dwordx4 a[68:71], v32, s[24:27], s29 offen offset:3072 sc1
s_add_u32 s29, s29, 4096
buffer_load_dwordx4 a[72:75], v32, s[24:27], s29 offen offset:0 sc1
buffer_load_dwordx4 a[76:79], v32, s[24:27], s29 offen offset:1024 sc1
buffer_load_dwordx4 a[80:83], v32, s[24:27], s29 offen offset:2048 sc1
buffer_load_dwordx4 a[84:87], v32, s[24:27], s29 offen offset:3072 sc1
s_add_u32 s29, s29, 4096
buffer_load_dwordx4 a[88:91], v32, s[24:27], s29 offen offset:0 sc1
buffer_load_dwordx4 a[92:95], v32, s[24:27], s29 offen offset:1024 sc1
buffer_load_dwordx4 a[96:99], v32, s[24:27], s29 offen offset:2048 sc1
buffer_load_dwordx4 a[100:103], v32, s[24:27], s29 offen offset:3072 sc1
s_add_u32 s29, s29, 4096
buffer_load_dwordx4 a[104:107], v32, s[24:27], s29 offen offset:0 sc1
buffer_load_dwordx4 a[108:111], v32, s[24:27], s29 offen offset:1024 sc1
buffer_load_dwordx4 a[112:115], v32, s[24:27], s29 offen offset:2048 sc1
buffer_load_dwordx4 a[116:119], v32, s[24:27], s29 offen offset:3072 sc1
s_add_u32 s29, s29, 4096
buffer_load_dwordx4 a[120:123], v32, s[24:27], s29 offen offset:0 sc1
buffer_load_dwordx4 a[124:127], v32, s[24:27], s29 offen offset:1024 sc1
buffer_load_dwordx4 a[128:131], v32, s[24:27], s29 offen offset:2048 sc1
buffer_load_dwordx4 a[132:135], v32, s[24:27], s29 offen offset:3072 sc1
s_add_u32 s29, s29, 4096
buffer_load_dwordx4 a[136:139], v32, s[24:27], s29 offen offset:0 sc1
buffer_load_dwordx4 a[140:143], v32, s[24:27], s29 offen offset:1024 sc1
buffer_load_dwordx4 a[144:147], v32, s[24:27], s29 offen offset:2048 sc1
buffer_load_dwordx4 a[148:151], v32, s[24:27], s29 offen offset:3072 sc1
s_add_u32 s29, s29, 4096
v_accvgpr_write_b32 a0, 0
v_accvgpr_write_b32 a1, 0
v_accvgpr_write_b32 a2, 0
v_accvgpr_write_b32 a3, 0
v_accvgpr_write_b32 a4, 0
v_accvgpr_write_b32 a5, 0
v_accvgpr_write_b32 a6, 0
v_accvgpr_write_b32 a7, 0
v_accvgpr_write_b32 a8, 0
v_accvgpr_write_b32 a9, 0
v_accvgpr_write_b32 a10, 0
v_accvgpr_write_b32 a11, 0
v_accvgpr_write_b32 a12, 0
v_accvgpr_write_b32 a13, 0
v_accvgpr_write_b32 a14, 0
v_accvgpr_write_b32 a15, 0
v_accvgpr_write_b32 a16, 0
v_accvgpr_write_b32 a17, 0
v_accvgpr_write_b32 a18, 0
v_accvgpr_write_b32 a19, 0
v_accvgpr_write_b32 a20, 0
v_accvgpr_write_b32 a21, 0
v_accvgpr_write_b32 a22, 0
v_accvgpr_write_b32 a23, 0
v_accvgpr_write_b32 a24, 0
v_accvgpr_write_b32 a25, 0
v_accvgpr_write_b32 a26, 0
v_accvgpr_write_b32 a27, 0
v_accvgpr_write_b32 a28, 0
v_accvgpr_write_b32 a29, 0
v_accvgpr_write_b32 a30, 0
v_accvgpr_write_b32 a31, 0
v_accvgpr_write_b32 a32, 0
v_accvgpr_write_b32 a33, 0
v_accvgpr_write_b32 a34, 0
v_accvgpr_write_b32 a35, 0
v_accvgpr_write_b32 a36, 0
v_accvgpr_write_b32 a37, 0
v_accvgpr_write_b32 a38, 0
v_accvgpr_write_b32 a39, 0
v_accvgpr_write_b32 a40, 0
v_accvgpr_write_b32 a41, 0
v_accvgpr_write_b32 a42, 0
v_accvgpr_write_b32 a43, 0
v_accvgpr_write_b32 a44, 0
v_accvgpr_write_b32 a45, 0
v_accvgpr_write_b32 a46, 0
v_accvgpr_write_b32 a47, 0
v_accvgpr_write_b32 a48, 0
v_accvgpr_write_b32 a49, 0
v_accvgpr_write_b32 a50, 0
v_accvgpr_write_b32 a51, 0
v_accvgpr_write_b32 a52, 0
v_accvgpr_write_b32 a53, 0
v_accvgpr_write_b32 a54, 0
v_accvgpr_write_b32 a55, 0
v_accvgpr_write_b32 a56, 0
v_accvgpr_write_b32 a57, 0
v_accvgpr_write_b32 a58, 0
v_accvgpr_write_b32 a59, 0
v_accvgpr_write_b32 a60, 0
v_accvgpr_write_b32 a61, 0
v_accvgpr_write_b32 a62, 0
v_accvgpr_write_b32 a63, 0
s_mov_b32 s36, 0x8000
s_waitcnt vmcnt(63)
L_o_chk:
v_cmp_eq_u32 vcc, 0, v37
s_sub_u32 s36, s36, 1
s_cbranch_scc1 L_o_ok
s_cbranch_vccz L_o_ok
s_sleep 4
buffer_load_dword v37, v38, s[24:27], s30 offen sc1
s_waitcnt vmcnt(0)
s_branch L_o_chk
L_o_ok:
buffer_load_dwordx4 a[152:155], v32, s[24:27], s28 offen offset:0 sc1
buffer_load_dwordx4 a[156:159], v33, s[24:27], s28 offen offset:0 sc1
buffer_load_dwordx4 a[160:163], v34, s[24:27], s28 offen offset:0 sc1
buffer_load_dwordx4 a[164:167], v35, s[24:27], s28 offen offset:0 sc1
buffer_load_dwordx4 a[168:171], v32, s[24:27], s28 offen offset:1024 sc1
buffer_load_dwordx4 a[172:175], v33, s[24:27], s28 offen offset:1024 sc1
buffer_load_dwordx4 a[176:179], v34, s[24:27], s28 offen offset:1024 sc1
buffer_load_dwordx4 a[180:183], v35, s[24:27], s28 offen offset:1024 sc1
buffer_load_dwordx4 a[184:187], v32, s[24:27], s28 offen offset:2048 sc1
buffer_load_dwordx4 a[188:191], v33, s[24:27], s28 offen offset:2048 sc1
buffer_load_dwordx4 a[192:195], v34, s[24:27], s28 offen offset:2048 sc1
buffer_load_dwordx4 a[196:199], v35, s[24:27], s28 offen offset:2048 sc1
buffer_load_dwordx4 a[200:203], v32, s[24:27], s28 offen offset:3072 sc1
buffer_load_dwordx4 a[204:207], v33, s[24:27], s28 offen offset:3072 sc1
buffer_load_dwordx4 a[208:211], v34, s[24:27], s28 offen offset:3072 sc1
buffer_load_dwordx4 a[212:215], v35, s[24:27], s28 offen offset:3072 sc1
s_add_u32 s28, s28, 4096
buffer_load_dwordx4 a[216:219], v32, s[24:27], s28 offen offset:0 sc1
buffer_load_dwordx4 a[220:223], v33, s[24:27], s28 offen offset:0 sc1
buffer_load_dwordx4 a[224:227], v34, s[24:27], s28 offen offset:0 sc1
buffer_load_dwordx4 a[228:231], v35, s[24:27], s28 offen offset:0 sc1
buffer_load_dwordx4 a[232:235], v32, s[24:27], s28 offen offset:1024 sc1
buffer_load_dwordx4 a[236:239], v33, s[24:27], s28 offen offset:1024 sc1
buffer_load_dwordx4 a[240:243], v34, s[24:27], s28 offen offset:1024 sc1
buffer_load_dwordx4 a[244:247], v35, s[24:27], s28 offen offset:1024 sc1
s_waitcnt vmcnt(20)
v_mfma_f32_16x16x32_bf16 a[0:3], v[40:43], a[152:155], a[0:3]
v_mfma_f32_16x16x32_bf16 a[4:7], v[44:47], a[152:155], a[4:7]
v_mfma_f32_16x16x32_bf16 a[8:11], v[48:51], a[152:155], a[8:11]
v_mfma_f32_16x16x32_bf16 a[12:15], v[52:55], a[152:155], a[12:15]
v_mfma_f32_16x16x32_bf16 a[16:19], v[40:43], a[156:159], a[16:19]
v_mfma_f32_16x16x32_bf16 a[20:23], v[44:47], a[156:159], a[20:23]
v_mfma_f32_16x16x32_bf16 a[24:27], v[48:51], a[156:159], a[24:27]
v_mfma_f32_16x16x32_bf16 a[28:31], v[52:55], a[156:159], a[28:31]
v_mfma_f32_16x16x32_bf16 a[32:35], v[40:43], a[160:163], a[32:35]
v_mfma_f32_16x16x32_bf16 a[36:39], v[44:47], a[160:163], a[36:39]
v_mfma_f32_16x16x32_bf16 a[40:43], v[48:51], a[160:163], a[40:43]
v_mfma_f32_16x16x32_bf16 a[44:47], v[52:55], a[160:163], a[44:47]
v_mfma_f32_16x16x32_bf16 a[48:51], v[40:43], a[164:167], a[48:51]
v_mfma_f32_16x16x32_bf16 a[52:55], v[44:47], a[164:167], a[52:55]
v_mfma_f32_16x16x32_bf16 a[56:59], v[48:51], a[164:167], a[56:59]
v_mfma_f32_16x16x32_bf16 a[60:63], v[52:55], a[164:167], a[60:63]
s_waitcnt vmcnt(16)
v_mfma_f32_16x16x32_bf16 a[0:3], v[56:59], a[168:171], a[0:3]
v_mfma_f32_16x16x32_bf16 a[4:7], v[60:63], a[168:171], a[4:7]
v_mfma_f32_16x16x32_bf16 a[8:11], v[64:67], a[168:171], a[8:11]
buffer_load_dwordx4 a[248:251], v32, s[24:27], s28 offen offset:2048 sc1
v_mfma_f32_16x16x32_bf16 a[12:15], v[68:71], a[168:171], a[12:15]
buffer_load_dwordx4 a[252:255], v33, s[24:27], s28 offen offset:2048 sc1
v_mfma_f32_16x16x32_bf16 a[16:19], v[56:59], a[172:175], a[16:19]
buffer_load_dwordx4 a[152:155], v34, s[24:27], s28 offen offset:2048 sc1
v_mfma_f32_16x16x32_bf16 a[20:23], v[60:63], a[172:175], a[20:23]
buffer_load_dwordx4 a[156:159], v35, s[24:27], s28 offen offset:2048 sc1
v_mfma_f32_16x16x32_bf16 a[24:27], v[64:67], a[172:175], a[24:27]
buffer_load_dwordx4 a[160:163], v32, s[24:27], s28 offen offset:3072 sc1
v_mfma_f32_16x16x32_bf16 a[28:31], v[68:71], a[172:175], a[28:31]
buffer_load_dwordx4 a[164:167], v33, s[24:27], s28 offen offset:3072 sc1
v_mfma_f32_16x16x32_bf16 a[32:35], v[56:59], a[176:179], a[32:35]
buffer_load_dwordx4 v[40:43], v34, s[24:27], s28 offen offset:3072 sc1
v_mfma_f32_16x16x32_bf16 a[36:39], v[60:63], a[176:179], a[36:39]
buffer_load_dwordx4 v[44:47], v35, s[24:27], s28 offen offset:3072 sc1
v_mfma_f32_16x16x32_bf16 a[40:43], v[64:67], a[176:179], a[40:43]
s_add_u32 s28, s28, 4096
v_mfma_f32_16x16x32_bf16 a[44:47], v[68:71], a[176:179], a[44:47]
v_mfma_f32_16x16x32_bf16 a[48:51], v[56:59], a[180:183], a[48:51]
v_mfma_f32_16x16x32_bf16 a[52:55], v[60:63], a[180:183], a[52:55]
v_mfma_f32_16x16x32_bf16 a[56:59], v[64:67], a[180:183], a[56:59]
v_mfma_f32_16x16x32_bf16 a[60:63], v[68:71], a[180:183], a[60:63]
s_waitcnt vmcnt(20)
v_mfma_f32_16x16x32_bf16 a[0:3], v[72:75], a[184:187], a[0:3]
v_mfma_f32_16x16x32_bf16 a[4:7], v[76:79], a[184:187], a[4:7]
v_mfma_f32_16x16x32_bf16 a[8:11], v[80:83], a[184:187], a[8:11]
buffer_load_dwordx4 v[48:51], v32, s[24:27], s28 offen offset:0 sc1
v_mfma_f32_16x16x32_bf16 a[12:15], v[84:87], a[184:187], a[12:15]
buffer_load_dwordx4 v[52:55], v33, s[24:27], s28 offen offset:0 sc1
v_mfma_f32_16x16x32_bf16 a[16:19], v[72:75], a[188:191], a[16:19]
buffer_load_dwordx4 a[168:171], v34, s[24:27], s28 offen offset:0 sc1
v_mfma_f32_16x16x32_bf16 a[20:23], v[76:79], a[188:191], a[20:23]
buffer_load_dwordx4 a[172:175], v35, s[24:27], s28 offen offset:0 sc1
v_mfma_f32_16x16x32_bf16 a[24:27], v[80:83], a[188:191], a[24:27]
buffer_load_dwordx4 a[176:179], v32, s[24:27], s28 offen offset:1024 sc1
v_mfma_f32_16x16x32_bf16 a[28:31], v[84:87], a[188:191], a[28:31]
buffer_load_dwordx4 a[180:183], v33, s[24:27], s28 offen offset:1024 sc1
v_mfma_f32_16x16x32_bf16 a[32:35], v[72:75], a[192:195], a[32:35]
buffer_load_dwordx4 v[56:59], v34, s[24:27], s28 offen offset:1024 sc1
v_mfma_f32_16x16x32_bf16 a[36:39], v[76:79], a[192:195], a[36:39]
buffer_load_dwordx4 v[60:63], v35, s[24:27], s28 offen offset:1024 sc1
v_mfma_f32_16x16x32_bf16 a[40:43], v[80:83], a[192:195], a[40:43]
v_mfma_f32_16x16x32_bf16 a[44:47], v[84:87], a[192:195], a[44:47]
v_mfma_f32_16x16x32_bf16 a[48:51], v[72:75], a[196:199], a[48:51]
v_mfma_f32_16x16x32_bf16 a[52:55], v[76:79], a[196:199], a[52:55]
v_mfma_f32_16x16x32_bf16 a[56:59], v[80:83], a[196:199], a[56:59]
v_mfma_f32_16x16x32_bf16 a[60:63], v[84:87], a[196:199], a[60:63]
s_waitcnt vmcnt(24)
v_mfma_f32_16x16x32_bf16 a[0:3], v[88:91], a[200:203], a[0:3]
v_mfma_f32_16x16x32_bf16 a[4:7], v[92:95], a[200:203], a[4:7]
v_mfma_f32_16x16x32_bf16 a[8:11], v[96:99], a[200:203], a[8:11]
buffer_load_dwordx4 v[64:67], v32, s[24:27], s28 offen offset:2048 sc1
v_mfma_f32_16x16x32_bf16 a[12:15], v[100:103], a[200:203], a[12:15]
buffer_load_dwordx4 v[68:71], v33, s[24:27], s28 offen offset:2048 sc1
v_mfma_f32_16x16x32_bf16 a[16:19], v[88:91], a[204:207], a[16:19]
buffer_load_dwordx4 a[184:187], v34, s[24:27], s28 offen offset:2048 sc1
v_mfma_f32_16x16x32_bf16 a[20:23], v[92:95], a[204:207], a[20:23]
buffer_load_dwordx4 a[188:191], v35, s[24:27], s28 offen offset:2048 sc1
v_mfma_f32_16x16x32_bf16 a[24:27], v[96:99], a[204:207], a[24:27]
buffer_load_dwordx4 a[192:195], v32, s[24:27], s28 offen offset:3072 sc1
v_mfma_f32_16x16x32_bf16 a[28:31], v[100:103], a[204:207], a[28:31]
buffer_load_dwordx4 a[196:199], v33, s[24:27], s28 offen offset:3072 sc1
v_mfma_f32_16x16x32_bf16 a[32:35], v[88:91], a[208:211], a[32:35]
buffer_load_dwordx4 v[72:75], v34, s[24:27], s28 offen offset:3072 sc1
v_mfma_f32_16x16x32_bf16 a[36:39], v[92:95], a[208:211], a[36:39]
buffer_load_dwordx4 v[76:79], v35, s[24:27], s28 offen offset:3072 sc1
v_mfma_f32_16x16x32_bf16 a[40:43], v[96:99], a[208:211], a[40:43]
s_add_u32 s28, s28, 4096
v_mfma_f32_16x16x32_bf16 a[44:47], v[100:103], a[208:211], a[44:47]
v_mfma_f32_16x16x32_bf16 a[48:51], v[88:91], a[212:215], a[48:51]
v_mfma_f32_16x16x32_bf16 a[52:55], v[92:95], a[212:215], a[52:55]
v_mfma_f32_16x16x32_bf16 a[56:59], v[96:99], a[212:215], a[56:59]
v_mfma_f32_16x16x32_bf16 a[60:63], v[100:103], a[212:215], a[60:63]
s_waitcnt vmcnt(28)
v_mfma_f32_16x16x32_bf16 a[0:3], v[104:107], a[216:219], a[0:3]
v_mfma_f32_16x16x32_bf16 a[4:7], v[108:111], a[216:219], a[4:7]
v_mfma_f32_16x16x32_bf16 a[8:11], v[112:115], a[216:219], a[8:11]
buffer_load_dwordx4 v[80:83], v32, s[24:27], s28 offen offset:0 sc1
v_mfma_f32_16x16x32_bf16 a[12:15], v[116:119], a[216:219], a[12:15]
buffer_load_dwordx4 v[84:87], v33, s[24:27], s28 offen offset:0 sc1
v_mfma_f32_16x16x32_bf16 a[16:19], v[104:107], a[220:223], a[16:19]
buffer_load_dwordx4 a[200:203], v34, s[24:27], s28 offen offset:0 sc1
v_mfma_f32_16x16x32_bf16 a[20:23], v[108:111], a[220:223], a[20:23]
buffer_load_dwordx4 a[204:207], v35, s[24:27], s28 offen offset:0 sc1
v_mfma_f32_16x16x32_bf16 a[24:27], v[112:115], a[220:223], a[24:27]
buffer_load_dwordx4 a[208:211], v32, s[24:27], s28 offen offset:1024 sc1
v_mfma_f32_16x16x32_bf16 a[28:31], v[116:119], a[220:223], a[28:31]
buffer_load_dwordx4 a[212:215], v33, s[24:27], s28 offen offset:1024 sc1
v_mfma_f32_16x16x32_bf16 a[32:35], v[104:107], a[224:227], a[32:35]
buffer_load_dwordx4 v[88:91], v34, s[24:27], s28 offen offset:1024 sc1
v_mfma_f32_16x16x32_bf16 a[36:39], v[108:111], a[224:227], a[36:39]
buffer_load_dwordx4 v[92:95], v35, s[24:27], s28 offen offset:1024 sc1
v_mfma_f32_16x16x32_bf16 a[40:43], v[112:115], a[224:227], a[40:43]
v_mfma_f32_16x16x32_bf16 a[44:47], v[116:119], a[224:227], a[44:47]
v_mfma_f32_16x16x32_bf16 a[48:51], v[104:107], a[228:231], a[48:51]
v_mfma_f32_16x16x32_bf16 a[52:55], v[108:111], a[228:231], a[52:55]
v_mfma_f32_16x16x32_bf16 a[56:59], v[112:115], a[228:231], a[56:59]
v_mfma_f32_16x16x32_bf16 a[60:63], v[116:119], a[228:231], a[60:63]
s_waitcnt vmcnt(32)
v_mfma_f32_16x16x32_bf16 a[0:3], v[120:123], a[232:235], a[0:3]
v_mfma_f32_16x16x32_bf16 a[4:7], v[124:127], a[232:235], a[4:7]
v_mfma_f32_16x16x32_bf16 a[8:11], v[128:131], a[232:235], a[8:11]
buffer_load_dwordx4 v[96:99], v32, s[24:27], s28 offen offset:2048 sc1
v_mfma_f32_16x16x32_bf16 a[12:15], v[132:135], a[232:235], a[12:15]
buffer_load_dwordx4 v[100:103], v33, s[24:27], s28 offen offset:2048 sc1
v_mfma_f32_16x16x32_bf16 a[16:19], v[120:123], a[236:239], a[16:19]
buffer_load_dwordx4 a[216:219], v34, s[24:27], s28 offen offset:2048 sc1
v_mfma_f32_16x16x32_bf16 a[20:23], v[124:127], a[236:239], a[20:23]
buffer_load_dwordx4 a[220:223], v35, s[24:27], s28 offen offset:2048 sc1
v_mfma_f32_16x16x32_bf16 a[24:27], v[128:131], a[236:239], a[24:27]
buffer_load_dwordx4 a[224:227], v32, s[24:27], s28 offen offset:3072 sc1
v_mfma_f32_16x16x32_bf16 a[28:31], v[132:135], a[236:239], a[28:31]
buffer_load_dwordx4 a[228:231], v33, s[24:27], s28 offen offset:3072 sc1
v_mfma_f32_16x16x32_bf16 a[32:35], v[120:123], a[240:243], a[32:35]
buffer_load_dwordx4 v[104:107], v34, s[24:27], s28 offen offset:3072 sc1
v_mfma_f32_16x16x32_bf16 a[36:39], v[124:127], a[240:243], a[36:39]
buffer_load_dwordx4 v[108:111], v35, s[24:27], s28 offen offset:3072 sc1
v_mfma_f32_16x16x32_bf16 a[40:43], v[128:131], a[240:243], a[40:43]
s_add_u32 s28, s28, 4096
v_mfma_f32_16x16x32_bf16 a[44:47], v[132:135], a[240:243], a[44:47]
v_mfma_f32_16x16x32_bf16 a[48:51], v[120:123], a[244:247], a[48:51]
v_mfma_f32_16x16x32_bf16 a[52:55], v[124:127], a[244:247], a[52:55]
v_mfma_f32_16x16x32_bf16 a[56:59], v[128:131], a[244:247], a[56:59]
v_mfma_f32_16x16x32_bf16 a[60:63], v[132:135], a[244:247], a[60:63]
s_waitcnt vmcnt(36)
v_mfma_f32_16x16x32_bf16 a[0:3], v[136:139], a[248:251], a[0:3]
v_mfma_f32_16x16x32_bf16 a[4:7], v[140:143], a[248:251], a[4:7]
v_mfma_f32_16x16x32_bf16 a[8:11], v[144:147], a[248:251], a[8:11]
buffer_load_dwordx4 v[112:115], v32, s[24:27], s28 offen offset:0 sc1
v_mfma_f32_16x16x32_bf16 a[12:15], v[148:151], a[248:251], a[12:15]
buffer_load_dwordx4 v[116:119], v33, s[24:27], s28 offen offset:0 sc1
v_mfma_f32_16x16x32_bf16 a[16:19], v[136:139], a[252:255], a[16:19]
buffer_load_dwordx4 a[232:235], v34, s[24:27], s28 offen offset:0 sc1
v_mfma_f32_16x16x32_bf16 a[20:23], v[140:143], a[252:255], a[20:23]
buffer_load_dwordx4 a[236:239], v35, s[24:27], s28 offen offset:0 sc1
v_mfma_f32_16x16x32_bf16 a[24:27], v[144:147], a[252:255], a[24:27]
buffer_load_dwordx4 a[240:243], v32, s[24:27], s28 offen offset:1024 sc1
v_mfma_f32_16x16x32_bf16 a[28:31], v[148:151], a[252:255], a[28:31]
buffer_load_dwordx4 a[244:247], v33, s[24:27], s28 offen offset:1024 sc1
v_mfma_f32_16x16x32_bf16 a[32:35], v[136:139], a[152:155], a[32:35]
buffer_load_dwordx4 v[120:123], v34, s[24:27], s28 offen offset:1024 sc1
v_mfma_f32_16x16x32_bf16 a[36:39], v[140:143], a[152:155], a[36:39]
buffer_load_dwordx4 v[124:127], v35, s[24:27], s28 offen offset:1024 sc1
v_mfma_f32_16x16x32_bf16 a[40:43], v[144:147], a[152:155], a[40:43]
v_mfma_f32_16x16x32_bf16 a[44:47], v[148:151], a[152:155], a[44:47]
v_mfma_f32_16x16x32_bf16 a[48:51], v[136:139], a[156:159], a[48:51]
v_mfma_f32_16x16x32_bf16 a[52:55], v[140:143], a[156:159], a[52:55]
v_mfma_f32_16x16x32_bf16 a[56:59], v[144:147], a[156:159], a[56:59]
v_mfma_f32_16x16x32_bf16 a[60:63], v[148:151], a[156:159], a[60:63]
s_waitcnt vmcnt(40)
v_mfma_f32_16x16x32_bf16 a[0:3], v[152:155], a[160:163], a[0:3]
v_mfma_f32_16x16x32_bf16 a[4:7], v[156:159], a[160:163], a[4:7]
v_mfma_f32_16x16x32_bf16 a[8:11], v[160:163], a[160:163], a[8:11]
buffer_load_dwordx4 v[128:131], v32, s[24:27], s28 offen offset:2048 sc1
v_mfma_f32_16x16x32_bf16 a[12:15], v[164:167], a[160:163], a[12:15]
buffer_load_dwordx4 v[132:135], v33, s[24:27], s28 offen offset:2048 sc1
v_mfma_f32_16x16x32_bf16 a[16:19], v[152:155], a[164:167], a[16:19]
buffer_load_dwordx4 a[248:251], v34, s[24:27], s28 offen offset:2048 sc1
v_mfma_f32_16x16x32_bf16 a[20:23], v[156:159], a[164:167], a[20:23]
buffer_load_dwordx4 a[252:255], v35, s[24:27], s28 offen offset:2048 sc1
v_mfma_f32_16x16x32_bf16 a[24:27], v[160:163], a[164:167], a[24:27]
buffer_load_dwordx4 a[152:155], v32, s[24:27], s28 offen offset:3072 sc1
v_mfma_f32_16x16x32_bf16 a[28:31], v[164:167], a[164:167], a[28:31]
buffer_load_dwordx4 a[156:159], v33, s[24:27], s28 offen offset:3072 sc1
v_mfma_f32_16x16x32_bf16 a[32:35], v[152:155], v[40:43], a[32:35]
buffer_load_dwordx4 v[136:139], v34, s[24:27], s28 offen offset:3072 sc1
v_mfma_f32_16x16x32_bf16 a[36:39], v[156:159], v[40:43], a[36:39]
buffer_load_dwordx4 v[140:143], v35, s[24:27], s28 offen offset:3072 sc1
v_mfma_f32_16x16x32_bf16 a[40:43], v[160:163], v[40:43], a[40:43]
s_add_u32 s28, s28, 4096
v_mfma_f32_16x16x32_bf16 a[44:47], v[164:167], v[40:43], a[44:47]
v_mfma_f32_16x16x32_bf16 a[48:51], v[152:155], v[44:47], a[48:51]
v_mfma_f32_16x16x32_bf16 a[52:55], v[156:159], v[44:47], a[52:55]
v_mfma_f32_16x16x32_bf16 a[56:59], v[160:163], v[44:47], a[56:59]
v_mfma_f32_16x16x32_bf16 a[60:63], v[164:167], v[44:47], a[60:63]
s_waitcnt vmcnt(44)
v_mfma_f32_16x16x32_bf16 a[0:3], v[168:171], v[48:51], a[0:3]
v_mfma_f32_16x16x32_bf16 a[4:7], v[172:175], v[48:51], a[4:7]
v_mfma_f32_16x16x32_bf16 a[8:11], v[176:179], v[48:51], a[8:11]
buffer_load_dwordx4 v[144:147], v32, s[24:27], s29 offen offset:0 sc1
v_mfma_f32_16x16x32_bf16 a[12:15], v[180:183], v[48:51], a[12:15]
buffer_load_dwordx4 v[148:151], v32, s[24:27], s29 offen offset:1024 sc1
v_mfma_f32_16x16x32_bf16 a[16:19], v[168:171], v[52:55], a[16:19]
buffer_load_dwordx4 a[160:163], v32, s[24:27], s29 offen offset:2048 sc1
v_mfma_f32_16x16x32_bf16 a[20:23], v[172:175], v[52:55], a[20:23]
buffer_load_dwordx4 a[164:167], v32, s[24:27], s29 offen offset:3072 sc1
v_mfma_f32_16x16x32_bf16 a[24:27], v[176:179], v[52:55], a[24:27]
s_add_u32 s29, s29, 4096
v_mfma_f32_16x16x32_bf16 a[28:31], v[180:183], v[52:55], a[28:31]
buffer_load_dwordx4 v[40:43], v32, s[24:27], s28 offen offset:0 sc1
v_mfma_f32_16x16x32_bf16 a[32:35], v[168:171], a[168:171], a[32:35]
buffer_load_dwordx4 v[44:47], v33, s[24:27], s28 offen offset:0 sc1
v_mfma_f32_16x16x32_bf16 a[36:39], v[172:175], a[168:171], a[36:39]
buffer_load_dwordx4 v[152:155], v34, s[24:27], s28 offen offset:0 sc1
v_mfma_f32_16x16x32_bf16 a[40:43], v[176:179], a[168:171], a[40:43]
buffer_load_dwordx4 v[156:159], v35, s[24:27], s28 offen offset:0 sc1
v_mfma_f32_16x16x32_bf16 a[44:47], v[180:183], a[168:171], a[44:47]
v_mfma_f32_16x16x32_bf16 a[48:51], v[168:171], a[172:175], a[48:51]
v_mfma_f32_16x16x32_bf16 a[52:55], v[172:175], a[172:175], a[52:55]
v_mfma_f32_16x16x32_bf16 a[56:59], v[176:179], a[172:175], a[56:59]
v_mfma_f32_16x16x32_bf16 a[60:63], v[180:183], a[172:175], a[60:63]
s_waitcnt vmcnt(48)
v_mfma_f32_16x16x32_bf16 a[0:3], v[184:187], a[176:179], a[0:3]
v_mfma_f32_16x16x32_bf16 a[4:7], v[188:191], a[176:179], a[4:7]
v_mfma_f32_16x16x32_bf16 a[8:11], v[192:195], a[176:179], a[8:11]
buffer_load_dwordx4 v[160:163], v32, s[24:27], s29 offen offset:0 sc1
v_mfma_f32_16x16x32_bf16 a[12:15], v[196:199], a[176:179], a[12:15]
buffer_load_dwordx4 v[164:167], v32, s[24:27], s29 offen offset:1024 sc1
v_mfma_f32_16x16x32_bf16 a[16:19], v[184:187], a[180:183], a[16:19]
buffer_load_dwordx4 v[48:51], v32, s[24:27], s29 offen offset:2048 sc1
v_mfma_f32_16x16x32_bf16 a[20:23], v[188:191], a[180:183], a[20:23]
buffer_load_dwordx4 v[52:55], v32, s[24:27], s29 offen offset:3072 sc1
v_mfma_f32_16x16x32_bf16 a[24:27], v[192:195], a[180:183], a[24:27]
s_add_u32 s29, s29, 4096
v_mfma_f32_16x16x32_bf16 a[28:31], v[196:199], a[180:183], a[28:31]
buffer_load_dwordx4 a[168:171], v32, s[24:27], s28 offen offset:1024 sc1
v_mfma_f32_16x16x32_bf16 a[32:35], v[184:187], v[56:59], a[32:35]
buffer_load_dwordx4 a[172:175], v33, s[24:27], s28 offen offset:1024 sc1
v_mfma_f32_16x16x32_bf16 a[36:39], v[188:191], v[56:59], a[36:39]
buffer_load_dwordx4 v[168:171], v34, s[24:27], s28 offen offset:1024 sc1
v_mfma_f32_16x16x32_bf16 a[40:43], v[192:195], v[56:59], a[40:43]
buffer_load_dwordx4 v[172:175], v35, s[24:27], s28 offen offset:1024 sc1
v_mfma_f32_16x16x32_bf16 a[44:47], v[196:199], v[56:59], a[44:47]
v_mfma_f32_16x16x32_bf16 a[48:51], v[184:187], v[60:63], a[48:51]
v_mfma_f32_16x16x32_bf16 a[52:55], v[188:191], v[60:63], a[52:55]
v_mfma_f32_16x16x32_bf16 a[56:59], v[192:195], v[60:63], a[56:59]
v_mfma_f32_16x16x32_bf16 a[60:63], v[196:199], v[60:63], a[60:63]
s_waitcnt vmcnt(52)
v_mfma_f32_16x16x32_bf16 a[0:3], v[200:203], v[64:67], a[0:3]
v_mfma_f32_16x16x32_bf16 a[4:7], v[204:207], v[64:67], a[4:7]
v_mfma_f32_16x16x32_bf16 a[8:11], v[208:211], v[64:67], a[8:11]
buffer_load_dwordx4 v[176:179], v32, s[24:27], s29 offen offset:0 sc1
v_mfma_f32_16x16x32_bf16 a[12:15], v[212:215], v[64:67], a[12:15]
buffer_load_dwordx4 v[180:183], v32, s[24:27], s29 offen offset:1024 sc1
v_mfma_f32_16x16x32_bf16 a[16:19], v[200:203], v[68:71], a[16:19]
buffer_load_dwordx4 a[176:179], v32, s[24:27], s29 offen offset:2048 sc1
v_mfma_f32_16x16x32_bf16 a[20:23], v[204:207], v[68:71], a[20:23]
buffer_load_dwordx4 a[180:183], v32, s[24:27], s29 offen offset:3072 sc1
v_mfma_f32_16x16x32_bf16 a[24:27], v[208:211], v[68:71], a[24:27]
s_add_u32 s29, s29, 4096
v_mfma_f32_16x16x32_bf16 a[28:31], v[212:215], v[68:71], a[28:31]
buffer_load_dwordx4 v[56:59], v32, s[24:27], s28 offen offset:2048 sc1
v_mfma_f32_16x16x32_bf16 a[32:35], v[200:203], a[184:187], a[32:35]
buffer_load_dwordx4 v[60:63], v33, s[24:27], s28 offen offset:2048 sc1
v_mfma_f32_16x16x32_bf16 a[36:39], v[204:207], a[184:187], a[36:39]
buffer_load_dwordx4 v[184:187], v34, s[24:27], s28 offen offset:2048 sc1
v_mfma_f32_16x16x32_bf16 a[40:43], v[208:211], a[184:187], a[40:43]
buffer_load_dwordx4 v[188:191], v35, s[24:27], s28 offen offset:2048 sc1
v_mfma_f32_16x16x32_bf16 a[44:47], v[212:215], a[184:187], a[44:47]
v_mfma_f32_16x16x32_bf16 a[48:51], v[200:203], a[188:191], a[48:51]
v_mfma_f32_16x16x32_bf16 a[52:55], v[204:207], a[188:191], a[52:55]
v_mfma_f32_16x16x32_bf16 a[56:59], v[208:211], a[188:191], a[56:59]
v_mfma_f32_16x16x32_bf16 a[60:63], v[212:215], a[188:191], a[60:63]
s_waitcnt vmcnt(56)
v_mfma_f32_16x16x32_bf16 a[0:3], v[216:219], a[192:195], a[0:3]
v_mfma_f32_16x16x32_bf16 a[4:7], v[220:223], a[192:195], a[4:7]
v_mfma_f32_16x16x32_bf16 a[8:11], v[224:227], a[192:195], a[8:11]
buffer_load_dwordx4 v[192:195], v32, s[24:27], s29 offen offset:0 sc1
v_mfma_f32_16x16x32_bf16 a[12:15], v[228:231], a[192:195], a[12:15]
buffer_load_dwordx4 v[196:199], v32, s[24:27], s29 offen offset:1024 sc1
v_mfma_f32_16x16x32_bf16 a[16:19], v[216:219], a[196:199], a[16:19]
buffer_load_dwordx4 v[64:67], v32, s[24:27], s29 offen offset:2048 sc1
v_mfma_f32_16x16x32_bf16 a[20:23], v[220:223], a[196:199], a[20:23]
buffer_load_dwordx4 v[68:71], v32, s[24:27], s29 offen offset:3072 sc1
v_mfma_f32_16x16x32_bf16 a[24:27], v[224:227], a[196:199], a[24:27]
s_add_u32 s29, s29, 4096
v_mfma_f32_16x16x32_bf16 a[28:31], v[228:231], a[196:199], a[28:31]
buffer_load_dwordx4 a[184:187], v32, s[24:27], s28 offen offset:3072 sc1
v_mfma_f32_16x16x32_bf16 a[32:35], v[216:219], v[72:75], a[32:35]
buffer_load_dwordx4 a[188:191], v33, s[24:27], s28 offen offset:3072 sc1
v_mfma_f32_16x16x32_bf16 a[36:39], v[220:223], v[72:75], a[36:39]
buffer_load_dwordx4 v[200:203], v34, s[24:27], s28 offen offset:3072 sc1
v_mfma_f32_16x16x32_bf16 a[40:43], v[224:227], v[72:75], a[40:43]
buffer_load_dwordx4 v[204:207], v35, s[24:27], s28 offen offset:3072 sc1
v_mfma_f32_16x16x32_bf16 a[44:47], v[228:231], v[72:75], a[44:47]
s_add_u32 s28, s28, 4096
v_mfma_f32_16x16x32_bf16 a[48:51], v[216:219], v[76:79], a[48:51]
v_mfma_f32_16x16x32_bf16 a[52:55], v[220:223], v[76:79], a[52:55]
v_mfma_f32_16x16x32_bf16 a[56:59], v[224:227], v[76:79], a[56:59]
v_mfma_f32_16x16x32_bf16 a[60:63], v[228:231], v[76:79], a[60:63]
s_waitcnt vmcnt(60)
v_mfma_f32_16x16x32_bf16 a[0:3], v[232:235], v[80:83], a[0:3]
v_mfma_f32_16x16x32_bf16 a[4:7], v[236:239], v[80:83], a[4:7]
v_mfma_f32_16x16x32_bf16 a[8:11], v[240:243], v[80:83], a[8:11]
buffer_load_dwordx4 v[208:211], v32, s[24:27], s29 offen offset:0 sc1
v_mfma_f32_16x16x32_bf16 a[12:15], v[244:247], v[80:83], a[12:15]
buffer_load_dwordx4 v[212:215], v32, s[24:27], s29 offen offset:1024 sc1
v_mfma_f32_16x16x32_bf16 a[16:19], v[232:235], v[84:87], a[16:19]
buffer_load_dwordx4 a[192:195], v32, s[24:27], s29 offen offset:2048 sc1
v_mfma_f32_16x16x32_bf16 a[20:23], v[236:239], v[84:87], a[20:23]
buffer_load_dwordx4 a[196:199], v32, s[24:27], s29 offen offset:3072 sc1
v_mfma_f32_16x16x32_bf16 a[24:27], v[240:243], v[84:87], a[24:27]
s_add_u32 s29, s29, 4096
v_mfma_f32_16x16x32_bf16 a[28:31], v[244:247], v[84:87], a[28:31]
buffer_load_dwordx4 v[72:75], v32, s[24:27], s28 offen offset:0 sc1
v_mfma_f32_16x16x32_bf16 a[32:35], v[232:235], a[200:203], a[32:35]
buffer_load_dwordx4 v[76:79], v33, s[24:27], s28 offen offset:0 sc1
v_mfma_f32_16x16x32_bf16 a[36:39], v[236:239], a[200:203], a[36:39]
buffer_load_dwordx4 v[216:219], v34, s[24:27], s28 offen offset:0 sc1
v_mfma_f32_16x16x32_bf16 a[40:43], v[240:243], a[200:203], a[40:43]
buffer_load_dwordx4 v[220:223], v35, s[24:27], s28 offen offset:0 sc1
v_mfma_f32_16x16x32_bf16 a[44:47], v[244:247], a[200:203], a[44:47]
v_mfma_f32_16x16x32_bf16 a[48:51], v[232:235], a[204:207], a[48:51]
v_mfma_f32_16x16x32_bf16 a[52:55], v[236:239], a[204:207], a[52:55]
v_mfma_f32_16x16x32_bf16 a[56:59], v[240:243], a[204:207], a[56:59]
v_mfma_f32_16x16x32_bf16 a[60:63], v[244:247], a[204:207], a[60:63]
s_waitcnt vmcnt(63)
v_mfma_f32_16x16x32_bf16 a[0:3], v[248:251], a[208:211], a[0:3]
v_mfma_f32_16x16x32_bf16 a[4:7], v[252:255], a[208:211], a[4:7]
v_mfma_f32_16x16x32_bf16 a[8:11], a[64:67], a[208:211], a[8:11]
buffer_load_dwordx4 v[224:227], v32, s[24:27], s29 offen offset:0 sc1
v_mfma_f32_16x16x32_bf16 a[12:15], a[68:71], a[208:211], a[12:15]
buffer_load_dwordx4 v[228:231], v32, s[24:27], s29 offen offset:1024 sc1
v_mfma_f32_16x16x32_bf16 a[16:19], v[248:251], a[212:215], a[16:19]
buffer_load_dwordx4 v[80:83], v32, s[24:27], s29 offen offset:2048 sc1
v_mfma_f32_16x16x32_bf16 a[20:23], v[252:255], a[212:215], a[20:23]
buffer_load_dwordx4 v[84:87], v32, s[24:27], s29 offen offset:3072 sc1
v_mfma_f32_16x16x32_bf16 a[24:27], a[64:67], a[212:215], a[24:27]
s_add_u32 s29, s29, 4096
v_mfma_f32_16x16x32_bf16 a[28:31], a[68:71], a[212:215], a[28:31]
buffer_load_dwordx4 a[200:203], v32, s[24:27], s28 offen offset:1024 sc1
v_mfma_f32_16x16x32_bf16 a[32:35], v[248:251], v[88:91], a[32:35]
buffer_load_dwordx4 a[204:207], v33, s[24:27], s28 offen offset:1024 sc1
v_mfma_f32_16x16x32_bf16 a[36:39], v[252:255], v[88:91], a[36:39]
buffer_load_dwordx4 v[232:235], v34, s[24:27], s28 offen offset:1024 sc1
v_mfma_f32_16x16x32_bf16 a[40:43], a[64:67], v[88:91], a[40:43]
buffer_load_dwordx4 v[236:239], v35, s[24:27], s28 offen offset:1024 sc1
v_mfma_f32_16x16x32_bf16 a[44:47], a[68:71], v[88:91], a[44:47]
v_mfma_f32_16x16x32_bf16 a[48:51], v[248:251], v[92:95], a[48:51]
v_mfma_f32_16x16x32_bf16 a[52:55], v[252:255], v[92:95], a[52:55]
v_mfma_f32_16x16x32_bf16 a[56:59], a[64:67], v[92:95], a[56:59]
v_mfma_f32_16x16x32_bf16 a[60:63], a[68:71], v[92:95], a[60:63]
s_waitcnt vmcnt(63)
v_mfma_f32_16x16x32_bf16 a[0:3], a[72:75], v[96:99], a[0:3]
v_mfma_f32_16x16x32_bf16 a[4:7], a[76:79], v[96:99], a[4:7]
v_mfma_f32_16x16x32_bf16 a[8:11], a[80:83], v[96:99], a[8:11]
buffer_load_dwordx4 v[240:243], v32, s[24:27], s29 offen offset:0 sc1
v_mfma_f32_16x16x32_bf16 a[12:15], a[84:87], v[96:99], a[12:15]
buffer_load_dwordx4 v[244:247], v32, s[24:27], s29 offen offset:1024 sc1
v_mfma_f32_16x16x32_bf16 a[16:19], a[72:75], v[100:103], a[16:19]
buffer_load_dwordx4 a[208:211], v32, s[24:27], s29 offen offset:2048 sc1
v_mfma_f32_16x16x32_bf16 a[20:23], a[76:79], v[100:103], a[20:23]
buffer_load_dwordx4 a[212:215], v32, s[24:27], s29 offen offset:3072 sc1
v_mfma_f32_16x16x32_bf16 a[24:27], a[80:83], v[100:103], a[24:27]
s_add_u32 s29, s29, 4096
v_mfma_f32_16x16x32_bf16 a[28:31], a[84:87], v[100:103], a[28:31]
buffer_load_dwordx4 v[88:91], v32, s[24:27], s28 offen offset:2048 sc1
v_mfma_f32_16x16x32_bf16 a[32:35], a[72:75], a[216:219], a[32:35]
buffer_load_dwordx4 v[92:95], v33, s[24:27], s28 offen offset:2048 sc1
v_mfma_f32_16x16x32_bf16 a[36:39], a[76:79], a[216:219], a[36:39]
buffer_load_dwordx4 v[248:251], v34, s[24:27], s28 offen offset:2048 sc1
v_mfma_f32_16x16x32_bf16 a[40:43], a[80:83], a[216:219], a[40:43]
buffer_load_dwordx4 v[252:255], v35, s[24:27], s28 offen offset:2048 sc1
v_mfma_f32_16x16x32_bf16 a[44:47], a[84:87], a[216:219], a[44:47]
v_mfma_f32_16x16x32_bf16 a[48:51], a[72:75], a[220:223], a[48:51]
v_mfma_f32_16x16x32_bf16 a[52:55], a[76:79], a[220:223], a[52:55]
v_mfma_f32_16x16x32_bf16 a[56:59], a[80:83], a[220:223], a[56:59]
v_mfma_f32_16x16x32_bf16 a[60:63], a[84:87], a[220:223], a[60:63]
s_waitcnt vmcnt(63)
v_mfma_f32_16x16x32_bf16 a[0:3], a[88:91], a[224:227], a[0:3]
v_mfma_f32_16x16x32_bf16 a[4:7], a[92:95], a[224:227], a[4:7]
v_mfma_f32_16x16x32_bf16 a[8:11], a[96:99], a[224:227], a[8:11]
buffer_load_dwordx4 a[64:67], v32, s[24:27], s29 offen offset:0 sc1
v_mfma_f32_16x16x32_bf16 a[12:15], a[100:103], a[224:227], a[12:15]
buffer_load_dwordx4 a[68:71], v32, s[24:27], s29 offen offset:1024 sc1
v_mfma_f32_16x16x32_bf16 a[16:19], a[88:91], a[228:231], a[16:19]
buffer_load_dwordx4 v[96:99], v32, s[24:27], s29 offen offset:2048 sc1
v_mfma_f32_16x16x32_bf16 a[20:23], a[92:95], a[228:231], a[20:23]
buffer_load_dwordx4 v[100:103], v32, s[24:27], s29 offen offset:3072 sc1
v_mfma_f32_16x16x32_bf16 a[24:27], a[96:99], a[228:231], a[24:27]
s_add_u32 s29, s29, 4096
v_mfma_f32_16x16x32_bf16 a[28:31], a[100:103], a[228:231], a[28:31]
buffer_load_dwordx4 a[216:219], v32, s[24:27], s28 offen offset:3072 sc1
v_mfma_f32_16x16x32_bf16 a[32:35], a[88:91], v[104:107], a[32:35]
buffer_load_dwordx4 a[220:223], v33, s[24:27], s28 offen offset:3072 sc1
v_mfma_f32_16x16x32_bf16 a[36:39], a[92:95], v[104:107], a[36:39]
buffer_load_dwordx4 a[72:75], v34, s[24:27], s28 offen offset:3072 sc1
v_mfma_f32_16x16x32_bf16 a[40:43], a[96:99], v[104:107], a[40:43]
buffer_load_dwordx4 a[76:79], v35, s[24:27], s28 offen offset:3072 sc1
v_mfma_f32_16x16x32_bf16 a[44:47], a[100:103], v[104:107], a[44:47]
s_add_u32 s28, s28, 4096
v_mfma_f32_16x16x32_bf16 a[48:51], a[88:91], v[108:111], a[48:51]
v_mfma_f32_16x16x32_bf16 a[52:55], a[92:95], v[108:111], a[52:55]
v_mfma_f32_16x16x32_bf16 a[56:59], a[96:99], v[108:111], a[56:59]
v_mfma_f32_16x16x32_bf16 a[60:63], a[100:103], v[108:111], a[60:63]
s_waitcnt vmcnt(63)
v_mfma_f32_16x16x32_bf16 a[0:3], a[104:107], v[112:115], a[0:3]
v_mfma_f32_16x16x32_bf16 a[4:7], a[108:111], v[112:115], a[4:7]
v_mfma_f32_16x16x32_bf16 a[8:11], a[112:115], v[112:115], a[8:11]
buffer_load_dwordx4 a[80:83], v32, s[24:27], s29 offen offset:0 sc1
v_mfma_f32_16x16x32_bf16 a[12:15], a[116:119], v[112:115], a[12:15]
buffer_load_dwordx4 a[84:87], v32, s[24:27], s29 offen offset:1024 sc1
v_mfma_f32_16x16x32_bf16 a[16:19], a[104:107], v[116:119], a[16:19]
buffer_load_dwordx4 a[224:227], v32, s[24:27], s29 offen offset:2048 sc1
v_mfma_f32_16x16x32_bf16 a[20:23], a[108:111], v[116:119], a[20:23]
buffer_load_dwordx4 a[228:231], v32, s[24:27], s29 offen offset:3072 sc1
v_mfma_f32_16x16x32_bf16 a[24:27], a[112:115], v[116:119], a[24:27]
s_add_u32 s29, s29, 4096
v_mfma_f32_16x16x32_bf16 a[28:31], a[116:119], v[116:119], a[28:31]
buffer_load_dwordx4 v[104:107], v32, s[24:27], s28 offen offset:0 sc1
v_mfma_f32_16x16x32_bf16 a[32:35], a[104:107], a[232:235], a[32:35]
buffer_load_dwordx4 v[108:111], v33, s[24:27], s28 offen offset:0 sc1
v_mfma_f32_16x16x32_bf16 a[36:39], a[108:111], a[232:235], a[36:39]
buffer_load_dwordx4 a[88:91], v34, s[24:27], s28 offen offset:0 sc1
v_mfma_f32_16x16x32_bf16 a[40:43], a[112:115], a[232:235], a[40:43]
buffer_load_dwordx4 a[92:95], v35, s[24:27], s28 offen offset:0 sc1
v_mfma_f32_16x16x32_bf16 a[44:47], a[116:119], a[232:235], a[44:47]
v_mfma_f32_16x16x32_bf16 a[48:51], a[104:107], a[236:239], a[48:51]
v_mfma_f32_16x16x32_bf16 a[52:55], a[108:111], a[236:239], a[52:55]
v_mfma_f32_16x16x32_bf16 a[56:59], a[112:115], a[236:239], a[56:59]
v_mfma_f32_16x16x32_bf16 a[60:63], a[116:119], a[236:239], a[60:63]
s_waitcnt vmcnt(63)
v_mfma_f32_16x16x32_bf16 a[0:3], a[120:123], a[240:243], a[0:3]
v_mfma_f32_16x16x32_bf16 a[4:7], a[124:127], a[240:243], a[4:7]
v_mfma_f32_16x16x32_bf16 a[8:11], a[128:131], a[240:243], a[8:11]
buffer_load_dwordx4 a[96:99], v32, s[24:27], s29 offen offset:0 sc1
v_mfma_f32_16x16x32_bf16 a[12:15], a[132:135], a[240:243], a[12:15]
buffer_load_dwordx4 a[100:103], v32, s[24:27], s29 offen offset:1024 sc1
v_mfma_f32_16x16x32_bf16 a[16:19], a[120:123], a[244:247], a[16:19]
buffer_load_dwordx4 v[112:115], v32, s[24:27], s29 offen offset:2048 sc1
v_mfma_f32_16x16x32_bf16 a[20:23], a[124:127], a[244:247], a[20:23]
buffer_load_dwordx4 v[116:119], v32, s[24:27], s29 offen offset:3072 sc1
v_mfma_f32_16x16x32_bf16 a[24:27], a[128:131], a[244:247], a[24:27]
s_add_u32 s29, s29, 4096
v_mfma_f32_16x16x32_bf16 a[28:31], a[132:135], a[244:247], a[28:31]
buffer_load_dwordx4 a[232:235], v32, s[24:27], s28 offen offset:1024 sc1
v_mfma_f32_16x16x32_bf16 a[32:35], a[120:123], v[120:123], a[32:35]
buffer_load_dwordx4 a[236:239], v33, s[24:27], s28 offen offset:1024 sc1
v_mfma_f32_16x16x32_bf16 a[36:39], a[124:127], v[120:123], a[36:39]
buffer_load_dwordx4 a[104:107], v34, s[24:27], s28 offen offset:1024 sc1
v_mfma_f32_16x16x32_bf16 a[40:43], a[128:131], v[120:123], a[40:43]
buffer_load_dwordx4 a[108:111], v35, s[24:27], s28 offen offset:1024 sc1
v_mfma_f32_16x16x32_bf16 a[44:47], a[132:135], v[120:123], a[44:47]
v_mfma_f32_16x16x32_bf16 a[48:51], a[120:123], v[124:127], a[48:51]
v_mfma_f32_16x16x32_bf16 a[52:55], a[124:127], v[124:127], a[52:55]
v_mfma_f32_16x16x32_bf16 a[56:59], a[128:131], v[124:127], a[56:59]
v_mfma_f32_16x16x32_bf16 a[60:63], a[132:135], v[124:127], a[60:63]
s_waitcnt vmcnt(63)
v_mfma_f32_16x16x32_bf16 a[0:3], a[136:139], v[128:131], a[0:3]
v_mfma_f32_16x16x32_bf16 a[4:7], a[140:143], v[128:131], a[4:7]
v_mfma_f32_16x16x32_bf16 a[8:11], a[144:147], v[128:131], a[8:11]
buffer_load_dwordx4 a[112:115], v32, s[24:27], s29 offen offset:0 sc1
v_mfma_f32_16x16x32_bf16 a[12:15], a[148:151], v[128:131], a[12:15]
buffer_load_dwordx4 a[116:119], v32, s[24:27], s29 offen offset:1024 sc1
v_mfma_f32_16x16x32_bf16 a[16:19], a[136:139], v[132:135], a[16:19]
buffer_load_dwordx4 a[240:243], v32, s[24:27], s29 offen offset:2048 sc1
v_mfma_f32_16x16x32_bf16 a[20:23], a[140:143], v[132:135], a[20:23]
buffer_load_dwordx4 a[244:247], v32, s[24:27], s29 offen offset:3072 sc1
v_mfma_f32_16x16x32_bf16 a[24:27], a[144:147], v[132:135], a[24:27]
s_add_u32 s29, s29, 4096
v_mfma_f32_16x16x32_bf16 a[28:31], a[148:151], v[132:135], a[28:31]
buffer_load_dwordx4 v[120:123], v32, s[24:27], s28 offen offset:2048 sc1
v_mfma_f32_16x16x32_bf16 a[32:35], a[136:139], a[248:251], a[32:35]
buffer_load_dwordx4 v[124:127], v33, s[24:27], s28 offen offset:2048 sc1
v_mfma_f32_16x16x32_bf16 a[36:39], a[140:143], a[248:251], a[36:39]
buffer_load_dwordx4 a[120:123], v34, s[24:27], s28 offen offset:2048 sc1
v_mfma_f32_16x16x32_bf16 a[40:43], a[144:147], a[248:251], a[40:43]
buffer_load_dwordx4 a[124:127], v35, s[24:27], s28 offen offset:2048 sc1
v_mfma_f32_16x16x32_bf16 a[44:47], a[148:151], a[248:251], a[44:47]
v_mfma_f32_16x16x32_bf16 a[48:51], a[136:139], a[252:255], a[48:51]
v_mfma_f32_16x16x32_bf16 a[52:55], a[140:143], a[252:255], a[52:55]
v_mfma_f32_16x16x32_bf16 a[56:59], a[144:147], a[252:255], a[56:59]
v_mfma_f32_16x16x32_bf16 a[60:63], a[148:151], a[252:255], a[60:63]
s_waitcnt vmcnt(63)
v_mfma_f32_16x16x32_bf16 a[0:3], v[144:147], a[152:155], a[0:3]
v_mfma_f32_16x16x32_bf16 a[4:7], v[148:151], a[152:155], a[4:7]
v_mfma_f32_16x16x32_bf16 a[8:11], a[160:163], a[152:155], a[8:11]
buffer_load_dwordx4 a[128:131], v32, s[24:27], s29 offen offset:0 sc1
v_mfma_f32_16x16x32_bf16 a[12:15], a[164:167], a[152:155], a[12:15]
buffer_load_dwordx4 a[132:135], v32, s[24:27], s29 offen offset:1024 sc1
v_mfma_f32_16x16x32_bf16 a[16:19], v[144:147], a[156:159], a[16:19]
buffer_load_dwordx4 v[128:131], v32, s[24:27], s29 offen offset:2048 sc1
v_mfma_f32_16x16x32_bf16 a[20:23], v[148:151], a[156:159], a[20:23]
buffer_load_dwordx4 v[132:135], v32, s[24:27], s29 offen offset:3072 sc1
v_mfma_f32_16x16x32_bf16 a[24:27], a[160:163], a[156:159], a[24:27]
s_add_u32 s29, s29, 4096
v_mfma_f32_16x16x32_bf16 a[28:31], a[164:167], a[156:159], a[28:31]
buffer_load_dwordx4 a[248:251], v32, s[24:27], s28 offen offset:3072 sc1
v_mfma_f32_16x16x32_bf16 a[32:35], v[144:147], v[136:139], a[32:35]
buffer_load_dwordx4 a[252:255], v33, s[24:27], s28 offen offset:3072 sc1
v_mfma_f32_16x16x32_bf16 a[36:39], v[148:151], v[136:139], a[36:39]
buffer_load_dwordx4 a[136:139], v34, s[24:27], s28 offen offset:3072 sc1
v_mfma_f32_16x16x32_bf16 a[40:43], a[160:163], v[136:139], a[40:43]
buffer_load_dwordx4 a[140:143], v35, s[24:27], s28 offen offset:3072 sc1
v_mfma_f32_16x16x32_bf16 a[44:47], a[164:167], v[136:139], a[44:47]
s_add_u32 s28, s28, 4096
v_mfma_f32_16x16x32_bf16 a[48:51], v[144:147], v[140:143], a[48:51]
v_mfma_f32_16x16x32_bf16 a[52:55], v[148:151], v[140:143], a[52:55]
v_mfma_f32_16x16x32_bf16 a[56:59], a[160:163], v[140:143], a[56:59]
v_mfma_f32_16x16x32_bf16 a[60:63], a[164:167], v[140:143], a[60:63]
s_waitcnt vmcnt(63)
v_mfma_f32_16x16x32_bf16 a[0:3], v[160:163], v[40:43], a[0:3]
v_mfma_f32_16x16x32_bf16 a[4:7], v[164:167], v[40:43], a[4:7]
v_mfma_f32_16x16x32_bf16 a[8:11], v[48:51], v[40:43], a[8:11]
buffer_load_dwordx4 a[144:147], v32, s[24:27], s29 offen offset:0 sc1
v_mfma_f32_16x16x32_bf16 a[12:15], v[52:55], v[40:43], a[12:15]
buffer_load_dwordx4 a[148:151], v32, s[24:27], s29 offen offset:1024 sc1
v_mfma_f32_16x16x32_bf16 a[16:19], v[160:163], v[44:47], a[16:19]
buffer_load_dwordx4 a[152:155], v32, s[24:27], s29 offen offset:2048 sc1
v_mfma_f32_16x16x32_bf16 a[20:23], v[164:167], v[44:47], a[20:23]
buffer_load_dwordx4 a[156:159], v32, s[24:27], s29 offen offset:3072 sc1
v_mfma_f32_16x16x32_bf16 a[24:27], v[48:51], v[44:47], a[24:27]
s_add_u32 s29, s29, 4096
v_mfma_f32_16x16x32_bf16 a[28:31], v[52:55], v[44:47], a[28:31]
v_mfma_f32_16x16x32_bf16 a[32:35], v[160:163], v[152:155], a[32:35]
v_mfma_f32_16x16x32_bf16 a[36:39], v[164:167], v[152:155], a[36:39]
v_mfma_f32_16x16x32_bf16 a[40:43], v[48:51], v[152:155], a[40:43]
v_mfma_f32_16x16x32_bf16 a[44:47], v[52:55], v[152:155], a[44:47]
v_mfma_f32_16x16x32_bf16 a[48:51], v[160:163], v[156:159], a[48:51]
v_mfma_f32_16x16x32_bf16 a[52:55], v[164:167], v[156:159], a[52:55]
v_mfma_f32_16x16x32_bf16 a[56:59], v[48:51], v[156:159], a[56:59]
v_mfma_f32_16x16x32_bf16 a[60:63], v[52:55], v[156:159], a[60:63]
s_waitcnt vmcnt(63)
v_mfma_f32_16x16x32_bf16 a[0:3], v[176:179], a[168:171], a[0:3]
v_mfma_f32_16x16x32_bf16 a[4:7], v[180:183], a[168:171], a[4:7]
v_mfma_f32_16x16x32_bf16 a[8:11], a[176:179], a[168:171], a[8:11]
v_mfma_f32_16x16x32_bf16 a[12:15], a[180:183], a[168:171], a[12:15]
v_mfma_f32_16x16x32_bf16 a[16:19], v[176:179], a[172:175], a[16:19]
v_mfma_f32_16x16x32_bf16 a[20:23], v[180:183], a[172:175], a[20:23]
v_mfma_f32_16x16x32_bf16 a[24:27], a[176:179], a[172:175], a[24:27]
v_mfma_f32_16x16x32_bf16 a[28:31], a[180:183], a[172:175], a[28:31]
v_mfma_f32_16x16x32_bf16 a[32:35], v[176:179], v[168:171], a[32:35]
v_mfma_f32_16x16x32_bf16 a[36:39], v[180:183], v[168:171], a[36:39]
v_mfma_f32_16x16x32_bf16 a[40:43], a[176:179], v[168:171], a[40:43]
v_mfma_f32_16x16x32_bf16 a[44:47], a[180:183], v[168:171], a[44:47]
v_mfma_f32_16x16x32_bf16 a[48:51], v[176:179], v[172:175], a[48:51]
v_mfma_f32_16x16x32_bf16 a[52:55], v[180:183], v[172:175], a[52:55]
v_mfma_f32_16x16x32_bf16 a[56:59], a[176:179], v[172:175], a[56:59]
v_mfma_f32_16x16x32_bf16 a[60:63], a[180:183], v[172:175], a[60:63]
s_waitcnt vmcnt(63)
v_mfma_f32_16x16x32_bf16 a[0:3], v[192:195], v[56:59], a[0:3]
v_mfma_f32_16x16x32_bf16 a[4:7], v[196:199], v[56:59], a[4:7]
v_mfma_f32_16x16x32_bf16 a[8:11], v[64:67], v[56:59], a[8:11]
global_load_dwordx2 %4, %17, off offset:0
v_mfma_f32_16x16x32_bf16 a[12:15], v[68:71], v[56:59], a[12:15]
global_load_dwordx2 %5, %17, off offset:32
v_mfma_f32_16x16x32_bf16 a[16:19], v[192:195], v[60:63], a[16:19]
global_load_dwordx2 %6, %17, off offset:64
v_mfma_f32_16x16x32_bf16 a[20:23], v[196:199], v[60:63], a[20:23]
global_load_dwordx2 %7, %17, off offset:96
v_mfma_f32_16x16x32_bf16 a[24:27], v[64:67], v[60:63], a[24:27]
buffer_load_dword v37, %13, s[24:27], 0 offen sc1
v_mfma_f32_16x16x32_bf16 a[28:31], v[68:71], v[60:63], a[28:31]
v_mfma_f32_16x16x32_bf16 a[32:35], v[192:195], v[184:187], a[32:35]
v_mfma_f32_16x16x32_bf16 a[36:39], v[196:199], v[184:187], a[36:39]
v_mfma_f32_16x16x32_bf16 a[40:43], v[64:67], v[184:187], a[40:43]
v_mfma_f32_16x16x32_bf16 a[44:47], v[68:71], v[184:187], a[44:47]
v_mfma_f32_16x16x32_bf16 a[48:51], v[192:195], v[188:191], a[48:51]
v_mfma_f32_16x16x32_bf16 a[52:55], v[196:199], v[188:191], a[52:55]
v_mfma_f32_16x16x32_bf16 a[56:59], v[64:67], v[188:191], a[56:59]
v_mfma_f32_16x16x32_bf16 a[60:63], v[68:71], v[188:191], a[60:63]
s_waitcnt vmcnt(63)
v_mfma_f32_16x16x32_bf16 a[0:3], v[208:211], a[184:187], a[0:3]
v_mfma_f32_16x16x32_bf16 a[4:7], v[212:215], a[184:187], a[4:7]
v_mfma_f32_16x16x32_bf16 a[8:11], a[192:195], a[184:187], a[8:11]
v_mfma_f32_16x16x32_bf16 a[12:15], a[196:199], a[184:187], a[12:15]
v_mfma_f32_16x16x32_bf16 a[16:19], v[208:211], a[188:191], a[16:19]
v_mfma_f32_16x16x32_bf16 a[20:23], v[212:215], a[188:191], a[20:23]
v_mfma_f32_16x16x32_bf16 a[24:27], a[192:195], a[188:191], a[24:27]
v_mfma_f32_16x16x32_bf16 a[28:31], a[196:199], a[188:191], a[28:31]
v_mfma_f32_16x16x32_bf16 a[32:35], v[208:211], v[200:203], a[32:35]
v_mfma_f32_16x16x32_bf16 a[36:39], v[212:215], v[200:203], a[36:39]
v_mfma_f32_16x16x32_bf16 a[40:43], a[192:195], v[200:203], a[40:43]
v_mfma_f32_16x16x32_bf16 a[44:47], a[196:199], v[200:203], a[44:47]
v_mfma_f32_16x16x32_bf16 a[48:51], v[208:211], v[204:207], a[48:51]
v_mfma_f32_16x16x32_bf16 a[52:55], v[212:215], v[204:207], a[52:55]
v_mfma_f32_16x16x32_bf16 a[56:59], a[192:195], v[204:207], a[56:59]
v_mfma_f32_16x16x32_bf16 a[60:63], a[196:199], v[204:207], a[60:63]
s_waitcnt vmcnt(61)
v_mfma_f32_16x16x32_bf16 a[0:3], v[224:227], v[72:75], a[0:3]
v_mfma_f32_16x16x32_bf16 a[4:7], v[228:231], v[72:75], a[4:7]
v_mfma_f32_16x16x32_bf16 a[8:11], v[80:83], v[72:75], a[8:11]
v_mfma_f32_16x16x32_bf16 a[12:15], v[84:87], v[72:75], a[12:15]
v_mfma_f32_16x16x32_bf16 a[16:19], v[224:227], v[76:79], a[16:19]
v_mfma_f32_16x16x32_bf16 a[20:23], v[228:231], v[76:79], a[20:23]
v_mfma_f32_16x16x32_bf16 a[24:27], v[80:83], v[76:79], a[24:27]
v_mfma_f32_16x16x32_bf16 a[28:31], v[84:87], v[76:79], a[28:31]
v_mfma_f32_16x16x32_bf16 a[32:35], v[224:227], v[216:219], a[32:35]
v_mfma_f32_16x16x32_bf16 a[36:39], v[228:231], v[216:219], a[36:39]
v_mfma_f32_16x16x32_bf16 a[40:43], v[80:83], v[216:219], a[40:43]
v_mfma_f32_16x16x32_bf16 a[44:47], v[84:87], v[216:219], a[44:47]
v_mfma_f32_16x16x32_bf16 a[48:51], v[224:227], v[220:223], a[48:51]
v_mfma_f32_16x16x32_bf16 a[52:55], v[228:231], v[220:223], a[52:55]
v_mfma_f32_16x16x32_bf16 a[56:59], v[80:83], v[220:223], a[56:59]
v_mfma_f32_16x16x32_bf16 a[60:63], v[84:87], v[220:223], a[60:63]
s_waitcnt vmcnt(53)
v_mfma_f32_16x16x32_bf16 a[0:3], v[240:243], a[200:203], a[0:3]
v_mfma_f32_16x16x32_bf16 a[4:7], v[244:247], a[200:203], a[4:7]
v_mfma_f32_16x16x32_bf16 a[8:11], a[208:211], a[200:203], a[8:11]
v_mfma_f32_16x16x32_bf16 a[12:15], a[212:215], a[200:203], a[12:15]
v_mfma_f32_16x16x32_bf16 a[16:19], v[240:243], a[204:207], a[16:19]
v_mfma_f32_16x16x32_bf16 a[20:23], v[244:247], a[204:207], a[20:23]
v_mfma_f32_16x16x32_bf16 a[24:27], a[208:211], a[204:207], a[24:27]
v_mfma_f32_16x16x32_bf16 a[28:31], a[212:215], a[204:207], a[28:31]
v_mfma_f32_16x16x32_bf16 a[32:35], v[240:243], v[232:235], a[32:35]
v_mfma_f32_16x16x32_bf16 a[36:39], v[244:247], v[232:235], a[36:39]
v_mfma_f32_16x16x32_bf16 a[40:43], a[208:211], v[232:235], a[40:43]
v_mfma_f32_16x16x32_bf16 a[44:47], a[212:215], v[232:235], a[44:47]
v_mfma_f32_16x16x32_bf16 a[48:51], v[240:243], v[236:239], a[48:51]
v_mfma_f32_16x16x32_bf16 a[52:55], v[244:247], v[236:239], a[52:55]
v_mfma_f32_16x16x32_bf16 a[56:59], a[208:211], v[236:239], a[56:59]
v_mfma_f32_16x16x32_bf16 a[60:63], a[212:215], v[236:239], a[60:63]
s_waitcnt vmcnt(45)
v_mfma_f32_16x16x32_bf16 a[0:3], a[64:67], v[88:91], a[0:3]
v_mfma_f32_16x16x32_bf16 a[4:7], a[68:71], v[88:91], a[4:7]
v_mfma_f32_16x16x32_bf16 a[8:11], v[96:99], v[88:91], a[8:11]
v_mfma_f32_16x16x32_bf16 a[12:15], v[100:103], v[88:91], a[12:15]
v_mfma_f32_16x16x32_bf16 a[16:19], a[64:67], v[92:95], a[16:19]
v_mfma_f32_16x16x32_bf16 a[20:23], a[68:71], v[92:95], a[20:23]
v_mfma_f32_16x16x32_bf16 a[24:27], v[96:99], v[92:95], a[24:27]
v_mfma_f32_16x16x32_bf16 a[28:31], v[100:103], v[92:95], a[28:31]
v_mfma_f32_16x16x32_bf16 a[32:35], a[64:67], v[248:251], a[32:35]
v_mfma_f32_16x16x32_bf16 a[36:39], a[68:71], v[248:251], a[36:39]
v_mfma_f32_16x16x32_bf16 a[40:43], v[96:99], v[248:251], a[40:43]
v_mfma_f32_16x16x32_bf16 a[44:47], v[100:103], v[248:251], a[44:47]
v_mfma_f32_16x16x32_bf16 a[48:51], a[64:67], v[252:255], a[48:51]
v_mfma_f32_16x16x32_bf16 a[52:55], a[68:71], v[252:255], a[52:55]
v_mfma_f32_16x16x32_bf16 a[56:59], v[96:99], v[252:255], a[56:59]
v_mfma_f32_16x16x32_bf16 a[60:63], v[100:103], v[252:255], a[60:63]
s_waitcnt vmcnt(37)
v_mfma_f32_16x16x32_bf16 a[0:3], a[80:83], a[216:219], a[0:3]
v_mfma_f32_16x16x32_bf16 a[4:7], a[84:87], a[216:219], a[4:7]
v_mfma_f32_16x16x32_bf16 a[8:11], a[224:227], a[216:219], a[8:11]
v_mfma_f32_16x16x32_bf16 a[12:15], a[228:231], a[216:219], a[12:15]
v_mfma_f32_16x16x32_bf16 a[16:19], a[80:83], a[220:223], a[16:19]
v_mfma_f32_16x16x32_bf16 a[20:23], a[84:87], a[220:223], a[20:23]
v_mfma_f32_16x16x32_bf16 a[24:27], a[224:227], a[220:223], a[24:27]
v_mfma_f32_16x16x32_bf16 a[28:31], a[228:231], a[220:223], a[28:31]
v_mfma_f32_16x16x32_bf16 a[32:35], a[80:83], a[72:75], a[32:35]
v_mfma_f32_16x16x32_bf16 a[36:39], a[84:87], a[72:75], a[36:39]
v_mfma_f32_16x16x32_bf16 a[40:43], a[224:227], a[72:75], a[40:43]
v_mfma_f32_16x16x32_bf16 a[44:47], a[228:231], a[72:75], a[44:47]
v_mfma_f32_16x16x32_bf16 a[48:51], a[80:83], a[76:79], a[48:51]
v_mfma_f32_16x16x32_bf16 a[52:55], a[84:87], a[76:79], a[52:55]
v_mfma_f32_16x16x32_bf16 a[56:59], a[224:227], a[76:79], a[56:59]
v_mfma_f32_16x16x32_bf16 a[60:63], a[228:231], a[76:79], a[60:63]
s_mov_b32 s36, 0x8000
s_waitcnt vmcnt(0)
L_z_chk:
v_cmp_eq_u32 vcc, 0, v37
s_sub_u32 s36, s36, 1
s_cbranch_scc1 L_z_ok
s_cbranch_vccz L_z_ok
s_sleep 4
buffer_load_dword v37, %13, s[24:27], 0 offen sc1
s_waitcnt vmcnt(0)
s_branch L_z_chk
L_z_ok:
s_mov_b32 s34, 0
buffer_load_dwordx2 %0, %15, s[24:27], s34 offen sc1
s_add_u32 s34, s34, 65536
buffer_load_dwordx2 %1, %15, s[24:27], s34 offen sc1
s_add_u32 s34, s34, 65536
buffer_load_dwordx2 %2, %15, s[24:27], s34 offen sc1
s_add_u32 s34, s34, 65536
buffer_load_dwordx2 %3, %15, s[24:27], s34 offen sc1
buffer_load_dword %8, %18, s[24:27], 0 offen sc1
s_waitcnt vmcnt(34)
v_mfma_f32_16x16x32_bf16 a[0:3], a[96:99], v[104:107], a[0:3]
v_mfma_f32_16x16x32_bf16 a[4:7], a[100:103], v[104:107], a[4:7]
v_mfma_f32_16x16x32_bf16 a[8:11], v[112:115], v[104:107], a[8:11]
v_mfma_f32_16x16x32_bf16 a[12:15], v[116:119], v[104:107], a[12:15]
v_mfma_f32_16x16x32_bf16 a[16:19], a[96:99], v[108:111], a[16:19]
v_mfma_f32_16x16x32_bf16 a[20:23], a[100:103], v[108:111], a[20:23]
v_mfma_f32_16x16x32_bf16 a[24:27], v[112:115], v[108:111], a[24:27]
v_mfma_f32_16x16x32_bf16 a[28:31], v[116:119], v[108:111], a[28:31]
v_mfma_f32_16x16x32_bf16 a[32:35], a[96:99], a[88:91], a[32:35]
v_mfma_f32_16x16x32_bf16 a[36:39], a[100:103], a[88:91], a[36:39]
v_mfma_f32_16x16x32_bf16 a[40:43], v[112:115], a[88:91], a[40:43]
v_mfma_f32_16x16x32_bf16 a[44:47], v[116:119], a[88:91], a[44:47]
v_mfma_f32_16x16x32_bf16 a[48:51], a[96:99], a[92:95], a[48:51]
v_mfma_f32_16x16x32_bf16 a[52:55], a[100:103], a[92:95], a[52:55]
v_mfma_f32_16x16x32_bf16 a[56:59], v[112:115], a[92:95], a[56:59]
v_mfma_f32_16x16x32_bf16 a[60:63], v[116:119], a[92:95], a[60:63]
s_waitcnt vmcnt(26)
v_mfma_f32_16x16x32_bf16 a[0:3], a[112:115], a[232:235], a[0:3]
v_mfma_f32_16x16x32_bf16 a[4:7], a[116:119], a[232:235], a[4:7]
v_mfma_f32_16x16x32_bf16 a[8:11], a[240:243], a[232:235], a[8:11]
v_mfma_f32_16x16x32_bf16 a[12:15], a[244:247], a[232:235], a[12:15]
v_mfma_f32_16x16x32_bf16 a[16:19], a[112:115], a[236:239], a[16:19]
v_mfma_f32_16x16x32_bf16 a[20:23], a[116:119], a[236:239], a[20:23]
v_mfma_f32_16x16x32_bf16 a[24:27], a[240:243], a[236:239], a[24:27]
v_mfma_f32_16x16x32_bf16 a[28:31], a[244:247], a[236:239], a[28:31]
v_mfma_f32_16x16x32_bf16 a[32:35], a[112:115], a[104:107], a[32:35]
v_mfma_f32_16x16x32_bf16 a[36:39], a[116:119], a[104:107], a[36:39]
v_mfma_f32_16x16x32_bf16 a[40:43], a[240:243], a[104:107], a[40:43]
v_mfma_f32_16x16x32_bf16 a[44:47], a[244:247], a[104:107], a[44:47]
v_mfma_f32_16x16x32_bf16 a[48:51], a[112:115], a[108:111], a[48:51]
v_mfma_f32_16x16x32_bf16 a[52:55], a[116:119], a[108:111], a[52:55]
v_mfma_f32_16x16x32_bf16 a[56:59], a[240:243], a[108:111], a[56:59]
v_mfma_f32_16x16x32_bf16 a[60:63], a[244:247], a[108:111], a[60:63]
s_waitcnt vmcnt(18)
v_mfma_f32_16x16x32_bf16 a[0:3], a[128:131], v[120:123], a[0:3]
v_mfma_f32_16x16x32_bf16 a[4:7], a[132:135], v[120:123], a[4:7]
v_mfma_f32_16x16x32_bf16 a[8:11], v[128:131], v[120:123], a[8:11]
v_mfma_f32_16x16x32_bf16 a[12:15], v[132:135], v[120:123], a[12:15]
v_mfma_f32_16x16x32_bf16 a[16:19], a[128:131], v[124:127], a[16:19]
v_mfma_f32_16x16x32_bf16 a[20:23], a[132:135], v[124:127], a[20:23]
v_mfma_f32_16x16x32_bf16 a[24:27], v[128:131], v[124:127], a[24:27]
v_mfma_f32_16x16x32_bf16 a[28:31], v[132:135], v[124:127], a[28:31]
v_mfma_f32_16x16x32_bf16 a[32:35], a[128:131], a[120:123], a[32:35]
v_mfma_f32_16x16x32_bf16 a[36:39], a[132:135], a[120:123], a[36:39]
v_mfma_f32_16x16x32_bf16 a[40:43], v[128:131], a[120:123], a[40:43]
v_mfma_f32_16x16x32_bf16 a[44:47], v[132:135], a[120:123], a[44:47]
v_mfma_f32_16x16x32_bf16 a[48:51], a[128:131], a[124:127], a[48:51]
v_mfma_f32_16x16x32_bf16 a[52:55], a[132:135], a[124:127], a[52:55]
v_mfma_f32_16x16x32_bf16 a[56:59], v[128:131], a[124:127], a[56:59]
v_mfma_f32_16x16x32_bf16 a[60:63], v[132:135], a[124:127], a[60:63]
s_waitcnt vmcnt(10)
v_mfma_f32_16x16x32_bf16 a[0:3], a[144:147], a[248:251], a[0:3]
v_mfma_f32_16x16x32_bf16 a[4:7], a[148:151], a[248:251], a[4:7]
v_mfma_f32_16x16x32_bf16 a[8:11], a[152:155], a[248:251], a[8:11]
v_mfma_f32_16x16x32_bf16 a[12:15], a[156:159], a[248:251], a[12:15]
v_mfma_f32_16x16x32_bf16 a[16:19], a[144:147], a[252:255], a[16:19]
v_mfma_f32_16x16x32_bf16 a[20:23], a[148:151], a[252:255], a[20:23]
v_mfma_f32_16x16x32_bf16 a[24:27], a[152:155], a[252:255], a[24:27]
v_mfma_f32_16x16x32_bf16 a[28:31], a[156:159], a[252:255], a[28:31]
v_mfma_f32_16x16x32_bf16 a[32:35], a[144:147], a[136:139], a[32:35]
v_mfma_f32_16x16x32_bf16 a[36:39], a[148:151], a[136:139], a[36:39]
v_mfma_f32_16x16x32_bf16 a[40:43], a[152:155], a[136:139], a[40:43]
v_mfma_f32_16x16x32_bf16 a[44:47], a[156:159], a[136:139], a[44:47]
v_mfma_f32_16x16x32_bf16 a[48:51], a[144:147], a[140:143], a[48:51]
v_mfma_f32_16x16x32_bf16 a[52:55], a[148:151], a[140:143], a[52:55]
v_mfma_f32_16x16x32_bf16 a[56:59], a[152:155], a[140:143], a[56:59]
v_mfma_f32_16x16x32_bf16 a[60:63], a[156:159], a[140:143], a[60:63]
s_nop 7
s_nop 3
ds_write_b128 %16, a[0:3] offset:0
ds_write_b128 %16, a[4:7] offset:1024
ds_write_b128 %16, a[8:11] offset:2048
ds_write_b128 %16, a[12:15] offset:3072
ds_write_b128 %16, a[16:19] offset:4096
ds_write_b128 %16, a[20:23] offset:5120
ds_write_b128 %16, a[24:27] offset:6144
ds_write_b128 %16, a[28:31] offset:7168
ds_write_b128 %16, a[32:35] offset:8192
ds_write_b128 %16, a[36:39] offset:9216
ds_write_b128 %16, a[40:43] offset:10240
ds_write_b128 %16, a[44:47] offset:11264
ds_write_b128 %16, a[48:51] offset:12288
ds_write_b128 %16, a[52:55] offset:13312
ds_write_b128 %16, a[56:59] offset:14336
ds_write_b128 %16, a[60:63] offset:15360
s_waitcnt vmcnt(0) lgkmcnt(0)
)ASM"
  : "=&v"(z0), "=&v"(z1), "=&v"(z2), "=&v"(z3), "=&v"(x0), "=&v"(x1), "=&v"(x2), "=&v"(x3), "=&v"(gg)
  : "s"(ws), "s"(oc), "s"(ow), "s"(ofx), "v"(ofc), "v"(lane * 16), "v"(oz), "v"((unsigned)(size_t)lds + (unsigned)(w * 16384 + lane * 16)),
    "v"(x + oo), "v"(WS_G + 4u * q)
  : "v32", "v33", "v34", "v35", "v36", "v37", "v38", "v39", "v40", "v41", "v42", "v43", "v44", "v45", "v46", "v47", "v48", "v49", "v50", "v51", "v52", "v53", "v54", "v55", "v56", "v57", "v58", "v59", "v60", "v61", "v62", "v63", "v64", "v65", "v66", "v67", "v68", "v69", "v70", "v71", "v72", "v73", "v74", "v75", "v76", "v77", "v78", "v79", "v80", "v81", "v82", "v83", "v84", "v85", "v86", "v87", "v88", "v89", "v90", "v91", "v92", "v93", "v94", "v95", "v96", "v97", "v98", "v99", "v100", "v101", "v102", "v103", "v104", "v105", "v106", "v107", "v108", "v109", "v110", "v111", "v112", "v113", "v114", "v115", "v116", "v117", "v118", "v119", "v120", "v121", "v122", "v123", "v124", "v125", "v126", "v127", "v128", "v129", "v130", "v131", "v132", "v133", "v134", "v135", "v136", "v137", "v138", "v139", "v140", "v141", "v142", "v143", "v144", "v145", "v146", "v147", "v148", "v149", "v150", "v151", "v152", "v153", "v154", "v155", "v156", "v157", "v158", "v159", "v160", "v161", "v162", "v163", "v164", "v165", "v166", "v167", "v168", "v169", "v170", "v171", "v172", "v173", "v174", "v175", "v176", "v177", "v178", "v179", "v180", "v181", "v182", "v183", "v184", "v185", "v186", "v187", "v188", "v189", "v190", "v191", "v192", "v193", "v194", "v195", "v196", "v197", "v198", "v199", "v200", "v201", "v202", "v203", "v204", "v205", "v206", "v207", "v208", "v209", "v210", "v211", "v212", "v213", "v214", "v215", "v216", "v217", "v218", "v219", "v220", "v221", "v222", "v223", "v224", "v225", "v226", "v227", "v228", "v229", "v230", "v231", "v232", "v233", "v234", "v235", "v236", "v237", "v238", "v239", "v240", "v241", "v242", "v243", "v244", "v245", "v246", "v247", "v248", "v249", "v250", "v251", "v252", "v253", "v254", "v255", "a0", "a1", "a2", "a3", "a4", "a5", "a6", "a7", "a8", "a9", "a10", "a11", "a12", "a13", "a14", "a15", "a16", "a17", "a18", "a19", "a20", "a21", "a22", "a23", "a24", "a25", "a26", "a27", "a28", "a29", "a30", "a31", "a32", "a33", "a34", "a35", "a36", "a37", "a38", "a39", "a40", "a41", "a42", "a43", "a44", "a45", "a46", "a47", "a48", "a49", "a50", "a51", "a52", "a53", "a54", "a55", "a56", "a57", "a58", "a59", "a60", "a61", "a62", "a63", "a64", "a65", "a66", "a67", "a68", "a69", "a70", "a71", "a72", "a73", "a74", "a75", "a76", "a77", "a78", "a79", "a80", "a81", "a82", "a83", "a84", "a85", "a86", "a87", "a88", "a89", "a90", "a91", "a92", "a93", "a94", "a95", "a96", "a97", "a98", "a99", "a100", "a101", "a102", "a103", "a104", "a105", "a106", "a107", "a108", "a109", "a110", "a111", "a112", "a113", "a114", "a115", "a116", "a117", "a118", "a119", "a120", "a121", "a122", "a123", "a124", "a125", "a126", "a127", "a128", "a129", "a130", "a131", "a132", "a133", "a134", "a135", "a136", "a137", "a138", "a139", "a140", "a141", "a142", "a143", "a144", "a145", "a146", "a147", "a148", "a149", "a150", "a151", "a152", "a153", "a154", "a155", "a156", "a157", "a158", "a159", "a160", "a161", "a162", "a163", "a164", "a165", "a166", "a167", "a168", "a169", "a170", "a171", "a172", "a173", "a174", "a175", "a176", "a177", "a178", "a179", "a180", "a181", "a182", "a183", "a184", "a185", "a186", "a187", "a188", "a189", "a190", "a191", "a192", "a193", "a194", "a195", "a196", "a197", "a198", "a199", "a200", "a201", "a202", "a203", "a204", "a205", "a206", "a207", "a208", "a209", "a210", "a211", "a212", "a213", "a214", "a215", "a216", "a217", "a218", "a219", "a220", "a221", "a222", "a223", "a224", "a225", "a226", "a227", "a228", "a229", "a230", "a231", "a232", "a233", "a234", "a235", "a236", "a237", "a238", "a239", "a240", "a241", "a242", "a243", "a244", "a245", "a246", "a247", "a248", "a249", "a250", "a251", "a252", "a253", "a254", "a255", "s24", "s25", "s26", "s27", "s28", "s29", "s30", "s31", "s34", "s35", "s36", "vcc", "scc", "memory");
  const u32x2 xo[4] = {x0, x1, x2, x3};
  const float og = rbf(1.0f - gg);
  __syncthreads();
  const float* red = (const float*)lds;
  const u32x2 zz[4] = {z0, z1, z2, z3};
#pragma unroll
  for (int j = 0; j < 4; j++) {
    f32x4 sm = *(const f32x4*)(red + ((0 * 16 + w * 4 + j) * 64 + lane) * 4);
#pragma unroll
    for (int u = 1; u < 4; u++) sm += *(const f32x4*)(red + ((u * 16 + w * 4 + j) * 64 + lane) * 4);
    u32x2 ov;
#pragma unroll
    for (int h = 0; h < 2; h++) {
      unsigned av = pk2(lo16(xo[j][h]) * lo16(zz[j][h]), hi16(xo[j][h]) * hi16(zz[j][h]));
      unsigned ga = pk2(gg * lo16(av), gg * hi16(av));
      unsigned bb = pk2(sm[2 * h], sm[2 * h + 1]);
      unsigned gb = pk2(og * lo16(bb), og * hi16(bb));
      ov[h] = pk2(lo16(ga) + lo16(gb), hi16(ga) + hi16(gb));
    }
    *(u32x2*)(out + oo + j * 16) = ov;
  }
}

__global__ __launch_bounds__(256, 1) void kf_main(const void* c, const void* wuk, const void* wuv, char* ws, const u16* x, const u16* wq,
                                                   const u16* kin, const u16* vin, const u16* shk, const u16* wo, const u16* gw,
                                                   const u16* ssk, const u16* ssd, u16* out) {
  __shared__ __attribute__((aligned(16))) char lds[163840];
  asm volatile(R"ASM(
L_kf0:
s_mov_b64 s[58:59], %0
s_mov_b64 s[62:63], %1
s_mov_b64 s[64:65], %2
s_mov_b64 s[66:67], %3
s_mov_b32 s68, %4
s_mov_b32 s48, %6
v_and_b32 v8, 63, %5
v_lshrrev_b32 v44, 6, %5
s_nop 1
v_readfirstlane_b32 s45, v44
s_nop 3
s_lshr_b32 s46, s45, 1
s_and_b32 s47, s45, 1
s_and_b32 s49, s68, 7
s_lshr_b32 s50, s68, 3
s_lshl_b32 s49, s49, 1
s_and_b32 s51, s50, 1
s_add_u32 s70, s49, s51
s_bfe_u32 s69, s50, 0x10001
s_lshr_b32 s71, s50, 2
s_mov_b32 s27, 0x20000
s_mov_b32 s31, 0x20000
s_lshl_b32 s49, s71, 19
s_add_u32 s24, s58, s49
s_addc_u32 s25, s59, 0
s_and_b32 s25, s25, 0xffff
s_mov_b32 s43, 524288
s_cmp_eq_u32 s69, 0
s_cselect_b64 s[28:29], s[62:63], s[64:65]
s_lshl_b32 s49, s70, 9
s_add_u32 s28, s28, s49
s_addc_u32 s29, s29, 0
s_and_b32 s29, s29, 0xffff
s_sub_u32 s30, 8388608, s49
v_lshrrev_b32 v44, 3, v8
v_lshrrev_b32 v45, 4, v8
v_and_b32 v46, 7, v8
v_xor_b32 v46, v46, v45
v_xor_b32 v47, 4, v46
s_mul_i32 s49, s45, 131072
v_mul_u32_u24 v44, 2048, v44
v_add_u32 v44, s49, v44
v_lshl_add_u32 v10, v46, 4, v44
v_lshl_add_u32 v11, v47, 4, v44
v_add_u32 v11, 15360, v11
v_lshl_add_u32 v12, v46, 4, v44
v_add_u32 v12, 30720, v12
v_lshl_add_u32 v13, v47, 4, v44
v_add_u32 v13, 46080, v13
v_lshl_add_u32 v14, v46, 4, v44
v_add_u32 v14, 65536, v14
v_lshl_add_u32 v15, v47, 4, v44
v_add_u32 v15, 80896, v15
v_lshl_add_u32 v16, v46, 4, v44
v_add_u32 v16, 96256, v16
v_lshl_add_u32 v17, v47, 4, v44
v_add_u32 v17, 111616, v17
s_mov_b32 s36, 0
s_mov_b32 s37, 0
s_mul_i32 s38, s45, 8192
s_add_u32 s38, s38, s48
s_mov_b32 s39, 0
s_mov_b32 s40, 0
s_mov_b32 s41, 0
s_mov_b32 s44, 16
s_mov_b32 s26, s43
s_add_u32 s52, s48, 98304
s_lshl_b32 s51, s45, 12
s_add_u32 s51, s51, s52
s_add_u32 m0, s38, 0
s_nop 0
buffer_load_dwordx4 v10, s[24:27], s36 offen offset:0 lds
buffer_load_dwordx4 v11, s[24:27], s36 offen offset:1024 lds
buffer_load_dwordx4 v12, s[24:27], s36 offen offset:2048 lds
buffer_load_dwordx4 v13, s[24:27], s36 offen offset:3072 lds
v_lshrrev_b32 v44, 5, v8
v_and_b32 v45, 31, v8
v_lshlrev_b32 v46, 2, v44
v_xor_b32 v45, v45, v46
s_lshl_b32 s49, s47, 1
v_xor_b32 v45, s49, v45
v_xor_b32 v46, 8, v45
s_lshl_b32 s49, s45, 16
v_lshl_add_u32 v44, v44, 13, s49
v_lshl_add_u32 v18, v45, 4, v44
v_lshl_add_u32 v19, v46, 4, v44
v_add_u32 v19, 15360, v19
v_lshl_add_u32 v20, v45, 4, v44
v_add_u32 v20, 30720, v20
v_lshl_add_u32 v21, v46, 4, v44
v_add_u32 v21, 46080, v21
v_bfe_u32 v44, v8, 2, 2
v_lshrrev_b32 v45, 4, v8
v_lshl_add_u32 v45, v45, 3, v44
v_lshlrev_b32 v45, 9, v45
v_and_b32 v46, 3, v8
v_lshl_add_u32 v45, v46, 3, v45
s_lshl_b32 s49, s47, 8
s_add_u32 s49, s49, 98304
s_add_u32 s49, s49, s48
v_add_u32 v45, s49, v45
v_bfe_u32 v46, v8, 4, 1
v_lshl_add_u32 v44, v44, 1, v46
v_xor_b32 v46, 0, v44
v_lshl_add_u32 v26, v46, 5, v45
v_xor_b32 v46, 1, v44
v_lshl_add_u32 v27, v46, 5, v45
v_xor_b32 v46, 2, v44
v_lshl_add_u32 v28, v46, 5, v45
v_xor_b32 v46, 3, v44
v_lshl_add_u32 v29, v46, 5, v45
v_xor_b32 v46, 4, v44
v_lshl_add_u32 v30, v46, 5, v45
v_xor_b32 v46, 5, v44
v_lshl_add_u32 v31, v46, 5, v45
v_xor_b32 v46, 6, v44
v_lshl_add_u32 v32, v46, 5, v45
v_xor_b32 v46, 7, v44
v_lshl_add_u32 v33, v46, 5, v45
s_add_u32 m0, s39, s51
s_nop 0
buffer_load_dwordx4 v18, s[28:31], s37 offen offset:0 lds
buffer_load_dwordx4 v19, s[28:31], s37 offen offset:1024 lds
buffer_load_dwordx4 v20, s[28:31], s37 offen offset:2048 lds
buffer_load_dwordx4 v21, s[28:31], s37 offen offset:3072 lds
s_add_u32 s39, s39, 16384
s_and_b32 s39, s39, 0xffff
s_add_u32 s37, s37, 262144
s_add_u32 m0, s38, 4096
s_nop 0
buffer_load_dwordx4 v14, s[24:27], s36 offen offset:0 lds
buffer_load_dwordx4 v15, s[24:27], s36 offen offset:1024 lds
buffer_load_dwordx4 v16, s[24:27], s36 offen offset:2048 lds
buffer_load_dwordx4 v17, s[24:27], s36 offen offset:3072 lds
s_add_u32 s38, s38, 32768
s_cmp_ge_u32 s38, s52
s_cselect_b32 s49, 98304, 0
s_sub_u32 s38, s38, s49
s_add_u32 s36, s36, 128
s_sub_u32 s44, s44, 1
s_cmp_gt_i32 s44, 0
s_cselect_b32 s26, s43, 0
s_lshl_b32 s54, s68, 2
s_add_u32 s54, s54, s45
s_mov_b64 s[76:77], %8
s_mov_b32 s78, 3670016
s_mov_b32 s55, s54
s_sub_u32 s56, s54, 200
s_cmp_ge_u32 s54, 200
s_cselect_b64 s[76:77], %7, s[76:77]
s_cselect_b32 s78, 1835008, s78
s_cselect_b32 s55, s56, s55
s_sub_u32 s56, s54, 300
s_cmp_ge_u32 s54, 300
s_cselect_b64 s[76:77], %12, s[76:77]
s_cselect_b32 s78, 3670016, s78
s_cselect_b32 s55, s56, s55
s_sub_u32 s56, s54, 500
s_cmp_ge_u32 s54, 500
s_cselect_b64 s[76:77], %9, s[76:77]
s_cselect_b32 s78, 1835008, s78
s_cselect_b32 s55, s56, s55
s_sub_u32 s56, s54, 600
s_cmp_ge_u32 s54, 600
s_cselect_b64 s[76:77], %10, s[76:77]
s_cselect_b32 s78, 1835008, s78
s_cselect_b32 s55, s56, s55
s_sub_u32 s56, s54, 700
s_cmp_ge_u32 s54, 700
s_cselect_b64 s[76:77], %13, s[76:77]
s_cselect_b32 s78, 1835008, s78
s_cselect_b32 s55, s56, s55
s_sub_u32 s56, s54, 800
s_cmp_ge_u32 s54, 800
s_cselect_b64 s[76:77], %13, s[76:77]
s_cselect_b32 s78, 0, s78
s_cselect_b32 s55, s56, s55
s_getpc_b64 s[60:61]
L_code0:
s_mov_b32 s56, L_z_ok-L_code0
s_cmp_lt_u32 s56, 0x12000
s_cselect_b32 s56, s56, 0
s_lshr_b32 s57, s54, 5
s_cmp_eq_u32 s57, 25
s_cselect_b64 s[76:77], s[60:61], s[76:77]
s_cselect_b32 s78, s56, s78
s_cselect_b32 s55, s45, s55
s_and_b32 s77, s77, 0xffff
s_mov_b32 s79, 0x20000
s_mul_i32 s55, s55, 18432
v_lshlrev_b32 v52, 4, v8
v_add_u32 v52, s55, v52
buffer_load_dwordx4 v[48:51], v52, s[76:79], 0 offen
v_add_u32 v52, 1024, v52
s_add_u32 m0, s39, s51
s_nop 0
buffer_load_dwordx4 v18, s[28:31], s37 offen offset:0 lds
buffer_load_dwordx4 v19, s[28:31], s37 offen offset:1024 lds
buffer_load_dwordx4 v20, s[28:31], s37 offen offset:2048 lds
buffer_load_dwordx4 v21, s[28:31], s37 offen offset:3072 lds
s_add_u32 s39, s39, 16384
s_and_b32 s39, s39, 0xffff
s_add_u32 s37, s37, 262144
s_add_u32 m0, s38, 0
s_nop 0
buffer_load_dwordx4 v10, s[24:27], s36 offen offset:0 lds
buffer_load_dwordx4 v11, s[24:27], s36 offen offset:1024 lds
buffer_load_dwordx4 v12, s[24:27], s36 offen offset:2048 lds
buffer_load_dwordx4 v13, s[24:27], s36 offen offset:3072 lds
s_add_u32 m0, s39, s51
s_nop 0
buffer_load_dwordx4 v18, s[28:31], s37 offen offset:0 lds
buffer_load_dwordx4 v19, s[28:31], s37 offen offset:1024 lds
buffer_load_dwordx4 v20, s[28:31], s37 offen offset:2048 lds
buffer_load_dwordx4 v21, s[28:31], s37 offen offset:3072 lds
s_add_u32 s39, s39, 16384
s_and_b32 s39, s39, 0xffff
s_add_u32 s37, s37, 262144
s_add_u32 m0, s38, 4096
s_nop 0
buffer_load_dwordx4 v14, s[24:27], s36 offen offset:0 lds
buffer_load_dwordx4 v15, s[24:27], s36 offen offset:1024 lds
buffer_load_dwordx4 v16, s[24:27], s36 offen offset:2048 lds
buffer_load_dwordx4 v17, s[24:27], s36 offen offset:3072 lds
s_add_u32 s38, s38, 32768
s_cmp_ge_u32 s38, s52
s_cselect_b32 s49, 98304, 0
s_sub_u32 s38, s38, s49
s_add_u32 s36, s36, 128
s_sub_u32 s44, s44, 1
s_cmp_gt_i32 s44, 0
s_cselect_b32 s26, s43, 0
buffer_load_dwordx4 v[48:51], v52, s[76:79], 0 offen
v_add_u32 v52, 1024, v52
s_add_u32 m0, s39, s51
s_nop 0
buffer_load_dwordx4 v18, s[28:31], s37 offen offset:0 lds
buffer_load_dwordx4 v19, s[28:31], s37 offen offset:1024 lds
buffer_load_dwordx4 v20, s[28:31], s37 offen offset:2048 lds
buffer_load_dwordx4 v21, s[28:31], s37 offen offset:3072 lds
s_add_u32 s39, s39, 16384
s_and_b32 s39, s39, 0xffff
s_add_u32 s37, s37, 262144
s_add_u32 m0, s38, 0
s_nop 0
buffer_load_dwordx4 v10, s[24:27], s36 offen offset:0 lds
buffer_load_dwordx4 v11, s[24:27], s36 offen offset:1024 lds
buffer_load_dwordx4 v12, s[24:27], s36 offen offset:2048 lds
buffer_load_dwordx4 v13, s[24:27], s36 offen offset:3072 lds
v_and_b32 v44, 15, v8
s_mul_i32 s49, s46, 16384
s_add_u32 s49, s49, s48
v_lshl_add_u32 v44, v44, 7, s49
v_lshrrev_b32 v45, 4, v8
v_bfe_u32 v46, v8, 1, 3
v_xor_b32 v45, v45, v46
v_xor_b32 v46, 0, v45
v_lshl_add_u32 v22, v46, 4, v44
v_mov_b32 v24, v22
v_xor_b32 v46, 4, v45
v_lshl_add_u32 v23, v46, 4, v44
v_mov_b32 v25, v23
v_mov_b32 v34, v26
v_mov_b32 v35, v27
v_mov_b32 v36, v28
v_mov_b32 v37, v29
v_mov_b32 v38, v30
v_mov_b32 v39, v31
v_mov_b32 v40, v32
v_mov_b32 v41, v33
v_accvgpr_write_b32 a0, 0
v_accvgpr_write_b32 a1, 0
v_accvgpr_write_b32 a2, 0
v_accvgpr_write_b32 a3, 0
v_accvgpr_write_b32 a4, 0
v_accvgpr_write_b32 a5, 0
v_accvgpr_write_b32 a6, 0
v_accvgpr_write_b32 a7, 0
v_accvgpr_write_b32 a8, 0
v_accvgpr_write_b32 a9, 0
v_accvgpr_write_b32 a10, 0
v_accvgpr_write_b32 a11, 0
v_accvgpr_write_b32 a12, 0
v_accvgpr_write_b32 a13, 0
v_accvgpr_write_b32 a14, 0
v_accvgpr_write_b32 a15, 0
v_accvgpr_write_b32 a16, 0
v_accvgpr_write_b32 a17, 0
v_accvgpr_write_b32 a18, 0
v_accvgpr_write_b32 a19, 0
v_accvgpr_write_b32 a20, 0
v_accvgpr_write_b32 a21, 0
v_accvgpr_write_b32 a22, 0
v_accvgpr_write_b32 a23, 0
v_accvgpr_write_b32 a24, 0
v_accvgpr_write_b32 a25, 0
v_accvgpr_write_b32 a26, 0
v_accvgpr_write_b32 a27, 0
v_accvgpr_write_b32 a28, 0
v_accvgpr_write_b32 a29, 0
v_accvgpr_write_b32 a30, 0
v_accvgpr_write_b32 a31, 0
v_accvgpr_write_b32 a32, 0
v_accvgpr_write_b32 a33, 0
v_accvgpr_write_b32 a34, 0
v_accvgpr_write_b32 a35, 0
v_accvgpr_write_b32 a36, 0
v_accvgpr_write_b32 a37, 0
v_accvgpr_write_b32 a38, 0
v_accvgpr_write_b32 a39, 0
v_accvgpr_write_b32 a40, 0
v_accvgpr_write_b32 a41, 0
v_accvgpr_write_b32 a42, 0
v_accvgpr_write_b32 a43, 0
v_accvgpr_write_b32 a44, 0
v_accvgpr_write_b32 a45, 0
v_accvgpr_write_b32 a46, 0
v_accvgpr_write_b32 a47, 0
v_accvgpr_write_b32 a48, 0
v_accvgpr_write_b32 a49, 0
v_accvgpr_write_b32 a50, 0
v_accvgpr_write_b32 a51, 0
v_accvgpr_write_b32 a52, 0
v_accvgpr_write_b32 a53, 0
v_accvgpr_write_b32 a54, 0
v_accvgpr_write_b32 a55, 0
v_accvgpr_write_b32 a56, 0
v_accvgpr_write_b32 a57, 0
v_accvgpr_write_b32 a58, 0
v_accvgpr_write_b32 a59, 0
v_accvgpr_write_b32 a60, 0
v_accvgpr_write_b32 a61, 0
v_accvgpr_write_b32 a62, 0
v_accvgpr_write_b32 a63, 0
v_accvgpr_write_b32 a64, 0
v_accvgpr_write_b32 a65, 0
v_accvgpr_write_b32 a66, 0
v_accvgpr_write_b32 a67, 0
v_accvgpr_write_b32 a68, 0
v_accvgpr_write_b32 a69, 0
v_accvgpr_write_b32 a70, 0
v_accvgpr_write_b32 a71, 0
v_accvgpr_write_b32 a72, 0
v_accvgpr_write_b32 a73, 0
v_accvgpr_write_b32 a74, 0
v_accvgpr_write_b32 a75, 0
v_accvgpr_write_b32 a76, 0
v_accvgpr_write_b32 a77, 0
v_accvgpr_write_b32 a78, 0
v_accvgpr_write_b32 a79, 0
v_accvgpr_write_b32 a80, 0
v_accvgpr_write_b32 a81, 0
v_accvgpr_write_b32 a82, 0
v_accvgpr_write_b32 a83, 0
v_accvgpr_write_b32 a84, 0
v_accvgpr_write_b32 a85, 0
v_accvgpr_write_b32 a86, 0
v_accvgpr_write_b32 a87, 0
v_accvgpr_write_b32 a88, 0
v_accvgpr_write_b32 a89, 0
v_accvgpr_write_b32 a90, 0
v_accvgpr_write_b32 a91, 0
v_accvgpr_write_b32 a92, 0
v_accvgpr_write_b32 a93, 0
v_accvgpr_write_b32 a94, 0
v_accvgpr_write_b32 a95, 0
v_accvgpr_write_b32 a96, 0
v_accvgpr_write_b32 a97, 0
v_accvgpr_write_b32 a98, 0
v_accvgpr_write_b32 a99, 0
v_accvgpr_write_b32 a100, 0
v_accvgpr_write_b32 a101, 0
v_accvgpr_write_b32 a102, 0
v_accvgpr_write_b32 a103, 0
v_accvgpr_write_b32 a104, 0
v_accvgpr_write_b32 a105, 0
v_accvgpr_write_b32 a106, 0
v_accvgpr_write_b32 a107, 0
v_accvgpr_write_b32 a108, 0
v_accvgpr_write_b32 a109, 0
v_accvgpr_write_b32 a110, 0
v_accvgpr_write_b32 a111, 0
v_accvgpr_write_b32 a112, 0
v_accvgpr_write_b32 a113, 0
v_accvgpr_write_b32 a114, 0
v_accvgpr_write_b32 a115, 0
v_accvgpr_write_b32 a116, 0
v_accvgpr_write_b32 a117, 0
v_accvgpr_write_b32 a118, 0
v_accvgpr_write_b32 a119, 0
v_accvgpr_write_b32 a120, 0
v_accvgpr_write_b32 a121, 0
v_accvgpr_write_b32 a122, 0
v_accvgpr_write_b32 a123, 0
v_accvgpr_write_b32 a124, 0
v_accvgpr_write_b32 a125, 0
v_accvgpr_write_b32 a126, 0
v_accvgpr_write_b32 a127, 0
v_accvgpr_write_b32 a128, 0
v_accvgpr_write_b32 a129, 0
v_accvgpr_write_b32 a130, 0
v_accvgpr_write_b32 a131, 0
v_accvgpr_write_b32 a132, 0
v_accvgpr_write_b32 a133, 0
v_accvgpr_write_b32 a134, 0
v_accvgpr_write_b32 a135, 0
v_accvgpr_write_b32 a136, 0
v_accvgpr_write_b32 a137, 0
v_accvgpr_write_b32 a138, 0
v_accvgpr_write_b32 a139, 0
v_accvgpr_write_b32 a140, 0
v_accvgpr_write_b32 a141, 0
v_accvgpr_write_b32 a142, 0
v_accvgpr_write_b32 a143, 0
v_accvgpr_write_b32 a144, 0
v_accvgpr_write_b32 a145, 0
v_accvgpr_write_b32 a146, 0
v_accvgpr_write_b32 a147, 0
v_accvgpr_write_b32 a148, 0
v_accvgpr_write_b32 a149, 0
v_accvgpr_write_b32 a150, 0
v_accvgpr_write_b32 a151, 0
v_accvgpr_write_b32 a152, 0
v_accvgpr_write_b32 a153, 0
v_accvgpr_write_b32 a154, 0
v_accvgpr_write_b32 a155, 0
v_accvgpr_write_b32 a156, 0
v_accvgpr_write_b32 a157, 0
v_accvgpr_write_b32 a158, 0
v_accvgpr_write_b32 a159, 0
v_accvgpr_write_b32 a160, 0
v_accvgpr_write_b32 a161, 0
v_accvgpr_write_b32 a162, 0
v_accvgpr_write_b32 a163, 0
v_accvgpr_write_b32 a164, 0
v_accvgpr_write_b32 a165, 0
v_accvgpr_write_b32 a166, 0
v_accvgpr_write_b32 a167, 0
v_accvgpr_write_b32 a168, 0
v_accvgpr_write_b32 a169, 0
v_accvgpr_write_b32 a170, 0
v_accvgpr_write_b32 a171, 0
v_accvgpr_write_b32 a172, 0
v_accvgpr_write_b32 a173, 0
v_accvgpr_write_b32 a174, 0
v_accvgpr_write_b32 a175, 0
v_accvgpr_write_b32 a176, 0
v_accvgpr_write_b32 a177, 0
v_accvgpr_write_b32 a178, 0
v_accvgpr_write_b32 a179, 0
v_accvgpr_write_b32 a180, 0
v_accvgpr_write_b32 a181, 0
v_accvgpr_write_b32 a182, 0
v_accvgpr_write_b32 a183, 0
v_accvgpr_write_b32 a184, 0
v_accvgpr_write_b32 a185, 0
v_accvgpr_write_b32 a186, 0
v_accvgpr_write_b32 a187, 0
v_accvgpr_write_b32 a188, 0
v_accvgpr_write_b32 a189, 0
v_accvgpr_write_b32 a190, 0
v_accvgpr_write_b32 a191, 0
v_accvgpr_write_b32 a192, 0
v_accvgpr_write_b32 a193, 0
v_accvgpr_write_b32 a194, 0
v_accvgpr_write_b32 a195, 0
v_accvgpr_write_b32 a196, 0
v_accvgpr_write_b32 a197, 0
v_accvgpr_write_b32 a198, 0
v_accvgpr_write_b32 a199, 0
v_accvgpr_write_b32 a200, 0
v_accvgpr_write_b32 a201, 0
v_accvgpr_write_b32 a202, 0
v_accvgpr_write_b32 a203, 0
v_accvgpr_write_b32 a204, 0
v_accvgpr_write_b32 a205, 0
v_accvgpr_write_b32 a206, 0
v_accvgpr_write_b32 a207, 0
v_accvgpr_write_b32 a208, 0
v_accvgpr_write_b32 a209, 0
v_accvgpr_write_b32 a210, 0
v_accvgpr_write_b32 a211, 0
v_accvgpr_write_b32 a212, 0
v_accvgpr_write_b32 a213, 0
v_accvgpr_write_b32 a214, 0
v_accvgpr_write_b32 a215, 0
v_accvgpr_write_b32 a216, 0
v_accvgpr_write_b32 a217, 0
v_accvgpr_write_b32 a218, 0
v_accvgpr_write_b32 a219, 0
v_accvgpr_write_b32 a220, 0
v_accvgpr_write_b32 a221, 0
v_accvgpr_write_b32 a222, 0
v_accvgpr_write_b32 a223, 0
v_accvgpr_write_b32 a224, 0
v_accvgpr_write_b32 a225, 0
v_accvgpr_write_b32 a226, 0
v_accvgpr_write_b32 a227, 0
v_accvgpr_write_b32 a228, 0
v_accvgpr_write_b32 a229, 0
v_accvgpr_write_b32 a230, 0
v_accvgpr_write_b32 a231, 0
v_accvgpr_write_b32 a232, 0
v_accvgpr_write_b32 a233, 0
v_accvgpr_write_b32 a234, 0
v_accvgpr_write_b32 a235, 0
v_accvgpr_write_b32 a236, 0
v_accvgpr_write_b32 a237, 0
v_accvgpr_write_b32 a238, 0
v_accvgpr_write_b32 a239, 0
v_accvgpr_write_b32 a240, 0
v_accvgpr_write_b32 a241, 0
v_accvgpr_write_b32 a242, 0
v_accvgpr_write_b32 a243, 0
v_accvgpr_write_b32 a244, 0
v_accvgpr_write_b32 a245, 0
v_accvgpr_write_b32 a246, 0
v_accvgpr_write_b32 a247, 0
v_accvgpr_write_b32 a248, 0
v_accvgpr_write_b32 a249, 0
v_accvgpr_write_b32 a250, 0
v_accvgpr_write_b32 a251, 0
v_accvgpr_write_b32 a252, 0
v_accvgpr_write_b32 a253, 0
v_accvgpr_write_b32 a254, 0
v_accvgpr_write_b32 a255, 0
s_waitcnt vmcnt(26)
s_barrier
ds_read_b64_tr_b16 v[128:129], v34 offset:0
ds_read_b64_tr_b16 v[130:131], v34 offset:2048
ds_read_b128 v[64:67], v24 offset:0
ds_read_b128 v[68:71], v24 offset:2048
ds_read_b128 v[72:75], v24 offset:4096
ds_read_b128 v[76:79], v24 offset:6144
ds_read_b128 v[80:83], v24 offset:8192
ds_read_b128 v[84:87], v24 offset:10240
ds_read_b128 v[88:91], v24 offset:12288
ds_read_b128 v[92:95], v24 offset:14336
ds_read_b64_tr_b16 v[132:133], v35 offset:0
ds_read_b64_tr_b16 v[134:135], v35 offset:2048
ds_read_b64_tr_b16 v[136:137], v36 offset:0
ds_read_b64_tr_b16 v[138:139], v36 offset:2048
ds_read_b64_tr_b16 v[140:141], v37 offset:0
ds_read_b64_tr_b16 v[142:143], v37 offset:2048
ds_read_b64_tr_b16 v[144:145], v38 offset:0
ds_read_b64_tr_b16 v[146:147], v38 offset:2048
ds_read_b64_tr_b16 v[148:149], v39 offset:0
ds_read_b64_tr_b16 v[150:151], v39 offset:2048
ds_read_b64_tr_b16 v[152:153], v40 offset:0
ds_read_b64_tr_b16 v[154:155], v40 offset:2048
ds_read_b64_tr_b16 v[156:157], v41 offset:0
ds_read_b64_tr_b16 v[158:159], v41 offset:2048
s_cmp_eq_u32 s69, 0
s_cbranch_scc0 L_k1_vjob
s_mov_b32 s42, 16
L_k1k_loop:
s_waitcnt lgkmcnt(0)
v_mfma_f32_16x16x32_bf16 a[0:3], v[128:131], v[64:67], a[0:3]
s_add_u32 s41, s41, 16384
s_and_b32 s41, s41, 0xffff
v_mfma_f32_16x16x32_bf16 a[32:35], v[128:131], v[68:71], a[32:35]
v_add_u32 v34, s41, v26
v_add_u32 v35, s41, v27
v_mfma_f32_16x16x32_bf16 a[64:67], v[128:131], v[72:75], a[64:67]
v_add_u32 v36, s41, v28
v_add_u32 v37, s41, v29
v_mfma_f32_16x16x32_bf16 a[96:99], v[128:131], v[76:79], a[96:99]
v_add_u32 v38, s41, v30
v_add_u32 v39, s41, v31
v_mfma_f32_16x16x32_bf16 a[128:131], v[128:131], v[80:83], a[128:131]
v_add_u32 v40, s41, v32
v_add_u32 v41, s41, v33
v_mfma_f32_16x16x32_bf16 a[160:163], v[128:131], v[84:87], a[160:163]
v_mfma_f32_16x16x32_bf16 a[192:195], v[128:131], v[88:91], a[192:195]
v_mfma_f32_16x16x32_bf16 a[224:227], v[128:131], v[92:95], a[224:227]
v_mfma_f32_16x16x32_bf16 a[4:7], v[132:135], v[64:67], a[4:7]
s_waitcnt vmcnt(21)
s_barrier
v_mfma_f32_16x16x32_bf16 a[36:39], v[132:135], v[68:71], a[36:39]
ds_read_b64_tr_b16 v[160:161], v34 offset:0
v_mfma_f32_16x16x32_bf16 a[68:71], v[132:135], v[72:75], a[68:71]
s_add_u32 m0, s39, s51
v_mfma_f32_16x16x32_bf16 a[100:103], v[132:135], v[76:79], a[100:103]
ds_read_b64_tr_b16 v[162:163], v34 offset:2048
v_mfma_f32_16x16x32_bf16 a[132:135], v[132:135], v[80:83], a[132:135]
buffer_load_dwordx4 v18, s[28:31], s37 offen offset:0 lds
v_mfma_f32_16x16x32_bf16 a[164:167], v[132:135], v[84:87], a[164:167]
ds_read_b128 v[96:99], v25 offset:0
v_mfma_f32_16x16x32_bf16 a[196:199], v[132:135], v[88:91], a[196:199]
v_mfma_f32_16x16x32_bf16 a[228:231], v[132:135], v[92:95], a[228:231]
ds_read_b128 v[100:103], v25 offset:2048
v_mfma_f32_16x16x32_bf16 a[8:11], v[136:139], v[64:67], a[8:11]
buffer_load_dwordx4 v19, s[28:31], s37 offen offset:1024 lds
v_mfma_f32_16x16x32_bf16 a[40:43], v[136:139], v[68:71], a[40:43]
ds_read_b128 v[104:107], v25 offset:4096
v_mfma_f32_16x16x32_bf16 a[72:75], v[136:139], v[72:75], a[72:75]
v_mfma_f32_16x16x32_bf16 a[104:107], v[136:139], v[76:79], a[104:107]
ds_read_b128 v[108:111], v25 offset:6144
v_mfma_f32_16x16x32_bf16 a[136:139], v[136:139], v[80:83], a[136:139]
v_mfma_f32_16x16x32_bf16 a[168:171], v[136:139], v[84:87], a[168:171]
ds_read_b128 v[112:115], v25 offset:8192
v_mfma_f32_16x16x32_bf16 a[200:203], v[136:139], v[88:91], a[200:203]
buffer_load_dwordx4 v20, s[28:31], s37 offen offset:2048 lds
v_mfma_f32_16x16x32_bf16 a[232:235], v[136:139], v[92:95], a[232:235]
ds_read_b128 v[116:119], v25 offset:10240
v_mfma_f32_16x16x32_bf16 a[12:15], v[140:143], v[64:67], a[12:15]
v_mfma_f32_16x16x32_bf16 a[44:47], v[140:143], v[68:71], a[44:47]
ds_read_b128 v[120:123], v25 offset:12288
v_mfma_f32_16x16x32_bf16 a[76:79], v[140:143], v[72:75], a[76:79]
buffer_load_dwordx4 v21, s[28:31], s37 offen offset:3072 lds
v_mfma_f32_16x16x32_bf16 a[108:111], v[140:143], v[76:79], a[108:111]
ds_read_b128 v[124:127], v25 offset:14336
v_mfma_f32_16x16x32_bf16 a[140:143], v[140:143], v[80:83], a[140:143]
s_add_u32 s39, s39, 16384
v_mfma_f32_16x16x32_bf16 a[172:175], v[140:143], v[84:87], a[172:175]
ds_read_b64_tr_b16 v[164:165], v35 offset:0
v_mfma_f32_16x16x32_bf16 a[204:207], v[140:143], v[88:91], a[204:207]
s_and_b32 s39, s39, 0xffff
v_mfma_f32_16x16x32_bf16 a[236:239], v[140:143], v[92:95], a[236:239]
ds_read_b64_tr_b16 v[166:167], v35 offset:2048
v_mfma_f32_16x16x32_bf16 a[16:19], v[144:147], v[64:67], a[16:19]
s_add_u32 s37, s37, 262144
v_mfma_f32_16x16x32_bf16 a[48:51], v[144:147], v[68:71], a[48:51]
ds_read_b64_tr_b16 v[168:169], v36 offset:0
v_mfma_f32_16x16x32_bf16 a[80:83], v[144:147], v[72:75], a[80:83]
s_add_u32 m0, s38, 4096
v_mfma_f32_16x16x32_bf16 a[112:115], v[144:147], v[76:79], a[112:115]
ds_read_b64_tr_b16 v[170:171], v36 offset:2048
v_mfma_f32_16x16x32_bf16 a[144:147], v[144:147], v[80:83], a[144:147]
buffer_load_dwordx4 v14, s[24:27], s36 offen offset:0 lds
v_mfma_f32_16x16x32_bf16 a[176:179], v[144:147], v[84:87], a[176:179]
ds_read_b64_tr_b16 v[172:173], v37 offset:0
v_mfma_f32_16x16x32_bf16 a[208:211], v[144:147], v[88:91], a[208:211]
buffer_load_dwordx4 v15, s[24:27], s36 offen offset:1024 lds
v_mfma_f32_16x16x32_bf16 a[240:243], v[144:147], v[92:95], a[240:243]
ds_read_b64_tr_b16 v[174:175], v37 offset:2048
v_mfma_f32_16x16x32_bf16 a[20:23], v[148:151], v[64:67], a[20:23]
v_mfma_f32_16x16x32_bf16 a[52:55], v[148:151], v[68:71], a[52:55]
ds_read_b64_tr_b16 v[176:177], v38 offset:0
v_mfma_f32_16x16x32_bf16 a[84:87], v[148:151], v[72:75], a[84:87]
buffer_load_dwordx4 v16, s[24:27], s36 offen offset:2048 lds
v_mfma_f32_16x16x32_bf16 a[116:119], v[148:151], v[76:79], a[116:119]
ds_read_b64_tr_b16 v[178:179], v38 offset:2048
v_mfma_f32_16x16x32_bf16 a[148:151], v[148:151], v[80:83], a[148:151]
v_mfma_f32_16x16x32_bf16 a[180:183], v[148:151], v[84:87], a[180:183]
ds_read_b64_tr_b16 v[180:181], v39 offset:0
v_mfma_f32_16x16x32_bf16 a[212:215], v[148:151], v[88:91], a[212:215]
buffer_load_dwordx4 v17, s[24:27], s36 offen offset:3072 lds
v_mfma_f32_16x16x32_bf16 a[244:247], v[148:151], v[92:95], a[244:247]
ds_read_b64_tr_b16 v[182:183], v39 offset:2048
v_mfma_f32_16x16x32_bf16 a[24:27], v[152:155], v[64:67], a[24:27]
buffer_load_dwordx4 v[48:51], v52, s[76:79], 0 offen
v_mfma_f32_16x16x32_bf16 a[56:59], v[152:155], v[68:71], a[56:59]
ds_read_b64_tr_b16 v[184:185], v40 offset:0
v_mfma_f32_16x16x32_bf16 a[88:91], v[152:155], v[72:75], a[88:91]
v_add_u32 v52, 1024, v52
v_mfma_f32_16x16x32_bf16 a[120:123], v[152:155], v[76:79], a[120:123]
ds_read_b64_tr_b16 v[186:187], v40 offset:2048
v_mfma_f32_16x16x32_bf16 a[152:155], v[152:155], v[80:83], a[152:155]
s_add_u32 s38, s38, 32768
v_mfma_f32_16x16x32_bf16 a[184:187], v[152:155], v[84:87], a[184:187]
ds_read_b64_tr_b16 v[188:189], v41 offset:0
v_mfma_f32_16x16x32_bf16 a[216:219], v[152:155], v[88:91], a[216:219]
s_cmp_ge_u32 s38, s52
v_mfma_f32_16x16x32_bf16 a[248:251], v[152:155], v[92:95], a[248:251]
ds_read_b64_tr_b16 v[190:191], v41 offset:2048
v_mfma_f32_16x16x32_bf16 a[28:31], v[156:159], v[64:67], a[28:31]
s_cselect_b32 s49, 98304, 0
v_mfma_f32_16x16x32_bf16 a[60:63], v[156:159], v[68:71], a[60:63]
s_sub_u32 s38, s38, s49
v_mfma_f32_16x16x32_bf16 a[92:95], v[156:159], v[72:75], a[92:95]
s_add_u32 s36, s36, 128
v_mfma_f32_16x16x32_bf16 a[124:127], v[156:159], v[76:79], a[124:127]
s_sub_u32 s44, s44, 1
v_mfma_f32_16x16x32_bf16 a[156:159], v[156:159], v[80:83], a[156:159]
s_cmp_gt_i32 s44, 0
v_mfma_f32_16x16x32_bf16 a[188:191], v[156:159], v[84:87], a[188:191]
s_cselect_b32 s26, s43, 0
v_mfma_f32_16x16x32_bf16 a[220:223], v[156:159], v[88:91], a[220:223]
v_mfma_f32_16x16x32_bf16 a[252:255], v[156:159], v[92:95], a[252:255]
s_waitcnt lgkmcnt(0)
v_mfma_f32_16x16x32_bf16 a[0:3], v[160:163], v[96:99], a[0:3]
s_add_u32 s41, s41, 16384
s_and_b32 s41, s41, 0xffff
v_mfma_f32_16x16x32_bf16 a[32:35], v[160:163], v[100:103], a[32:35]
v_add_u32 v34, s41, v26
v_add_u32 v35, s41, v27
v_mfma_f32_16x16x32_bf16 a[64:67], v[160:163], v[104:107], a[64:67]
v_add_u32 v36, s41, v28
v_add_u32 v37, s41, v29
v_mfma_f32_16x16x32_bf16 a[96:99], v[160:163], v[108:111], a[96:99]
v_add_u32 v38, s41, v30
v_add_u32 v39, s41, v31
v_mfma_f32_16x16x32_bf16 a[128:131], v[160:163], v[112:115], a[128:131]
v_add_u32 v40, s41, v32
v_add_u32 v41, s41, v33
v_mfma_f32_16x16x32_bf16 a[160:163], v[160:163], v[116:119], a[160:163]
s_add_u32 s40, s40, 32768
s_cmp_ge_u32 s40, 98304
v_mfma_f32_16x16x32_bf16 a[192:195], v[160:163], v[120:123], a[192:195]
s_cselect_b32 s40, 0, s40
v_add_u32 v24, s40, v22
v_mfma_f32_16x16x32_bf16 a[224:227], v[160:163], v[124:127], a[224:227]
v_add_u32 v25, s40, v23
v_mfma_f32_16x16x32_bf16 a[4:7], v[164:167], v[96:99], a[4:7]
s_waitcnt vmcnt(18)
s_barrier
v_mfma_f32_16x16x32_bf16 a[36:39], v[164:167], v[100:103], a[36:39]
ds_read_b64_tr_b16 v[128:129], v34 offset:0
v_mfma_f32_16x16x32_bf16 a[68:71], v[164:167], v[104:107], a[68:71]
s_add_u32 m0, s39, s51
v_mfma_f32_16x16x32_bf16 a[100:103], v[164:167], v[108:111], a[100:103]
ds_read_b64_tr_b16 v[130:131], v34 offset:2048
v_mfma_f32_16x16x32_bf16 a[132:135], v[164:167], v[112:115], a[132:135]
buffer_load_dwordx4 v18, s[28:31], s37 offen offset:0 lds
v_mfma_f32_16x16x32_bf16 a[164:167], v[164:167], v[116:119], a[164:167]
ds_read_b128 v[64:67], v24 offset:0
v_mfma_f32_16x16x32_bf16 a[196:199], v[164:167], v[120:123], a[196:199]
v_mfma_f32_16x16x32_bf16 a[228:231], v[164:167], v[124:127], a[228:231]
ds_read_b128 v[68:71], v24 offset:2048
v_mfma_f32_16x16x32_bf16 a[8:11], v[168:171], v[96:99], a[8:11]
buffer_load_dwordx4 v19, s[28:31], s37 offen offset:1024 lds
v_mfma_f32_16x16x32_bf16 a[40:43], v[168:171], v[100:103], a[40:43]
ds_read_b128 v[72:75], v24 offset:4096
v_mfma_f32_16x16x32_bf16 a[72:75], v[168:171], v[104:107], a[72:75]
v_mfma_f32_16x16x32_bf16 a[104:107], v[168:171], v[108:111], a[104:107]
ds_read_b128 v[76:79], v24 offset:6144
v_mfma_f32_16x16x32_bf16 a[136:139], v[168:171], v[112:115], a[136:139]
v_mfma_f32_16x16x32_bf16 a[168:171], v[168:171], v[116:119], a[168:171]
ds_read_b128 v[80:83], v24 offset:8192
v_mfma_f32_16x16x32_bf16 a[200:203], v[168:171], v[120:123], a[200:203]
buffer_load_dwordx4 v20, s[28:31], s37 offen offset:2048 lds
v_mfma_f32_16x16x32_bf16 a[232:235], v[168:171], v[124:127], a[232:235]
ds_read_b128 v[84:87], v24 offset:10240
v_mfma_f32_16x16x32_bf16 a[12:15], v[172:175], v[96:99], a[12:15]
v_mfma_f32_16x16x32_bf16 a[44:47], v[172:175], v[100:103], a[44:47]
ds_read_b128 v[88:91], v24 offset:12288
v_mfma_f32_16x16x32_bf16 a[76:79], v[172:175], v[104:107], a[76:79]
buffer_load_dwordx4 v21, s[28:31], s37 offen offset:3072 lds
v_mfma_f32_16x16x32_bf16 a[108:111], v[172:175], v[108:111], a[108:111]
ds_read_b128 v[92:95], v24 offset:14336
v_mfma_f32_16x16x32_bf16 a[140:143], v[172:175], v[112:115], a[140:143]
s_add_u32 s39, s39, 16384
v_mfma_f32_16x16x32_bf16 a[172:175], v[172:175], v[116:119], a[172:175]
ds_read_b64_tr_b16 v[132:133], v35 offset:0
v_mfma_f32_16x16x32_bf16 a[204:207], v[172:175], v[120:123], a[204:207]
s_and_b32 s39, s39, 0xffff
v_mfma_f32_16x16x32_bf16 a[236:239], v[172:175], v[124:127], a[236:239]
ds_read_b64_tr_b16 v[134:135], v35 offset:2048
v_mfma_f32_16x16x32_bf16 a[16:19], v[176:179], v[96:99], a[16:19]
s_add_u32 s37, s37, 262144
v_mfma_f32_16x16x32_bf16 a[48:51], v[176:179], v[100:103], a[48:51]
ds_read_b64_tr_b16 v[136:137], v36 offset:0
v_mfma_f32_16x16x32_bf16 a[80:83], v[176:179], v[104:107], a[80:83]
s_add_u32 m0, s38, 0
v_mfma_f32_16x16x32_bf16 a[112:115], v[176:179], v[108:111], a[112:115]
ds_read_b64_tr_b16 v[138:139], v36 offset:2048
v_mfma_f32_16x16x32_bf16 a[144:147], v[176:179], v[112:115], a[144:147]
buffer_load_dwordx4 v10, s[24:27], s36 offen offset:0 lds
v_mfma_f32_16x16x32_bf16 a[176:179], v[176:179], v[116:119], a[176:179]
ds_read_b64_tr_b16 v[140:141], v37 offset:0
v_mfma_f32_16x16x32_bf16 a[208:211], v[176:179], v[120:123], a[208:211]
buffer_load_dwordx4 v11, s[24:27], s36 offen offset:1024 lds
v_mfma_f32_16x16x32_bf16 a[240:243], v[176:179], v[124:127], a[240:243]
ds_read_b64_tr_b16 v[142:143], v37 offset:2048
v_mfma_f32_16x16x32_bf16 a[20:23], v[180:183], v[96:99], a[20:23]
v_mfma_f32_16x16x32_bf16 a[52:55], v[180:183], v[100:103], a[52:55]
ds_read_b64_tr_b16 v[144:145], v38 offset:0
v_mfma_f32_16x16x32_bf16 a[84:87], v[180:183], v[104:107], a[84:87]
buffer_load_dwordx4 v12, s[24:27], s36 offen offset:2048 lds
v_mfma_f32_16x16x32_bf16 a[116:119], v[180:183], v[108:111], a[116:119]
ds_read_b64_tr_b16 v[146:147], v38 offset:2048
v_mfma_f32_16x16x32_bf16 a[148:151], v[180:183], v[112:115], a[148:151]
v_mfma_f32_16x16x32_bf16 a[180:183], v[180:183], v[116:119], a[180:183]
ds_read_b64_tr_b16 v[148:149], v39 offset:0
v_mfma_f32_16x16x32_bf16 a[212:215], v[180:183], v[120:123], a[212:215]
buffer_load_dwordx4 v13, s[24:27], s36 offen offset:3072 lds
v_mfma_f32_16x16x32_bf16 a[244:247], v[180:183], v[124:127], a[244:247]
ds_read_b64_tr_b16 v[150:151], v39 offset:2048
v_mfma_f32_16x16x32_bf16 a[24:27], v[184:187], v[96:99], a[24:27]
v_mfma_f32_16x16x32_bf16 a[56:59], v[184:187], v[100:103], a[56:59]
ds_read_b64_tr_b16 v[152:153], v40 offset:0
v_mfma_f32_16x16x32_bf16 a[88:91], v[184:187], v[104:107], a[88:91]
v_mfma_f32_16x16x32_bf16 a[120:123], v[184:187], v[108:111], a[120:123]
ds_read_b64_tr_b16 v[154:155], v40 offset:2048
v_mfma_f32_16x16x32_bf16 a[152:155], v[184:187], v[112:115], a[152:155]
v_mfma_f32_16x16x32_bf16 a[184:187], v[184:187], v[116:119], a[184:187]
ds_read_b64_tr_b16 v[156:157], v41 offset:0
v_mfma_f32_16x16x32_bf16 a[216:219], v[184:187], v[120:123], a[216:219]
v_mfma_f32_16x16x32_bf16 a[248:251], v[184:187], v[124:127], a[248:251]
ds_read_b64_tr_b16 v[158:159], v41 offset:2048
v_mfma_f32_16x16x32_bf16 a[28:31], v[188:191], v[96:99], a[28:31]
v_mfma_f32_16x16x32_bf16 a[60:63], v[188:191], v[100:103], a[60:63]
v_mfma_f32_16x16x32_bf16 a[92:95], v[188:191], v[104:107], a[92:95]
v_mfma_f32_16x16x32_bf16 a[124:127], v[188:191], v[108:111], a[124:127]
v_mfma_f32_16x16x32_bf16 a[156:159], v[188:191], v[112:115], a[156:159]
v_mfma_f32_16x16x32_bf16 a[188:191], v[188:191], v[116:119], a[188:191]
v_mfma_f32_16x16x32_bf16 a[220:223], v[188:191], v[120:123], a[220:223]
v_mfma_f32_16x16x32_bf16 a[252:255], v[188:191], v[124:127], a[252:255]
s_sub_u32 s42, s42, 1
s_cmp_lg_u32 s42, 0
s_cbranch_scc1 L_k1k_loop
s_branch L_k1_done
L_k1_vjob:
s_mov_b32 s42, 16
L_k1v_loop:
s_waitcnt lgkmcnt(0)
v_mfma_f32_16x16x32_bf16 a[0:3], v[64:67], v[128:131], a[0:3]
s_add_u32 s41, s41, 16384
s_and_b32 s41, s41, 0xffff
v_mfma_f32_16x16x32_bf16 a[4:7], v[68:71], v[128:131], a[4:7]
v_add_u32 v34, s41, v26
v_add_u32 v35, s41, v27
v_mfma_f32_16x16x32_bf16 a[64:67], v[72:75], v[128:131], a[64:67]
v_add_u32 v36, s41, v28
v_add_u32 v37, s41, v29
v_mfma_f32_16x16x32_bf16 a[68:71], v[76:79], v[128:131], a[68:71]
v_add_u32 v38, s41, v30
v_add_u32 v39, s41, v31
v_mfma_f32_16x16x32_bf16 a[128:131], v[80:83], v[128:131], a[128:131]
v_add_u32 v40, s41, v32
v_add_u32 v41, s41, v33
v_mfma_f32_16x16x32_bf16 a[132:135], v[84:87], v[128:131], a[132:135]
v_mfma_f32_16x16x32_bf16 a[192:195], v[88:91], v[128:131], a[192:195]
v_mfma_f32_16x16x32_bf16 a[196:199], v[92:95], v[128:131], a[196:199]
v_mfma_f32_16x16x32_bf16 a[8:11], v[64:67], v[132:135], a[8:11]
s_waitcnt vmcnt(21)
s_barrier
v_mfma_f32_16x16x32_bf16 a[12:15], v[68:71], v[132:135], a[12:15]
ds_read_b64_tr_b16 v[160:161], v34 offset:0
v_mfma_f32_16x16x32_bf16 a[72:75], v[72:75], v[132:135], a[72:75]
s_add_u32 m0, s39, s51
v_mfma_f32_16x16x32_bf16 a[76:79], v[76:79], v[132:135], a[76:79]
ds_read_b64_tr_b16 v[162:163], v34 offset:2048
v_mfma_f32_16x16x32_bf16 a[136:139], v[80:83], v[132:135], a[136:139]
buffer_load_dwordx4 v18, s[28:31], s37 offen offset:0 lds
v_mfma_f32_16x16x32_bf16 a[140:143], v[84:87], v[132:135], a[140:143]
ds_read_b128 v[96:99], v25 offset:0
v_mfma_f32_16x16x32_bf16 a[200:203], v[88:91], v[132:135], a[200:203]
v_mfma_f32_16x16x32_bf16 a[204:207], v[92:95], v[132:135], a[204:207]
ds_read_b128 v[100:103], v25 offset:2048
v_mfma_f32_16x16x32_bf16 a[16:19], v[64:67], v[136:139], a[16:19]
buffer_load_dwordx4 v19, s[28:31], s37 offen offset:1024 lds
v_mfma_f32_16x16x32_bf16 a[20:23], v[68:71], v[136:139], a[20:23]
ds_read_b128 v[104:107], v25 offset:4096
v_mfma_f32_16x16x32_bf16 a[80:83], v[72:75], v[136:139], a[80:83]
v_mfma_f32_16x16x32_bf16 a[84:87], v[76:79], v[136:139], a[84:87]
ds_read_b128 v[108:111], v25 offset:6144
v_mfma_f32_16x16x32_bf16 a[144:147], v[80:83], v[136:139], a[144:147]
v_mfma_f32_16x16x32_bf16 a[148:151], v[84:87], v[136:139], a[148:151]
ds_read_b128 v[112:115], v25 offset:8192
v_mfma_f32_16x16x32_bf16 a[208:211], v[88:91], v[136:139], a[208:211]
buffer_load_dwordx4 v20, s[28:31], s37 offen offset:2048 lds
v_mfma_f32_16x16x32_bf16 a[212:215], v[92:95], v[136:139], a[212:215]
ds_read_b128 v[116:119], v25 offset:10240
v_mfma_f32_16x16x32_bf16 a[24:27], v[64:67], v[140:143], a[24:27]
v_mfma_f32_16x16x32_bf16 a[28:31], v[68:71], v[140:143], a[28:31]
ds_read_b128 v[120:123], v25 offset:12288
v_mfma_f32_16x16x32_bf16 a[88:91], v[72:75], v[140:143], a[88:91]
buffer_load_dwordx4 v21, s[28:31], s37 offen offset:3072 lds
v_mfma_f32_16x16x32_bf16 a[92:95], v[76:79], v[140:143], a[92:95]
ds_read_b128 v[124:127], v25 offset:14336
v_mfma_f32_16x16x32_bf16 a[152:155], v[80:83], v[140:143], a[152:155]
s_add_u32 s39, s39, 16384
v_mfma_f32_16x16x32_bf16 a[156:159], v[84:87], v[140:143], a[156:159]
ds_read_b64_tr_b16 v[164:165], v35 offset:0
v_mfma_f32_16x16x32_bf16 a[216:219], v[88:91], v[140:143], a[216:219]
s_and_b32 s39, s39, 0xffff
v_mfma_f32_16x16x32_bf16 a[220:223], v[92:95], v[140:143], a[220:223]
ds_read_b64_tr_b16 v[166:167], v35 offset:2048
v_mfma_f32_16x16x32_bf16 a[32:35], v[64:67], v[144:147], a[32:35]
s_add_u32 s37, s37, 262144
v_mfma_f32_16x16x32_bf16 a[36:39], v[68:71], v[144:147], a[36:39]
ds_read_b64_tr_b16 v[168:169], v36 offset:0
v_mfma_f32_16x16x32_bf16 a[96:99], v[72:75], v[144:147], a[96:99]
s_add_u32 m0, s38, 4096
v_mfma_f32_16x16x32_bf16 a[100:103], v[76:79], v[144:147], a[100:103]
ds_read_b64_tr_b16 v[170:171], v36 offset:2048
v_mfma_f32_16x16x32_bf16 a[160:163], v[80:83], v[144:147], a[160:163]
buffer_load_dwordx4 v14, s[24:27], s36 offen offset:0 lds
v_mfma_f32_16x16x32_bf16 a[164:167], v[84:87], v[144:147], a[164:167]
ds_read_b64_tr_b16 v[172:173], v37 offset:0
v_mfma_f32_16x16x32_bf16 a[224:227], v[88:91], v[144:147], a[224:227]
buffer_load_dwordx4 v15, s[24:27], s36 offen offset:1024 lds
v_mfma_f32_16x16x32_bf16 a[228:231], v[92:95], v[144:147], a[228:231]
ds_read_b64_tr_b16 v[174:175], v37 offset:2048
v_mfma_f32_16x16x32_bf16 a[40:43], v[64:67], v[148:151], a[40:43]
v_mfma_f32_16x16x32_bf16 a[44:47], v[68:71], v[148:151], a[44:47]
ds_read_b64_tr_b16 v[176:177], v38 offset:0
v_mfma_f32_16x16x32_bf16 a[104:107], v[72:75], v[148:151], a[104:107]
buffer_load_dwordx4 v16, s[24:27], s36 offen offset:2048 lds
v_mfma_f32_16x16x32_bf16 a[108:111], v[76:79], v[148:151], a[108:111]
ds_read_b64_tr_b16 v[178:179], v38 offset:2048
v_mfma_f32_16x16x32_bf16 a[168:171], v[80:83], v[148:151], a[168:171]
v_mfma_f32_16x16x32_bf16 a[172:175], v[84:87], v[148:151], a[172:175]
ds_read_b64_tr_b16 v[180:181], v39 offset:0
v_mfma_f32_16x16x32_bf16 a[232:235], v[88:91], v[148:151], a[232:235]
buffer_load_dwordx4 v17, s[24:27], s36 offen offset:3072 lds
v_mfma_f32_16x16x32_bf16 a[236:239], v[92:95], v[148:151], a[236:239]
ds_read_b64_tr_b16 v[182:183], v39 offset:2048
v_mfma_f32_16x16x32_bf16 a[48:51], v[64:67], v[152:155], a[48:51]
buffer_load_dwordx4 v[48:51], v52, s[76:79], 0 offen
v_mfma_f32_16x16x32_bf16 a[52:55], v[68:71], v[152:155], a[52:55]
ds_read_b64_tr_b16 v[184:185], v40 offset:0
v_mfma_f32_16x16x32_bf16 a[112:115], v[72:75], v[152:155], a[112:115]
v_add_u32 v52, 1024, v52
v_mfma_f32_16x16x32_bf16 a[116:119], v[76:79], v[152:155], a[116:119]
ds_read_b64_tr_b16 v[186:187], v40 offset:2048
v_mfma_f32_16x16x32_bf16 a[176:179], v[80:83], v[152:155], a[176:179]
s_add_u32 s38, s38, 32768
v_mfma_f32_16x16x32_bf16 a[180:183], v[84:87], v[152:155], a[180:183]
ds_read_b64_tr_b16 v[188:189], v41 offset:0
v_mfma_f32_16x16x32_bf16 a[240:243], v[88:91], v[152:155], a[240:243]
s_cmp_ge_u32 s38, s52
v_mfma_f32_16x16x32_bf16 a[244:247], v[92:95], v[152:155], a[244:247]
ds_read_b64_tr_b16 v[190:191], v41 offset:2048
v_mfma_f32_16x16x32_bf16 a[56:59], v[64:67], v[156:159], a[56:59]
s_cselect_b32 s49, 98304, 0
v_mfma_f32_16x16x32_bf16 a[60:63], v[68:71], v[156:159], a[60:63]
s_sub_u32 s38, s38, s49
v_mfma_f32_16x16x32_bf16 a[120:123], v[72:75], v[156:159], a[120:123]
s_add_u32 s36, s36, 128
v_mfma_f32_16x16x32_bf16 a[124:127], v[76:79], v[156:159], a[124:127]
s_sub_u32 s44, s44, 1
v_mfma_f32_16x16x32_bf16 a[184:187], v[80:83], v[156:159], a[184:187]
s_cmp_gt_i32 s44, 0
v_mfma_f32_16x16x32_bf16 a[188:191], v[84:87], v[156:159], a[188:191]
s_cselect_b32 s26, s43, 0
v_mfma_f32_16x16x32_bf16 a[248:251], v[88:91], v[156:159], a[248:251]
v_mfma_f32_16x16x32_bf16 a[252:255], v[92:95], v[156:159], a[252:255]
s_waitcnt lgkmcnt(0)
v_mfma_f32_16x16x32_bf16 a[0:3], v[96:99], v[160:163], a[0:3]
s_add_u32 s41, s41, 16384
s_and_b32 s41, s41, 0xffff
v_mfma_f32_16x16x32_bf16 a[4:7], v[100:103], v[160:163], a[4:7]
v_add_u32 v34, s41, v26
v_add_u32 v35, s41, v27
v_mfma_f32_16x16x32_bf16 a[64:67], v[104:107], v[160:163], a[64:67]
v_add_u32 v36, s41, v28
v_add_u32 v37, s41, v29
v_mfma_f32_16x16x32_bf16 a[68:71], v[108:111], v[160:163], a[68:71]
v_add_u32 v38, s41, v30
v_add_u32 v39, s41, v31
v_mfma_f32_16x16x32_bf16 a[128:131], v[112:115], v[160:163], a[128:131]
v_add_u32 v40, s41, v32
v_add_u32 v41, s41, v33
v_mfma_f32_16x16x32_bf16 a[132:135], v[116:119], v[160:163], a[132:135]
s_add_u32 s40, s40, 32768
s_cmp_ge_u32 s40, 98304
v_mfma_f32_16x16x32_bf16 a[192:195], v[120:123], v[160:163], a[192:195]
s_cselect_b32 s40, 0, s40
v_add_u32 v24, s40, v22
v_mfma_f32_16x16x32_bf16 a[196:199], v[124:127], v[160:163], a[196:199]
v_add_u32 v25, s40, v23
v_mfma_f32_16x16x32_bf16 a[8:11], v[96:99], v[164:167], a[8:11]
s_waitcnt vmcnt(18)
s_barrier
v_mfma_f32_16x16x32_bf16 a[12:15], v[100:103], v[164:167], a[12:15]
ds_read_b64_tr_b16 v[128:129], v34 offset:0
v_mfma_f32_16x16x32_bf16 a[72:75], v[104:107], v[164:167], a[72:75]
s_add_u32 m0, s39, s51
v_mfma_f32_16x16x32_bf16 a[76:79], v[108:111], v[164:167], a[76:79]
ds_read_b64_tr_b16 v[130:131], v34 offset:2048
v_mfma_f32_16x16x32_bf16 a[136:139], v[112:115], v[164:167], a[136:139]
buffer_load_dwordx4 v18, s[28:31], s37 offen offset:0 lds
v_mfma_f32_16x16x32_bf16 a[140:143], v[116:119], v[164:167], a[140:143]
ds_read_b128 v[64:67], v24 offset:0
v_mfma_f32_16x16x32_bf16 a[200:203], v[120:123], v[164:167], a[200:203]
v_mfma_f32_16x16x32_bf16 a[204:207], v[124:127], v[164:167], a[204:207]
ds_read_b128 v[68:71], v24 offset:2048
v_mfma_f32_16x16x32_bf16 a[16:19], v[96:99], v[168:171], a[16:19]
buffer_load_dwordx4 v19, s[28:31], s37 offen offset:1024 lds
v_mfma_f32_16x16x32_bf16 a[20:23], v[100:103], v[168:171], a[20:23]
ds_read_b128 v[72:75], v24 offset:4096
v_mfma_f32_16x16x32_bf16 a[80:83], v[104:107], v[168:171], a[80:83]
v_mfma_f32_16x16x32_bf16 a[84:87], v[108:111], v[168:171], a[84:87]
ds_read_b128 v[76:79], v24 offset:6144
v_mfma_f32_16x16x32_bf16 a[144:147], v[112:115], v[168:171], a[144:147]
v_mfma_f32_16x16x32_bf16 a[148:151], v[116:119], v[168:171], a[148:151]
ds_read_b128 v[80:83], v24 offset:8192
v_mfma_f32_16x16x32_bf16 a[208:211], v[120:123], v[168:171], a[208:211]
buffer_load_dwordx4 v20, s[28:31], s37 offen offset:2048 lds
v_mfma_f32_16x16x32_bf16 a[212:215], v[124:127], v[168:171], a[212:215]
ds_read_b128 v[84:87], v24 offset:10240
v_mfma_f32_16x16x32_bf16 a[24:27], v[96:99], v[172:175], a[24:27]
v_mfma_f32_16x16x32_bf16 a[28:31], v[100:103], v[172:175], a[28:31]
ds_read_b128 v[88:91], v24 offset:12288
v_mfma_f32_16x16x32_bf16 a[88:91], v[104:107], v[172:175], a[88:91]
buffer_load_dwordx4 v21, s[28:31], s37 offen offset:3072 lds
v_mfma_f32_16x16x32_bf16 a[92:95], v[108:111], v[172:175], a[92:95]
ds_read_b128 v[92:95], v24 offset:14336
v_mfma_f32_16x16x32_bf16 a[152:155], v[112:115], v[172:175], a[152:155]
s_add_u32 s39, s39, 16384
v_mfma_f32_16x16x32_bf16 a[156:159], v[116:119], v[172:175], a[156:159]
ds_read_b64_tr_b16 v[132:133], v35 offset:0
v_mfma_f32_16x16x32_bf16 a[216:219], v[120:123], v[172:175], a[216:219]
s_and_b32 s39, s39, 0xffff
v_mfma_f32_16x16x32_bf16 a[220:223], v[124:127], v[172:175], a[220:223]
ds_read_b64_tr_b16 v[134:135], v35 offset:2048
v_mfma_f32_16x16x32_bf16 a[32:35], v[96:99], v[176:179], a[32:35]
s_add_u32 s37, s37, 262144
v_mfma_f32_16x16x32_bf16 a[36:39], v[100:103], v[176:179], a[36:39]
ds_read_b64_tr_b16 v[136:137], v36 offset:0
v_mfma_f32_16x16x32_bf16 a[96:99], v[104:107], v[176:179], a[96:99]
s_add_u32 m0, s38, 0
v_mfma_f32_16x16x32_bf16 a[100:103], v[108:111], v[176:179], a[100:103]
ds_read_b64_tr_b16 v[138:139], v36 offset:2048
v_mfma_f32_16x16x32_bf16 a[160:163], v[112:115], v[176:179], a[160:163]
buffer_load_dwordx4 v10, s[24:27], s36 offen offset:0 lds
v_mfma_f32_16x16x32_bf16 a[164:167], v[116:119], v[176:179], a[164:167]
ds_read_b64_tr_b16 v[140:141], v37 offset:0
v_mfma_f32_16x16x32_bf16 a[224:227], v[120:123], v[176:179], a[224:227]
buffer_load_dwordx4 v11, s[24:27], s36 offen offset:1024 lds
v_mfma_f32_16x16x32_bf16 a[228:231], v[124:127], v[176:179], a[228:231]
ds_read_b64_tr_b16 v[142:143], v37 offset:2048
v_mfma_f32_16x16x32_bf16 a[40:43], v[96:99], v[180:183], a[40:43]
v_mfma_f32_16x16x32_bf16 a[44:47], v[100:103], v[180:183], a[44:47]
ds_read_b64_tr_b16 v[144:145], v38 offset:0
v_mfma_f32_16x16x32_bf16 a[104:107], v[104:107], v[180:183], a[104:107]
buffer_load_dwordx4 v12, s[24:27], s36 offen offset:2048 lds
v_mfma_f32_16x16x32_bf16 a[108:111], v[108:111], v[180:183], a[108:111]
ds_read_b64_tr_b16 v[146:147], v38 offset:2048
v_mfma_f32_16x16x32_bf16 a[168:171], v[112:115], v[180:183], a[168:171]
v_mfma_f32_16x16x32_bf16 a[172:175], v[116:119], v[180:183], a[172:175]
ds_read_b64_tr_b16 v[148:149], v39 offset:0
v_mfma_f32_16x16x32_bf16 a[232:235], v[120:123], v[180:183], a[232:235]
buffer_load_dwordx4 v13, s[24:27], s36 offen offset:3072 lds
v_mfma_f32_16x16x32_bf16 a[236:239], v[124:127], v[180:183], a[236:239]
ds_read_b64_tr_b16 v[150:151], v39 offset:2048
v_mfma_f32_16x16x32_bf16 a[48:51], v[96:99], v[184:187], a[48:51]
v_mfma_f32_16x16x32_bf16 a[52:55], v[100:103], v[184:187], a[52:55]
ds_read_b64_tr_b16 v[152:153], v40 offset:0
v_mfma_f32_16x16x32_bf16 a[112:115], v[104:107], v[184:187], a[112:115]
v_mfma_f32_16x16x32_bf16 a[116:119], v[108:111], v[184:187], a[116:119]
ds_read_b64_tr_b16 v[154:155], v40 offset:2048
v_mfma_f32_16x16x32_bf16 a[176:179], v[112:115], v[184:187], a[176:179]
v_mfma_f32_16x16x32_bf16 a[180:183], v[116:119], v[184:187], a[180:183]
ds_read_b64_tr_b16 v[156:157], v41 offset:0
v_mfma_f32_16x16x32_bf16 a[240:243], v[120:123], v[184:187], a[240:243]
v_mfma_f32_16x16x32_bf16 a[244:247], v[124:127], v[184:187], a[244:247]
ds_read_b64_tr_b16 v[158:159], v41 offset:2048
v_mfma_f32_16x16x32_bf16 a[56:59], v[96:99], v[188:191], a[56:59]
v_mfma_f32_16x16x32_bf16 a[60:63], v[100:103], v[188:191], a[60:63]
v_mfma_f32_16x16x32_bf16 a[120:123], v[104:107], v[188:191], a[120:123]
v_mfma_f32_16x16x32_bf16 a[124:127], v[108:111], v[188:191], a[124:127]
v_mfma_f32_16x16x32_bf16 a[184:187], v[112:115], v[188:191], a[184:187]
v_mfma_f32_16x16x32_bf16 a[188:191], v[116:119], v[188:191], a[188:191]
v_mfma_f32_16x16x32_bf16 a[248:251], v[120:123], v[188:191], a[248:251]
v_mfma_f32_16x16x32_bf16 a[252:255], v[124:127], v[188:191], a[252:255]
s_sub_u32 s42, s42, 1
s_cmp_lg_u32 s42, 0
s_cbranch_scc1 L_k1v_loop
L_k1_done:
s_waitcnt vmcnt(0) lgkmcnt(0)
s_barrier
s_mov_b64 s[64:65], %3
s_mov_b32 s66, %4
s_mov_b32 s67, %6
s_mov_b64 s[76:77], %7
s_mov_b64 s[80:81], %8
v_and_b32 v30, 63, %5
v_lshrrev_b32 v27, 6, %5
v_lshlrev_b32 v14, 4, v30
v_readfirstlane_b32 s49, v27
s_and_b32 s50, s66, 7
s_lshr_b32 s51, s66, 3
s_lshl_b32 s50, s50, 1
s_and_b32 s52, s51, 1
s_add_u32 s68, s50, s52
s_lshr_b32 s51, s51, 1
s_and_b32 s52, s51, 7
s_lshl_b32 s34, s68, 3
s_add_u32 s34, s34, s52
s_lshl_b32 s34, s34, 2
s_add_u32 s34, s34, s49
s_lshl_b32 s50, s52, 1
s_sub_i32 s75, 12, s50
s_max_i32 s75, s75, 0
s_cmp_lg_u32 s75, 0
s_cselect_b32 s53, 1, 0
s_sub_u32 s50, 15, s52
s_cmp_lt_u32 s51, 8
s_cselect_b32 s69, s50, s52
s_cselect_b32 s70, s75, 0
s_cselect_b32 s35, s53, 0
s_cselect_b32 s50, 1, 3
s_mul_i32 s74, s53, s50
s_lshl_b32 s71, s69, 1
s_add_u32 s71, s71, 2
s_sub_u32 s71, s71, s70
s_lshr_b32 s45, s49, 1
s_and_b32 s46, s49, 1
s_mul_i32 s50, s69, 114688
s_add_u32 s76, s76, s50
s_addc_u32 s77, s77, 0
s_and_b32 s77, s77, 0xffff
s_mov_b32 s61, 114688
s_mov_b32 s78, s61
s_mov_b32 s79, 0x20000
s_lshl_b32 s50, s68, 9
s_add_u32 s80, s80, s50
s_addc_u32 s81, s81, 0
s_and_b32 s81, s81, 0xffff
s_sub_u32 s82, 3670016, s50
s_mov_b32 s83, 0x20000
v_lshrrev_b32 v32, 3, v30
v_lshrrev_b32 v33, 4, v30
v_and_b32 v34, 7, v30
v_xor_b32 v34, v34, v33
v_xor_b32 v35, 4, v34
s_mul_i32 s50, s49, 28672
v_mul_u32_u24 v32, 896, v32
v_add_u32 v32, s50, v32
v_lshl_add_u32 v40, v34, 4, v32
v_lshl_add_u32 v41, v35, 4, v32
v_add_u32 v41, 6144, v41
v_lshl_add_u32 v42, v34, 4, v32
v_add_u32 v42, 12288, v42
v_lshl_add_u32 v43, v35, 4, v32
v_add_u32 v43, 18432, v43
v_lshrrev_b32 v32, 5, v30
v_and_b32 v33, 31, v30
v_lshlrev_b32 v34, 2, v32
v_xor_b32 v33, v33, v34
s_lshl_b32 s50, s46, 1
v_xor_b32 v33, s50, v33
v_xor_b32 v34, 8, v33
s_lshl_b32 s50, s49, 16
v_lshl_add_u32 v32, v32, 13, s50
v_lshl_add_u32 v44, v33, 4, v32
v_lshl_add_u32 v45, v34, 4, v32
v_add_u32 v45, 15360, v45
v_lshl_add_u32 v46, v33, 4, v32
v_add_u32 v46, 30720, v46
v_lshl_add_u32 v47, v34, 4, v32
v_add_u32 v47, 46080, v47
s_mov_b32 s54, 0
s_mov_b32 s55, 0
s_lshl_b32 s56, s49, 12
s_add_u32 s56, s56, s67
s_add_u32 s56, s56, 32768
s_mov_b32 s57, 0
s_mov_b32 s58, 0
s_mov_b32 s59, 0
s_mov_b32 s62, 7
s_add_u32 s44, s67, 81920
s_lshl_b32 s63, s49, 12
s_add_u32 s63, s63, s44
s_mov_b32 m0, s56
s_nop 0
buffer_load_dwordx4 v40, s[76:79], s54 offen offset:0 lds
buffer_load_dwordx4 v41, s[76:79], s54 offen offset:1024 lds
s_add_u32 m0, s57, s63
s_nop 0
buffer_load_dwordx4 v44, s[80:83], s55 offen offset:0 lds
buffer_load_dwordx4 v45, s[80:83], s55 offen offset:1024 lds
buffer_load_dwordx4 v46, s[80:83], s55 offen offset:2048 lds
buffer_load_dwordx4 v47, s[80:83], s55 offen offset:3072 lds
s_add_u32 s57, s57, 16384
s_and_b32 s57, s57, 0xffff
s_add_u32 s55, s55, 262144
s_mov_b32 m0, s56
s_nop 0
buffer_load_dwordx4 v42, s[76:79], s54 offen offset:2048 lds
buffer_load_dwordx4 v43, s[76:79], s54 offen offset:3072 lds
s_add_u32 s56, s56, 16384
s_cmp_ge_u32 s56, s44
s_cselect_b32 s50, 49152, 0
s_sub_u32 s56, s56, s50
s_add_u32 s54, s54, 128
s_sub_u32 s62, s62, 1
s_cmp_gt_i32 s62, 0
s_cselect_b32 s78, s61, 0
s_add_u32 m0, s57, s63
s_nop 0
buffer_load_dwordx4 v44, s[80:83], s55 offen offset:0 lds
buffer_load_dwordx4 v45, s[80:83], s55 offen offset:1024 lds
buffer_load_dwordx4 v46, s[80:83], s55 offen offset:2048 lds
buffer_load_dwordx4 v47, s[80:83], s55 offen offset:3072 lds
s_add_u32 s57, s57, 16384
s_and_b32 s57, s57, 0xffff
s_add_u32 s55, s55, 262144
s_mov_b32 m0, s56
s_nop 0
buffer_load_dwordx4 v40, s[76:79], s54 offen offset:0 lds
buffer_load_dwordx4 v41, s[76:79], s54 offen offset:1024 lds
s_add_u32 m0, s57, s63
s_nop 0
buffer_load_dwordx4 v44, s[80:83], s55 offen offset:0 lds
buffer_load_dwordx4 v45, s[80:83], s55 offen offset:1024 lds
buffer_load_dwordx4 v46, s[80:83], s55 offen offset:2048 lds
buffer_load_dwordx4 v47, s[80:83], s55 offen offset:3072 lds
s_add_u32 s57, s57, 16384
s_and_b32 s57, s57, 0xffff
s_add_u32 s55, s55, 262144
s_mov_b32 m0, s56
s_nop 0
buffer_load_dwordx4 v42, s[76:79], s54 offen offset:2048 lds
buffer_load_dwordx4 v43, s[76:79], s54 offen offset:3072 lds
s_add_u32 s56, s56, 16384
s_cmp_ge_u32 s56, s44
s_cselect_b32 s50, 49152, 0
s_sub_u32 s56, s56, s50
s_add_u32 s54, s54, 128
s_sub_u32 s62, s62, 1
s_cmp_gt_i32 s62, 0
s_cselect_b32 s78, s61, 0
s_add_u32 m0, s57, s63
s_nop 0
buffer_load_dwordx4 v44, s[80:83], s55 offen offset:0 lds
buffer_load_dwordx4 v45, s[80:83], s55 offen offset:1024 lds
buffer_load_dwordx4 v46, s[80:83], s55 offen offset:2048 lds
buffer_load_dwordx4 v47, s[80:83], s55 offen offset:3072 lds
s_add_u32 s57, s57, 16384
s_and_b32 s57, s57, 0xffff
s_add_u32 s55, s55, 262144
s_mov_b32 m0, s56
s_nop 0
buffer_load_dwordx4 v40, s[76:79], s54 offen offset:0 lds
buffer_load_dwordx4 v41, s[76:79], s54 offen offset:1024 lds
s_mov_b64 s[28:29], %12
s_and_b32 s29, s29, 0xffff
s_mov_b32 s30, 3670016
s_mov_b32 s31, 0x20000
s_and_b32 s50, s66, 127
s_lshr_b32 s51, s66, 7
s_lshl_b32 s51, s51, 2
s_add_u32 s51, s51, s49
s_min_u32 s51, s51, 6
s_lshl_b32 s52, s51, 7
s_add_u32 s52, s52, s50
s_lshl_b32 s52, s52, 12
s_add_u32 s24, s52, 67108864
s_mul_i32 s50, s50, 28672
s_lshl_b32 s51, s51, 7
s_add_u32 s50, s50, s51
v_lshrrev_b32 v121, 3, v30
v_and_b32 v122, 7, v30
v_lshlrev_b32 v122, 4, v122
v_mul_u32_u24 v120, 896, v121
v_add_u32 v120, v120, v122
buffer_load_dwordx4 v[104:107], v120, s[28:31], s50 offen
s_add_u32 s50, s50, 7168
buffer_load_dwordx4 v[108:111], v120, s[28:31], s50 offen
s_add_u32 s50, s50, 7168
buffer_load_dwordx4 v[112:115], v120, s[28:31], s50 offen
s_add_u32 s50, s50, 7168
buffer_load_dwordx4 v[116:119], v120, s[28:31], s50 offen
s_mul_i32 s51, s49, 4608
s_add_u32 s51, s51, s67
v_mul_u32_u24 v121, 144, v121
v_add3_u32 v121, v121, v122, s51
v_lshrrev_b32 v122, 2, v30
v_mul_u32_u24 v122, 144, v122
v_and_b32 v123, 3, v30
v_lshlrev_b32 v123, 3, v123
v_add3_u32 v122, v122, v123, s51
v_add_u32 v8, s67, v14
v_add_u32 v9, 0x10000, v8
v_add_u32 v10, 0, v14
v_add_u32 v11, 8192, v14
v_add_u32 v12, 16384, v14
v_add_u32 v13, 24576, v14
v_mov_b32 v23, 0xff800000
v_and_b32 v27, 15, v30
v_lshrrev_b32 v28, 4, v30
v_lshlrev_b32 v28, 2, v28
v_sub_u32 v22, v27, v28
s_lshl_b32 s50, s49, 5
v_add_u32 v22, s50, v22
s_mov_b32 s36, s64
s_and_b32 s37, s65, 0xffff
s_mov_b32 s38, 209715200
s_mov_b32 s39, 0x20000
v_bfe_u32 v68, v30, 2, 2
v_lshrrev_b32 v69, 4, v30
v_lshl_add_u32 v69, v69, 3, v68
v_lshlrev_b32 v69, 9, v69
v_and_b32 v70, 3, v30
v_lshl_add_u32 v69, v70, 3, v69
s_lshl_b32 s50, s46, 8
s_add_u32 s50, s50, s44
v_add_u32 v69, s50, v69
v_bfe_u32 v70, v30, 4, 1
v_lshl_add_u32 v68, v68, 1, v70
v_xor_b32 v70, 0, v68
v_lshl_add_u32 v52, v70, 5, v69
v_mov_b32 v60, v52
v_xor_b32 v70, 1, v68
v_lshl_add_u32 v53, v70, 5, v69
v_mov_b32 v61, v53
v_xor_b32 v70, 2, v68
v_lshl_add_u32 v54, v70, 5, v69
v_mov_b32 v62, v54
v_xor_b32 v70, 3, v68
v_lshl_add_u32 v55, v70, 5, v69
v_mov_b32 v63, v55
v_xor_b32 v70, 4, v68
v_lshl_add_u32 v56, v70, 5, v69
v_mov_b32 v64, v56
v_xor_b32 v70, 5, v68
v_lshl_add_u32 v57, v70, 5, v69
v_mov_b32 v65, v57
v_xor_b32 v70, 6, v68
v_lshl_add_u32 v58, v70, 5, v69
v_mov_b32 v66, v58
v_xor_b32 v70, 7, v68
v_lshl_add_u32 v59, v70, 5, v69
v_mov_b32 v67, v59
v_and_b32 v68, 15, v30
s_lshl_b32 s50, s45, 13
s_add_u32 s50, s50, s67
s_add_u32 s50, s50, 32768
v_lshl_add_u32 v68, v68, 7, s50
v_lshrrev_b32 v69, 4, v30
v_bfe_u32 v70, v30, 1, 3
v_xor_b32 v69, v69, v70
v_xor_b32 v70, 0, v69
v_lshl_add_u32 v48, v70, 4, v68
v_mov_b32 v50, v48
v_xor_b32 v70, 4, v69
v_lshl_add_u32 v49, v70, 4, v68
v_mov_b32 v51, v49
s_lshr_b32 s51, s66, 5
s_lshl_b32 s50, s68, 6
s_lshl_b32 s51, s51, 3
s_add_u32 s50, s50, s51
s_lshl_b32 s51, s45, 2
s_add_u32 s50, s50, s51
s_lshl_b32 s50, s50, 14
s_bitcmp1_b32 s66, 4
s_cbranch_scc1 L_cvV
s_lshl_b32 s51, s46, 12
s_add_u32 s50, s50, s51
s_add_u32 s40, s50, 16777216
s_add_u32 s41, s50, 16785408
s_add_u32 s42, s50, 16793600
s_add_u32 s43, s50, 16801792
s_add_u32 s47, s50, 16809984
s_add_u32 s48, s50, 16818176
s_add_u32 s72, s50, 16826368
s_add_u32 s73, s50, 16834560
s_branch L_cvdone
L_cvV:
s_lshl_b32 s51, s46, 13
s_add_u32 s50, s50, s51
s_add_u32 s40, s50, 33554432
s_add_u32 s41, s50, 33558528
s_add_u32 s42, s50, 33570816
s_add_u32 s43, s50, 33574912
s_add_u32 s47, s50, 33587200
s_add_u32 s48, s50, 33591296
s_add_u32 s72, s50, 33603584
s_add_u32 s73, s50, 33607680
L_cvdone:
v_accvgpr_read_b32 v128, a128
v_accvgpr_read_b32 v129, a129
v_accvgpr_read_b32 v130, a130
v_accvgpr_read_b32 v131, a131
v_accvgpr_read_b32 v132, a132
v_accvgpr_read_b32 v133, a133
v_accvgpr_read_b32 v134, a134
v_accvgpr_read_b32 v135, a135
v_cvt_pk_bf16_f32 v128, v128, v129
v_cvt_pk_bf16_f32 v129, v130, v131
v_cvt_pk_bf16_f32 v130, v132, v133
v_cvt_pk_bf16_f32 v131, v134, v135
buffer_store_dwordx4 v[128:131], v14, s[36:39], s47 offen offset:0 sc1
v_accvgpr_read_b32 v136, a136
v_accvgpr_read_b32 v137, a137
v_accvgpr_read_b32 v138, a138
v_accvgpr_read_b32 v139, a139
v_accvgpr_read_b32 v140, a140
v_accvgpr_read_b32 v141, a141
v_accvgpr_read_b32 v142, a142
v_accvgpr_read_b32 v143, a143
v_cvt_pk_bf16_f32 v136, v136, v137
v_cvt_pk_bf16_f32 v137, v138, v139
v_cvt_pk_bf16_f32 v138, v140, v141
v_cvt_pk_bf16_f32 v139, v142, v143
buffer_store_dwordx4 v[136:139], v14, s[36:39], s47 offen offset:1024 sc1
v_accvgpr_read_b32 v144, a144
v_accvgpr_read_b32 v145, a145
v_accvgpr_read_b32 v146, a146
v_accvgpr_read_b32 v147, a147
v_accvgpr_read_b32 v148, a148
v_accvgpr_read_b32 v149, a149
v_accvgpr_read_b32 v150, a150
v_accvgpr_read_b32 v151, a151
v_cvt_pk_bf16_f32 v144, v144, v145
v_cvt_pk_bf16_f32 v145, v146, v147
v_cvt_pk_bf16_f32 v146, v148, v149
v_cvt_pk_bf16_f32 v147, v150, v151
buffer_store_dwordx4 v[144:147], v14, s[36:39], s47 offen offset:2048 sc1
v_accvgpr_read_b32 v152, a152
v_accvgpr_read_b32 v153, a153
v_accvgpr_read_b32 v154, a154
v_accvgpr_read_b32 v155, a155
v_accvgpr_read_b32 v156, a156
v_accvgpr_read_b32 v157, a157
v_accvgpr_read_b32 v158, a158
v_accvgpr_read_b32 v159, a159
v_cvt_pk_bf16_f32 v152, v152, v153
v_cvt_pk_bf16_f32 v153, v154, v155
v_cvt_pk_bf16_f32 v154, v156, v157
v_cvt_pk_bf16_f32 v155, v158, v159
buffer_store_dwordx4 v[152:155], v14, s[36:39], s47 offen offset:3072 sc1
v_accvgpr_read_b32 v160, a160
v_accvgpr_read_b32 v161, a161
v_accvgpr_read_b32 v162, a162
v_accvgpr_read_b32 v163, a163
v_accvgpr_read_b32 v164, a164
v_accvgpr_read_b32 v165, a165
v_accvgpr_read_b32 v166, a166
v_accvgpr_read_b32 v167, a167
v_cvt_pk_bf16_f32 v160, v160, v161
v_cvt_pk_bf16_f32 v161, v162, v163
v_cvt_pk_bf16_f32 v162, v164, v165
v_cvt_pk_bf16_f32 v163, v166, v167
buffer_store_dwordx4 v[160:163], v14, s[36:39], s48 offen offset:0 sc1
v_accvgpr_read_b32 v168, a168
v_accvgpr_read_b32 v169, a169
v_accvgpr_read_b32 v170, a170
v_accvgpr_read_b32 v171, a171
v_accvgpr_read_b32 v172, a172
v_accvgpr_read_b32 v173, a173
v_accvgpr_read_b32 v174, a174
v_accvgpr_read_b32 v175, a175
v_cvt_pk_bf16_f32 v168, v168, v169
v_cvt_pk_bf16_f32 v169, v170, v171
v_cvt_pk_bf16_f32 v170, v172, v173
v_cvt_pk_bf16_f32 v171, v174, v175
buffer_store_dwordx4 v[168:171], v14, s[36:39], s48 offen offset:1024 sc1
v_accvgpr_read_b32 v176, a176
v_accvgpr_read_b32 v177, a177
v_accvgpr_read_b32 v178, a178
v_accvgpr_read_b32 v179, a179
v_accvgpr_read_b32 v180, a180
v_accvgpr_read_b32 v181, a181
v_accvgpr_read_b32 v182, a182
v_accvgpr_read_b32 v183, a183
v_cvt_pk_bf16_f32 v176, v176, v177
v_cvt_pk_bf16_f32 v177, v178, v179
v_cvt_pk_bf16_f32 v178, v180, v181
v_cvt_pk_bf16_f32 v179, v182, v183
buffer_store_dwordx4 v[176:179], v14, s[36:39], s48 offen offset:2048 sc1
v_accvgpr_read_b32 v184, a184
v_accvgpr_read_b32 v185, a185
v_accvgpr_read_b32 v186, a186
v_accvgpr_read_b32 v187, a187
v_accvgpr_read_b32 v188, a188
v_accvgpr_read_b32 v189, a189
v_accvgpr_read_b32 v190, a190
v_accvgpr_read_b32 v191, a191
v_cvt_pk_bf16_f32 v184, v184, v185
v_cvt_pk_bf16_f32 v185, v186, v187
v_cvt_pk_bf16_f32 v186, v188, v189
v_cvt_pk_bf16_f32 v187, v190, v191
buffer_store_dwordx4 v[184:187], v14, s[36:39], s48 offen offset:3072 sc1
v_accvgpr_read_b32 v192, a192
v_accvgpr_read_b32 v193, a193
v_accvgpr_read_b32 v194, a194
v_accvgpr_read_b32 v195, a195
v_accvgpr_read_b32 v196, a196
v_accvgpr_read_b32 v197, a197
v_accvgpr_read_b32 v198, a198
v_accvgpr_read_b32 v199, a199
v_cvt_pk_bf16_f32 v192, v192, v193
v_cvt_pk_bf16_f32 v193, v194, v195
v_cvt_pk_bf16_f32 v194, v196, v197
v_cvt_pk_bf16_f32 v195, v198, v199
buffer_store_dwordx4 v[192:195], v14, s[36:39], s72 offen offset:0 sc1
v_accvgpr_read_b32 v200, a200
v_accvgpr_read_b32 v201, a201
v_accvgpr_read_b32 v202, a202
v_accvgpr_read_b32 v203, a203
v_accvgpr_read_b32 v204, a204
v_accvgpr_read_b32 v205, a205
v_accvgpr_read_b32 v206, a206
v_accvgpr_read_b32 v207, a207
v_cvt_pk_bf16_f32 v200, v200, v201
v_cvt_pk_bf16_f32 v201, v202, v203
v_cvt_pk_bf16_f32 v202, v204, v205
v_cvt_pk_bf16_f32 v203, v206, v207
buffer_store_dwordx4 v[200:203], v14, s[36:39], s72 offen offset:1024 sc1
v_accvgpr_read_b32 v208, a208
v_accvgpr_read_b32 v209, a209
v_accvgpr_read_b32 v210, a210
v_accvgpr_read_b32 v211, a211
v_accvgpr_read_b32 v212, a212
v_accvgpr_read_b32 v213, a213
v_accvgpr_read_b32 v214, a214
v_accvgpr_read_b32 v215, a215
v_cvt_pk_bf16_f32 v208, v208, v209
v_cvt_pk_bf16_f32 v209, v210, v211
v_cvt_pk_bf16_f32 v210, v212, v213
v_cvt_pk_bf16_f32 v211, v214, v215
buffer_store_dwordx4 v[208:211], v14, s[36:39], s72 offen offset:2048 sc1
v_accvgpr_read_b32 v216, a216
v_accvgpr_read_b32 v217, a217
v_accvgpr_read_b32 v218, a218
v_accvgpr_read_b32 v219, a219
v_accvgpr_read_b32 v220, a220
v_accvgpr_read_b32 v221, a221
v_accvgpr_read_b32 v222, a222
v_accvgpr_read_b32 v223, a223
v_cvt_pk_bf16_f32 v216, v216, v217
v_cvt_pk_bf16_f32 v217, v218, v219
v_cvt_pk_bf16_f32 v218, v220, v221
v_cvt_pk_bf16_f32 v219, v222, v223
buffer_store_dwordx4 v[216:219], v14, s[36:39], s72 offen offset:3072 sc1
v_accvgpr_read_b32 v224, a224
v_accvgpr_read_b32 v225, a225
v_accvgpr_read_b32 v226, a226
v_accvgpr_read_b32 v227, a227
v_accvgpr_read_b32 v228, a228
v_accvgpr_read_b32 v229, a229
v_accvgpr_read_b32 v230, a230
v_accvgpr_read_b32 v231, a231
v_cvt_pk_bf16_f32 v224, v224, v225
v_cvt_pk_bf16_f32 v225, v226, v227
v_cvt_pk_bf16_f32 v226, v228, v229
v_cvt_pk_bf16_f32 v227, v230, v231
buffer_store_dwordx4 v[224:227], v14, s[36:39], s73 offen offset:0 sc1
v_accvgpr_read_b32 v232, a232
v_accvgpr_read_b32 v233, a233
v_accvgpr_read_b32 v234, a234
v_accvgpr_read_b32 v235, a235
v_accvgpr_read_b32 v236, a236
v_accvgpr_read_b32 v237, a237
v_accvgpr_read_b32 v238, a238
v_accvgpr_read_b32 v239, a239
v_cvt_pk_bf16_f32 v232, v232, v233
v_cvt_pk_bf16_f32 v233, v234, v235
v_cvt_pk_bf16_f32 v234, v236, v237
v_cvt_pk_bf16_f32 v235, v238, v239
buffer_store_dwordx4 v[232:235], v14, s[36:39], s73 offen offset:1024 sc1
v_accvgpr_read_b32 v240, a240
v_accvgpr_read_b32 v241, a241
v_accvgpr_read_b32 v242, a242
v_accvgpr_read_b32 v243, a243
v_accvgpr_read_b32 v244, a244
v_accvgpr_read_b32 v245, a245
v_accvgpr_read_b32 v246, a246
v_accvgpr_read_b32 v247, a247
v_cvt_pk_bf16_f32 v240, v240, v241
v_cvt_pk_bf16_f32 v241, v242, v243
v_cvt_pk_bf16_f32 v242, v244, v245
v_cvt_pk_bf16_f32 v243, v246, v247
buffer_store_dwordx4 v[240:243], v14, s[36:39], s73 offen offset:2048 sc1
v_accvgpr_read_b32 v248, a248
v_accvgpr_read_b32 v249, a249
v_accvgpr_read_b32 v250, a250
v_accvgpr_read_b32 v251, a251
v_accvgpr_read_b32 v252, a252
v_accvgpr_read_b32 v253, a253
v_accvgpr_read_b32 v254, a254
v_accvgpr_read_b32 v255, a255
v_cvt_pk_bf16_f32 v248, v248, v249
v_cvt_pk_bf16_f32 v249, v250, v251
v_cvt_pk_bf16_f32 v250, v252, v253
v_cvt_pk_bf16_f32 v251, v254, v255
buffer_store_dwordx4 v[248:251], v14, s[36:39], s73 offen offset:3072 sc1
s_waitcnt vmcnt(38)
s_barrier
ds_read_b64_tr_b16 a[160:161], v60 offset:0
ds_read_b64_tr_b16 a[162:163], v60 offset:2048
ds_read_b128 a[128:131], v50 offset:0
ds_read_b128 a[132:135], v50 offset:2048
ds_read_b128 a[136:139], v50 offset:4096
ds_read_b128 a[140:143], v50 offset:6144
ds_read_b64_tr_b16 a[164:165], v61 offset:0
ds_read_b64_tr_b16 a[166:167], v61 offset:2048
ds_read_b64_tr_b16 a[168:169], v62 offset:0
ds_read_b64_tr_b16 a[170:171], v62 offset:2048
ds_read_b64_tr_b16 a[172:173], v63 offset:0
ds_read_b64_tr_b16 a[174:175], v63 offset:2048
ds_read_b64_tr_b16 a[176:177], v64 offset:0
ds_read_b64_tr_b16 a[178:179], v64 offset:2048
ds_read_b64_tr_b16 a[180:181], v65 offset:0
ds_read_b64_tr_b16 a[182:183], v65 offset:2048
ds_read_b64_tr_b16 a[184:185], v66 offset:0
ds_read_b64_tr_b16 a[186:187], v66 offset:2048
ds_read_b64_tr_b16 a[188:189], v67 offset:0
ds_read_b64_tr_b16 a[190:191], v67 offset:2048
s_waitcnt lgkmcnt(0)
v_mfma_f32_16x16x32_bf16 v[128:131], a[160:163], a[128:131], 0
s_add_u32 s59, s59, 16384
s_and_b32 s59, s59, 0xffff
v_mfma_f32_16x16x32_bf16 v[160:163], a[160:163], a[132:135], 0
v_add_u32 v60, s59, v52
v_add_u32 v61, s59, v53
v_accvgpr_read_b32 v72, a0
v_mfma_f32_16x16x32_bf16 v[192:195], a[160:163], a[136:139], 0
v_add_u32 v62, s59, v54
v_add_u32 v63, s59, v55
v_accvgpr_read_b32 v73, a1
v_mfma_f32_16x16x32_bf16 v[224:227], a[160:163], a[140:143], 0
v_add_u32 v64, s59, v56
v_add_u32 v65, s59, v57
v_accvgpr_read_b32 v74, a2
v_mfma_f32_16x16x32_bf16 v[132:135], a[164:167], a[128:131], 0
v_add_u32 v66, s59, v58
v_add_u32 v67, s59, v59
v_accvgpr_read_b32 v75, a3
v_mfma_f32_16x16x32_bf16 v[164:167], a[164:167], a[132:135], 0
v_accvgpr_read_b32 v76, a4
v_mfma_f32_16x16x32_bf16 v[196:199], a[164:167], a[136:139], 0
v_accvgpr_read_b32 v77, a5
v_mfma_f32_16x16x32_bf16 v[228:231], a[164:167], a[140:143], 0
s_waitcnt vmcnt(34)
s_barrier
v_accvgpr_read_b32 v78, a6
v_mfma_f32_16x16x32_bf16 v[136:139], a[168:171], a[128:131], 0
ds_read_b64_tr_b16 a[192:193], v60 offset:0
ds_read_b64_tr_b16 a[194:195], v60 offset:2048
v_mfma_f32_16x16x32_bf16 v[168:171], a[168:171], a[132:135], 0
ds_read_b128 a[144:147], v51 offset:0
s_add_u32 m0, s57, s63
v_accvgpr_read_b32 v79, a7
v_mfma_f32_16x16x32_bf16 v[200:203], a[168:171], a[136:139], 0
ds_read_b128 a[148:151], v51 offset:2048
buffer_load_dwordx4 v44, s[80:83], s55 offen offset:0 lds
v_cvt_pk_bf16_f32 v72, v72, v73
v_mfma_f32_16x16x32_bf16 v[232:235], a[168:171], a[140:143], 0
ds_read_b128 a[152:155], v51 offset:4096
v_cvt_pk_bf16_f32 v73, v74, v75
v_mfma_f32_16x16x32_bf16 v[140:143], a[172:175], a[128:131], 0
ds_read_b128 a[156:159], v51 offset:6144
v_cvt_pk_bf16_f32 v74, v76, v77
v_mfma_f32_16x16x32_bf16 v[172:175], a[172:175], a[132:135], 0
ds_read_b64_tr_b16 a[196:197], v61 offset:0
ds_read_b64_tr_b16 a[198:199], v61 offset:2048
v_cvt_pk_bf16_f32 v75, v78, v79
v_mfma_f32_16x16x32_bf16 v[204:207], a[172:175], a[136:139], 0
ds_read_b64_tr_b16 a[200:201], v62 offset:0
buffer_load_dwordx4 v45, s[80:83], s55 offen offset:1024 lds
buffer_store_dwordx4 v[72:75], v14, s[36:39], s40 offen offset:0 sc1
v_mfma_f32_16x16x32_bf16 v[236:239], a[172:175], a[140:143], 0
ds_read_b64_tr_b16 a[202:203], v62 offset:2048
v_mfma_f32_16x16x32_bf16 v[144:147], a[176:179], a[128:131], 0
ds_read_b64_tr_b16 a[204:205], v63 offset:0
buffer_load_dwordx4 v46, s[80:83], s55 offen offset:2048 lds
v_accvgpr_read_b32 v80, a8
v_mfma_f32_16x16x32_bf16 v[176:179], a[176:179], a[132:135], 0
ds_read_b64_tr_b16 a[206:207], v63 offset:2048
v_accvgpr_read_b32 v81, a9
v_mfma_f32_16x16x32_bf16 v[208:211], a[176:179], a[136:139], 0
ds_read_b64_tr_b16 a[208:209], v64 offset:0
v_accvgpr_read_b32 v82, a10
v_mfma_f32_16x16x32_bf16 v[240:243], a[176:179], a[140:143], 0
ds_read_b64_tr_b16 a[210:211], v64 offset:2048
ds_read_b64_tr_b16 a[212:213], v65 offset:0
v_accvgpr_read_b32 v83, a11
v_mfma_f32_16x16x32_bf16 v[148:151], a[180:183], a[128:131], 0
ds_read_b64_tr_b16 a[214:215], v65 offset:2048
buffer_load_dwordx4 v47, s[80:83], s55 offen offset:3072 lds
v_accvgpr_read_b32 v84, a12
v_mfma_f32_16x16x32_bf16 v[180:183], a[180:183], a[132:135], 0
ds_read_b64_tr_b16 a[216:217], v66 offset:0
s_add_u32 s57, s57, 16384
v_accvgpr_read_b32 v85, a13
v_mfma_f32_16x16x32_bf16 v[212:215], a[180:183], a[136:139], 0
ds_read_b64_tr_b16 a[218:219], v66 offset:2048
s_and_b32 s57, s57, 0xffff
v_accvgpr_read_b32 v86, a14
v_mfma_f32_16x16x32_bf16 v[244:247], a[180:183], a[140:143], 0
ds_read_b64_tr_b16 a[220:221], v67 offset:0
s_add_u32 s55, s55, 262144
v_mfma_f32_16x16x32_bf16 v[152:155], a[184:187], a[128:131], 0
ds_read_b64_tr_b16 a[222:223], v67 offset:2048
s_mov_b32 m0, s56
v_accvgpr_read_b32 v87, a15
v_mfma_f32_16x16x32_bf16 v[184:187], a[184:187], a[132:135], 0
buffer_load_dwordx4 v42, s[76:79], s54 offen offset:2048 lds
buffer_load_dwordx4 v43, s[76:79], s54 offen offset:3072 lds
v_cvt_pk_bf16_f32 v80, v80, v81
v_mfma_f32_16x16x32_bf16 v[216:219], a[184:187], a[136:139], 0
s_add_u32 s56, s56, 16384
s_cmp_ge_u32 s56, s44
v_cvt_pk_bf16_f32 v81, v82, v83
v_mfma_f32_16x16x32_bf16 v[248:251], a[184:187], a[140:143], 0
s_cselect_b32 s50, 49152, 0
s_sub_u32 s56, s56, s50
v_cvt_pk_bf16_f32 v82, v84, v85
v_mfma_f32_16x16x32_bf16 v[156:159], a[188:191], a[128:131], 0
s_add_u32 s54, s54, 128
v_cvt_pk_bf16_f32 v83, v86, v87
v_mfma_f32_16x16x32_bf16 v[188:191], a[188:191], a[132:135], 0
buffer_store_dwordx4 v[80:83], v14, s[36:39], s40 offen offset:1024 sc1
v_mfma_f32_16x16x32_bf16 v[220:223], a[188:191], a[136:139], 0
v_mfma_f32_16x16x32_bf16 v[252:255], a[188:191], a[140:143], 0
s_waitcnt lgkmcnt(0)
v_mfma_f32_16x16x32_bf16 v[128:131], a[192:195], a[144:147], v[128:131]
s_add_u32 s59, s59, 16384
s_and_b32 s59, s59, 0xffff
v_mfma_f32_16x16x32_bf16 v[160:163], a[192:195], a[148:151], v[160:163]
v_add_u32 v60, s59, v52
v_add_u32 v61, s59, v53
v_accvgpr_read_b32 v72, a16
v_mfma_f32_16x16x32_bf16 v[192:195], a[192:195], a[152:155], v[192:195]
v_add_u32 v62, s59, v54
v_add_u32 v63, s59, v55
v_accvgpr_read_b32 v73, a17
v_mfma_f32_16x16x32_bf16 v[224:227], a[192:195], a[156:159], v[224:227]
v_add_u32 v64, s59, v56
v_add_u32 v65, s59, v57
v_accvgpr_read_b32 v74, a18
v_mfma_f32_16x16x32_bf16 v[132:135], a[196:199], a[144:147], v[132:135]
v_add_u32 v66, s59, v58
v_add_u32 v67, s59, v59
v_accvgpr_read_b32 v75, a19
v_mfma_f32_16x16x32_bf16 v[164:167], a[196:199], a[148:151], v[164:167]
s_add_u32 s58, s58, 16384
s_cmp_ge_u32 s58, 49152
v_accvgpr_read_b32 v76, a20
v_mfma_f32_16x16x32_bf16 v[196:199], a[196:199], a[152:155], v[196:199]
s_cselect_b32 s58, 0, s58
v_add_u32 v50, s58, v48
v_accvgpr_read_b32 v77, a21
v_mfma_f32_16x16x32_bf16 v[228:231], a[196:199], a[156:159], v[228:231]
v_add_u32 v51, s58, v49
s_waitcnt vmcnt(34)
v_accvgpr_read_b32 v78, a22
v_mfma_f32_16x16x32_bf16 v[136:139], a[200:203], a[144:147], v[136:139]
s_barrier
ds_read_b64_tr_b16 a[160:161], v60 offset:0
v_mfma_f32_16x16x32_bf16 v[168:171], a[200:203], a[148:151], v[168:171]
ds_read_b64_tr_b16 a[162:163], v60 offset:2048
ds_read_b128 a[128:131], v50 offset:0
v_accvgpr_read_b32 v79, a23
v_mfma_f32_16x16x32_bf16 v[200:203], a[200:203], a[152:155], v[200:203]
ds_read_b128 a[132:135], v50 offset:2048
s_add_u32 m0, s57, s63
v_cvt_pk_bf16_f32 v72, v72, v73
v_mfma_f32_16x16x32_bf16 v[232:235], a[200:203], a[156:159], v[232:235]
ds_read_b128 a[136:139], v50 offset:4096
buffer_load_dwordx4 v44, s[80:83], s55 offen offset:0 lds
v_cvt_pk_bf16_f32 v73, v74, v75
v_mfma_f32_16x16x32_bf16 v[140:143], a[204:207], a[144:147], v[140:143]
ds_read_b128 a[140:143], v50 offset:6144
v_cvt_pk_bf16_f32 v74, v76, v77
v_mfma_f32_16x16x32_bf16 v[172:175], a[204:207], a[148:151], v[172:175]
ds_read_b64_tr_b16 a[164:165], v61 offset:0
ds_read_b64_tr_b16 a[166:167], v61 offset:2048
v_cvt_pk_bf16_f32 v75, v78, v79
v_mfma_f32_16x16x32_bf16 v[204:207], a[204:207], a[152:155], v[204:207]
ds_read_b64_tr_b16 a[168:169], v62 offset:0
buffer_load_dwordx4 v45, s[80:83], s55 offen offset:1024 lds
buffer_store_dwordx4 v[72:75], v14, s[36:39], s40 offen offset:2048 sc1
v_mfma_f32_16x16x32_bf16 v[236:239], a[204:207], a[156:159], v[236:239]
ds_read_b64_tr_b16 a[170:171], v62 offset:2048
v_mfma_f32_16x16x32_bf16 v[144:147], a[208:211], a[144:147], v[144:147]
ds_read_b64_tr_b16 a[172:173], v63 offset:0
buffer_load_dwordx4 v46, s[80:83], s55 offen offset:2048 lds
v_accvgpr_read_b32 v80, a24
v_mfma_f32_16x16x32_bf16 v[176:179], a[208:211], a[148:151], v[176:179]
ds_read_b64_tr_b16 a[174:175], v63 offset:2048
v_accvgpr_read_b32 v81, a25
v_mfma_f32_16x16x32_bf16 v[208:211], a[208:211], a[152:155], v[208:211]
ds_read_b64_tr_b16 a[176:177], v64 offset:0
v_accvgpr_read_b32 v82, a26
v_mfma_f32_16x16x32_bf16 v[240:243], a[208:211], a[156:159], v[240:243]
ds_read_b64_tr_b16 a[178:179], v64 offset:2048
ds_read_b64_tr_b16 a[180:181], v65 offset:0
v_accvgpr_read_b32 v83, a27
v_mfma_f32_16x16x32_bf16 v[148:151], a[212:215], a[144:147], v[148:151]
ds_read_b64_tr_b16 a[182:183], v65 offset:2048
buffer_load_dwordx4 v47, s[80:83], s55 offen offset:3072 lds
v_accvgpr_read_b32 v84, a28
v_mfma_f32_16x16x32_bf16 v[180:183], a[212:215], a[148:151], v[180:183]
ds_read_b64_tr_b16 a[184:185], v66 offset:0
s_add_u32 s57, s57, 16384
v_accvgpr_read_b32 v85, a29
v_mfma_f32_16x16x32_bf16 v[212:215], a[212:215], a[152:155], v[212:215]
ds_read_b64_tr_b16 a[186:187], v66 offset:2048
s_and_b32 s57, s57, 0xffff
v_accvgpr_read_b32 v86, a30
v_mfma_f32_16x16x32_bf16 v[244:247], a[212:215], a[156:159], v[244:247]
ds_read_b64_tr_b16 a[188:189], v67 offset:0
s_add_u32 s55, s55, 262144
v_mfma_f32_16x16x32_bf16 v[152:155], a[216:219], a[144:147], v[152:155]
ds_read_b64_tr_b16 a[190:191], v67 offset:2048
s_mov_b32 m0, s56
v_accvgpr_read_b32 v87, a31
v_mfma_f32_16x16x32_bf16 v[184:187], a[216:219], a[148:151], v[184:187]
buffer_load_dwordx4 v40, s[76:79], s54 offen offset:0 lds
buffer_load_dwordx4 v41, s[76:79], s54 offen offset:1024 lds
v_cvt_pk_bf16_f32 v80, v80, v81
v_mfma_f32_16x16x32_bf16 v[216:219], a[216:219], a[152:155], v[216:219]
v_cvt_pk_bf16_f32 v81, v82, v83
v_mfma_f32_16x16x32_bf16 v[248:251], a[216:219], a[156:159], v[248:251]
v_cvt_pk_bf16_f32 v82, v84, v85
v_mfma_f32_16x16x32_bf16 v[156:159], a[220:223], a[144:147], v[156:159]
v_cvt_pk_bf16_f32 v83, v86, v87
v_mfma_f32_16x16x32_bf16 v[188:191], a[220:223], a[148:151], v[188:191]
buffer_store_dwordx4 v[80:83], v14, s[36:39], s40 offen offset:3072 sc1
v_mfma_f32_16x16x32_bf16 v[220:223], a[220:223], a[152:155], v[220:223]
v_mfma_f32_16x16x32_bf16 v[252:255], a[220:223], a[156:159], v[252:255]
s_waitcnt lgkmcnt(0)
v_mfma_f32_16x16x32_bf16 v[128:131], a[160:163], a[128:131], v[128:131]
s_add_u32 s59, s59, 16384
s_and_b32 s59, s59, 0xffff
v_mfma_f32_16x16x32_bf16 v[160:163], a[160:163], a[132:135], v[160:163]
v_add_u32 v60, s59, v52
v_add_u32 v61, s59, v53
v_accvgpr_read_b32 v72, a32
v_mfma_f32_16x16x32_bf16 v[192:195], a[160:163], a[136:139], v[192:195]
v_add_u32 v62, s59, v54
v_add_u32 v63, s59, v55
v_accvgpr_read_b32 v73, a33
v_mfma_f32_16x16x32_bf16 v[224:227], a[160:163], a[140:143], v[224:227]
v_add_u32 v64, s59, v56
v_add_u32 v65, s59, v57
v_accvgpr_read_b32 v74, a34
v_mfma_f32_16x16x32_bf16 v[132:135], a[164:167], a[128:131], v[132:135]
v_add_u32 v66, s59, v58
v_add_u32 v67, s59, v59
v_accvgpr_read_b32 v75, a35
v_mfma_f32_16x16x32_bf16 v[164:167], a[164:167], a[132:135], v[164:167]
v_accvgpr_read_b32 v76, a36
v_mfma_f32_16x16x32_bf16 v[196:199], a[164:167], a[136:139], v[196:199]
v_accvgpr_read_b32 v77, a37
v_mfma_f32_16x16x32_bf16 v[228:231], a[164:167], a[140:143], v[228:231]
s_waitcnt vmcnt(38)
s_barrier
v_accvgpr_read_b32 v78, a38
v_mfma_f32_16x16x32_bf16 v[136:139], a[168:171], a[128:131], v[136:139]
ds_read_b64_tr_b16 a[192:193], v60 offset:0
ds_read_b64_tr_b16 a[194:195], v60 offset:2048
v_mfma_f32_16x16x32_bf16 v[168:171], a[168:171], a[132:135], v[168:171]
ds_read_b128 a[144:147], v51 offset:0
s_add_u32 m0, s57, s63
v_accvgpr_read_b32 v79, a39
v_mfma_f32_16x16x32_bf16 v[200:203], a[168:171], a[136:139], v[200:203]
ds_read_b128 a[148:151], v51 offset:2048
buffer_load_dwordx4 v44, s[80:83], s55 offen offset:0 lds
v_cvt_pk_bf16_f32 v72, v72, v73
v_mfma_f32_16x16x32_bf16 v[232:235], a[168:171], a[140:143], v[232:235]
ds_read_b128 a[152:155], v51 offset:4096
v_cvt_pk_bf16_f32 v73, v74, v75
v_mfma_f32_16x16x32_bf16 v[140:143], a[172:175], a[128:131], v[140:143]
ds_read_b128 a[156:159], v51 offset:6144
v_cvt_pk_bf16_f32 v74, v76, v77
v_mfma_f32_16x16x32_bf16 v[172:175], a[172:175], a[132:135], v[172:175]
ds_read_b64_tr_b16 a[196:197], v61 offset:0
ds_read_b64_tr_b16 a[198:199], v61 offset:2048
v_cvt_pk_bf16_f32 v75, v78, v79
v_mfma_f32_16x16x32_bf16 v[204:207], a[172:175], a[136:139], v[204:207]
ds_read_b64_tr_b16 a[200:201], v62 offset:0
buffer_load_dwordx4 v45, s[80:83], s55 offen offset:1024 lds
buffer_store_dwordx4 v[72:75], v14, s[36:39], s41 offen offset:0 sc1
v_mfma_f32_16x16x32_bf16 v[236:239], a[172:175], a[140:143], v[236:239]
ds_read_b64_tr_b16 a[202:203], v62 offset:2048
v_mfma_f32_16x16x32_bf16 v[144:147], a[176:179], a[128:131], v[144:147]
ds_read_b64_tr_b16 a[204:205], v63 offset:0
buffer_load_dwordx4 v46, s[80:83], s55 offen offset:2048 lds
v_accvgpr_read_b32 v80, a40
v_mfma_f32_16x16x32_bf16 v[176:179], a[176:179], a[132:135], v[176:179]
ds_read_b64_tr_b16 a[206:207], v63 offset:2048
v_accvgpr_read_b32 v81, a41
v_mfma_f32_16x16x32_bf16 v[208:211], a[176:179], a[136:139], v[208:211]
ds_read_b64_tr_b16 a[208:209], v64 offset:0
v_accvgpr_read_b32 v82, a42
v_mfma_f32_16x16x32_bf16 v[240:243], a[176:179], a[140:143], v[240:243]
ds_read_b64_tr_b16 a[210:211], v64 offset:2048
ds_read_b64_tr_b16 a[212:213], v65 offset:0
v_accvgpr_read_b32 v83, a43
v_mfma_f32_16x16x32_bf16 v[148:151], a[180:183], a[128:131], v[148:151]
ds_read_b64_tr_b16 a[214:215], v65 offset:2048
buffer_load_dwordx4 v47, s[80:83], s55 offen offset:3072 lds
v_accvgpr_read_b32 v84, a44
v_mfma_f32_16x16x32_bf16 v[180:183], a[180:183], a[132:135], v[180:183]
ds_read_b64_tr_b16 a[216:217], v66 offset:0
s_add_u32 s57, s57, 16384
v_accvgpr_read_b32 v85, a45
v_mfma_f32_16x16x32_bf16 v[212:215], a[180:183], a[136:139], v[212:215]
ds_read_b64_tr_b16 a[218:219], v66 offset:2048
s_and_b32 s57, s57, 0xffff
v_accvgpr_read_b32 v86, a46
v_mfma_f32_16x16x32_bf16 v[244:247], a[180:183], a[140:143], v[244:247]
ds_read_b64_tr_b16 a[220:221], v67 offset:0
s_add_u32 s55, s55, 262144
v_mfma_f32_16x16x32_bf16 v[152:155], a[184:187], a[128:131], v[152:155]
ds_read_b64_tr_b16 a[222:223], v67 offset:2048
s_mov_b32 m0, s56
v_accvgpr_read_b32 v87, a47
v_mfma_f32_16x16x32_bf16 v[184:187], a[184:187], a[132:135], v[184:187]
buffer_load_dwordx4 v42, s[76:79], s54 offen offset:2048 lds
buffer_load_dwordx4 v43, s[76:79], s54 offen offset:3072 lds
v_cvt_pk_bf16_f32 v80, v80, v81
v_mfma_f32_16x16x32_bf16 v[216:219], a[184:187], a[136:139], v[216:219]
s_add_u32 s56, s56, 16384
s_cmp_ge_u32 s56, s44
v_cvt_pk_bf16_f32 v81, v82, v83
v_mfma_f32_16x16x32_bf16 v[248:251], a[184:187], a[140:143], v[248:251]
s_cselect_b32 s50, 49152, 0
s_sub_u32 s56, s56, s50
v_cvt_pk_bf16_f32 v82, v84, v85
v_mfma_f32_16x16x32_bf16 v[156:159], a[188:191], a[128:131], v[156:159]
s_add_u32 s54, s54, 128
v_cvt_pk_bf16_f32 v83, v86, v87
v_mfma_f32_16x16x32_bf16 v[188:191], a[188:191], a[132:135], v[188:191]
buffer_store_dwordx4 v[80:83], v14, s[36:39], s41 offen offset:1024 sc1
v_mfma_f32_16x16x32_bf16 v[220:223], a[188:191], a[136:139], v[220:223]
v_mfma_f32_16x16x32_bf16 v[252:255], a[188:191], a[140:143], v[252:255]
s_waitcnt lgkmcnt(0)
v_mfma_f32_16x16x32_bf16 v[128:131], a[192:195], a[144:147], v[128:131]
s_add_u32 s59, s59, 16384
s_and_b32 s59, s59, 0xffff
v_mfma_f32_16x16x32_bf16 v[160:163], a[192:195], a[148:151], v[160:163]
v_add_u32 v60, s59, v52
v_add_u32 v61, s59, v53
v_accvgpr_read_b32 v72, a48
v_mfma_f32_16x16x32_bf16 v[192:195], a[192:195], a[152:155], v[192:195]
v_add_u32 v62, s59, v54
v_add_u32 v63, s59, v55
v_accvgpr_read_b32 v73, a49
v_mfma_f32_16x16x32_bf16 v[224:227], a[192:195], a[156:159], v[224:227]
v_add_u32 v64, s59, v56
v_add_u32 v65, s59, v57
v_accvgpr_read_b32 v74, a50
v_mfma_f32_16x16x32_bf16 v[132:135], a[196:199], a[144:147], v[132:135]
v_add_u32 v66, s59, v58
v_add_u32 v67, s59, v59
v_accvgpr_read_b32 v75, a51
v_mfma_f32_16x16x32_bf16 v[164:167], a[196:199], a[148:151], v[164:167]
s_add_u32 s58, s58, 16384
s_cmp_ge_u32 s58, 49152
v_accvgpr_read_b32 v76, a52
v_mfma_f32_16x16x32_bf16 v[196:199], a[196:199], a[152:155], v[196:199]
s_cselect_b32 s58, 0, s58
v_add_u32 v50, s58, v48
v_accvgpr_read_b32 v77, a53
v_mfma_f32_16x16x32_bf16 v[228:231], a[196:199], a[156:159], v[228:231]
v_add_u32 v51, s58, v49
s_waitcnt vmcnt(16)
v_accvgpr_read_b32 v78, a54
v_mfma_f32_16x16x32_bf16 v[136:139], a[200:203], a[144:147], v[136:139]
s_barrier
ds_read_b64_tr_b16 a[160:161], v60 offset:0
v_mfma_f32_16x16x32_bf16 v[168:171], a[200:203], a[148:151], v[168:171]
ds_read_b64_tr_b16 a[162:163], v60 offset:2048
ds_read_b128 a[128:131], v50 offset:0
v_accvgpr_read_b32 v79, a55
v_mfma_f32_16x16x32_bf16 v[200:203], a[200:203], a[152:155], v[200:203]
ds_read_b128 a[132:135], v50 offset:2048
s_add_u32 m0, s57, s63
v_cvt_pk_bf16_f32 v72, v72, v73
v_mfma_f32_16x16x32_bf16 v[232:235], a[200:203], a[156:159], v[232:235]
ds_read_b128 a[136:139], v50 offset:4096
buffer_load_dwordx4 v44, s[80:83], s55 offen offset:0 lds
v_cvt_pk_bf16_f32 v73, v74, v75
v_mfma_f32_16x16x32_bf16 v[140:143], a[204:207], a[144:147], v[140:143]
ds_read_b128 a[140:143], v50 offset:6144
v_cvt_pk_bf16_f32 v74, v76, v77
v_mfma_f32_16x16x32_bf16 v[172:175], a[204:207], a[148:151], v[172:175]
ds_read_b64_tr_b16 a[164:165], v61 offset:0
ds_read_b64_tr_b16 a[166:167], v61 offset:2048
v_cvt_pk_bf16_f32 v75, v78, v79
v_mfma_f32_16x16x32_bf16 v[204:207], a[204:207], a[152:155], v[204:207]
ds_read_b64_tr_b16 a[168:169], v62 offset:0
buffer_load_dwordx4 v45, s[80:83], s55 offen offset:1024 lds
buffer_store_dwordx4 v[72:75], v14, s[36:39], s41 offen offset:2048 sc1
v_mfma_f32_16x16x32_bf16 v[236:239], a[204:207], a[156:159], v[236:239]
ds_read_b64_tr_b16 a[170:171], v62 offset:2048
v_mfma_f32_16x16x32_bf16 v[144:147], a[208:211], a[144:147], v[144:147]
ds_read_b64_tr_b16 a[172:173], v63 offset:0
buffer_load_dwordx4 v46, s[80:83], s55 offen offset:2048 lds
v_accvgpr_read_b32 v80, a56
v_mfma_f32_16x16x32_bf16 v[176:179], a[208:211], a[148:151], v[176:179]
ds_read_b64_tr_b16 a[174:175], v63 offset:2048
v_accvgpr_read_b32 v81, a57
v_mfma_f32_16x16x32_bf16 v[208:211], a[208:211], a[152:155], v[208:211]
ds_read_b64_tr_b16 a[176:177], v64 offset:0
v_accvgpr_read_b32 v82, a58
v_mfma_f32_16x16x32_bf16 v[240:243], a[208:211], a[156:159], v[240:243]
ds_read_b64_tr_b16 a[178:179], v64 offset:2048
ds_read_b64_tr_b16 a[180:181], v65 offset:0
v_accvgpr_read_b32 v83, a59
v_mfma_f32_16x16x32_bf16 v[148:151], a[212:215], a[144:147], v[148:151]
ds_read_b64_tr_b16 a[182:183], v65 offset:2048
buffer_load_dwordx4 v47, s[80:83], s55 offen offset:3072 lds
v_accvgpr_read_b32 v84, a60
v_mfma_f32_16x16x32_bf16 v[180:183], a[212:215], a[148:151], v[180:183]
ds_read_b64_tr_b16 a[184:185], v66 offset:0
s_add_u32 s57, s57, 16384
v_accvgpr_read_b32 v85, a61
v_mfma_f32_16x16x32_bf16 v[212:215], a[212:215], a[152:155], v[212:215]
ds_read_b64_tr_b16 a[186:187], v66 offset:2048
s_and_b32 s57, s57, 0xffff
v_accvgpr_read_b32 v86, a62
v_mfma_f32_16x16x32_bf16 v[244:247], a[212:215], a[156:159], v[244:247]
ds_read_b64_tr_b16 a[188:189], v67 offset:0
s_add_u32 s55, s55, 262144
v_mfma_f32_16x16x32_bf16 v[152:155], a[216:219], a[144:147], v[152:155]
ds_read_b64_tr_b16 a[190:191], v67 offset:2048
s_mov_b32 m0, s56
v_accvgpr_read_b32 v87, a63
v_mfma_f32_16x16x32_bf16 v[184:187], a[216:219], a[148:151], v[184:187]
buffer_load_dwordx4 v40, s[76:79], s54 offen offset:0 lds
buffer_load_dwordx4 v41, s[76:79], s54 offen offset:1024 lds
v_cvt_pk_bf16_f32 v80, v80, v81
v_mfma_f32_16x16x32_bf16 v[216:219], a[216:219], a[152:155], v[216:219]
v_cvt_pk_bf16_f32 v81, v82, v83
v_mfma_f32_16x16x32_bf16 v[248:251], a[216:219], a[156:159], v[248:251]
v_cvt_pk_bf16_f32 v82, v84, v85
v_mfma_f32_16x16x32_bf16 v[156:159], a[220:223], a[144:147], v[156:159]
v_cvt_pk_bf16_f32 v83, v86, v87
v_mfma_f32_16x16x32_bf16 v[188:191], a[220:223], a[148:151], v[188:191]
buffer_store_dwordx4 v[80:83], v14, s[36:39], s41 offen offset:3072 sc1
v_mfma_f32_16x16x32_bf16 v[220:223], a[220:223], a[152:155], v[220:223]
v_mfma_f32_16x16x32_bf16 v[252:255], a[220:223], a[156:159], v[252:255]
s_waitcnt lgkmcnt(0)
v_mfma_f32_16x16x32_bf16 v[128:131], a[160:163], a[128:131], v[128:131]
s_add_u32 s59, s59, 16384
s_and_b32 s59, s59, 0xffff
v_mfma_f32_16x16x32_bf16 v[160:163], a[160:163], a[132:135], v[160:163]
v_add_u32 v60, s59, v52
v_add_u32 v61, s59, v53
v_accvgpr_read_b32 v72, a64
v_mfma_f32_16x16x32_bf16 v[192:195], a[160:163], a[136:139], v[192:195]
v_add_u32 v62, s59, v54
v_add_u32 v63, s59, v55
v_accvgpr_read_b32 v73, a65
v_mfma_f32_16x16x32_bf16 v[224:227], a[160:163], a[140:143], v[224:227]
v_add_u32 v64, s59, v56
v_add_u32 v65, s59, v57
v_accvgpr_read_b32 v74, a66
v_mfma_f32_16x16x32_bf16 v[132:135], a[164:167], a[128:131], v[132:135]
v_add_u32 v66, s59, v58
v_add_u32 v67, s59, v59
v_accvgpr_read_b32 v75, a67
v_mfma_f32_16x16x32_bf16 v[164:167], a[164:167], a[132:135], v[164:167]
v_accvgpr_read_b32 v76, a68
v_mfma_f32_16x16x32_bf16 v[196:199], a[164:167], a[136:139], v[196:199]
v_accvgpr_read_b32 v77, a69
v_mfma_f32_16x16x32_bf16 v[228:231], a[164:167], a[140:143], v[228:231]
s_waitcnt vmcnt(18)
s_barrier
v_accvgpr_read_b32 v78, a70
v_mfma_f32_16x16x32_bf16 v[136:139], a[168:171], a[128:131], v[136:139]
ds_read_b64_tr_b16 a[192:193], v60 offset:0
ds_read_b64_tr_b16 a[194:195], v60 offset:2048
v_mfma_f32_16x16x32_bf16 v[168:171], a[168:171], a[132:135], v[168:171]
ds_read_b128 a[144:147], v51 offset:0
s_add_u32 m0, s57, s63
v_accvgpr_read_b32 v79, a71
v_mfma_f32_16x16x32_bf16 v[200:203], a[168:171], a[136:139], v[200:203]
ds_read_b128 a[148:151], v51 offset:2048
buffer_load_dwordx4 v44, s[80:83], s55 offen offset:0 lds
v_cvt_pk_bf16_f32 v72, v72, v73
v_mfma_f32_16x16x32_bf16 v[232:235], a[168:171], a[140:143], v[232:235]
ds_read_b128 a[152:155], v51 offset:4096
v_cvt_pk_bf16_f32 v73, v74, v75
v_mfma_f32_16x16x32_bf16 v[140:143], a[172:175], a[128:131], v[140:143]
ds_read_b128 a[156:159], v51 offset:6144
v_cvt_pk_bf16_f32 v74, v76, v77
v_mfma_f32_16x16x32_bf16 v[172:175], a[172:175], a[132:135], v[172:175]
ds_read_b64_tr_b16 a[196:197], v61 offset:0
ds_read_b64_tr_b16 a[198:199], v61 offset:2048
v_cvt_pk_bf16_f32 v75, v78, v79
v_mfma_f32_16x16x32_bf16 v[204:207], a[172:175], a[136:139], v[204:207]
ds_read_b64_tr_b16 a[200:201], v62 offset:0
buffer_load_dwordx4 v45, s[80:83], s55 offen offset:1024 lds
buffer_store_dwordx4 v[72:75], v14, s[36:39], s42 offen offset:0 sc1
v_mfma_f32_16x16x32_bf16 v[236:239], a[172:175], a[140:143], v[236:239]
ds_read_b64_tr_b16 a[202:203], v62 offset:2048
v_mfma_f32_16x16x32_bf16 v[144:147], a[176:179], a[128:131], v[144:147]
ds_read_b64_tr_b16 a[204:205], v63 offset:0
buffer_load_dwordx4 v46, s[80:83], s55 offen offset:2048 lds
v_accvgpr_read_b32 v80, a72
v_mfma_f32_16x16x32_bf16 v[176:179], a[176:179], a[132:135], v[176:179]
ds_read_b64_tr_b16 a[206:207], v63 offset:2048
v_accvgpr_read_b32 v81, a73
v_mfma_f32_16x16x32_bf16 v[208:211], a[176:179], a[136:139], v[208:211]
ds_read_b64_tr_b16 a[208:209], v64 offset:0
v_accvgpr_read_b32 v82, a74
v_mfma_f32_16x16x32_bf16 v[240:243], a[176:179], a[140:143], v[240:243]
ds_read_b64_tr_b16 a[210:211], v64 offset:2048
ds_read_b64_tr_b16 a[212:213], v65 offset:0
v_accvgpr_read_b32 v83, a75
v_mfma_f32_16x16x32_bf16 v[148:151], a[180:183], a[128:131], v[148:151]
ds_read_b64_tr_b16 a[214:215], v65 offset:2048
buffer_load_dwordx4 v47, s[80:83], s55 offen offset:3072 lds
v_accvgpr_read_b32 v84, a76
v_mfma_f32_16x16x32_bf16 v[180:183], a[180:183], a[132:135], v[180:183]
ds_read_b64_tr_b16 a[216:217], v66 offset:0
s_add_u32 s57, s57, 16384
v_accvgpr_read_b32 v85, a77
v_mfma_f32_16x16x32_bf16 v[212:215], a[180:183], a[136:139], v[212:215]
ds_read_b64_tr_b16 a[218:219], v66 offset:2048
s_and_b32 s57, s57, 0xffff
v_accvgpr_read_b32 v86, a78
v_mfma_f32_16x16x32_bf16 v[244:247], a[180:183], a[140:143], v[244:247]
ds_read_b64_tr_b16 a[220:221], v67 offset:0
s_add_u32 s55, s55, 262144
v_mfma_f32_16x16x32_bf16 v[152:155], a[184:187], a[128:131], v[152:155]
ds_read_b64_tr_b16 a[222:223], v67 offset:2048
s_mov_b32 m0, s56
v_accvgpr_read_b32 v87, a79
v_mfma_f32_16x16x32_bf16 v[184:187], a[184:187], a[132:135], v[184:187]
buffer_load_dwordx4 v42, s[76:79], s54 offen offset:2048 lds
buffer_load_dwordx4 v43, s[76:79], s54 offen offset:3072 lds
v_cvt_pk_bf16_f32 v80, v80, v81
v_mfma_f32_16x16x32_bf16 v[216:219], a[184:187], a[136:139], v[216:219]
s_add_u32 s56, s56, 16384
s_cmp_ge_u32 s56, s44
v_cvt_pk_bf16_f32 v81, v82, v83
v_mfma_f32_16x16x32_bf16 v[248:251], a[184:187], a[140:143], v[248:251]
s_cselect_b32 s50, 49152, 0
s_sub_u32 s56, s56, s50
v_cvt_pk_bf16_f32 v82, v84, v85
v_mfma_f32_16x16x32_bf16 v[156:159], a[188:191], a[128:131], v[156:159]
s_add_u32 s54, s54, 128
v_cvt_pk_bf16_f32 v83, v86, v87
v_mfma_f32_16x16x32_bf16 v[188:191], a[188:191], a[132:135], v[188:191]
buffer_store_dwordx4 v[80:83], v14, s[36:39], s42 offen offset:1024 sc1
v_mfma_f32_16x16x32_bf16 v[220:223], a[188:191], a[136:139], v[220:223]
v_mfma_f32_16x16x32_bf16 v[252:255], a[188:191], a[140:143], v[252:255]
s_waitcnt lgkmcnt(0)
v_mfma_f32_16x16x32_bf16 v[128:131], a[192:195], a[144:147], v[128:131]
s_add_u32 s59, s59, 16384
s_and_b32 s59, s59, 0xffff
v_mfma_f32_16x16x32_bf16 v[160:163], a[192:195], a[148:151], v[160:163]
v_add_u32 v60, s59, v52
v_add_u32 v61, s59, v53
v_accvgpr_read_b32 v72, a80
v_mfma_f32_16x16x32_bf16 v[192:195], a[192:195], a[152:155], v[192:195]
v_add_u32 v62, s59, v54
v_add_u32 v63, s59, v55
v_accvgpr_read_b32 v73, a81
v_mfma_f32_16x16x32_bf16 v[224:227], a[192:195], a[156:159], v[224:227]
v_add_u32 v64, s59, v56
v_add_u32 v65, s59, v57
v_accvgpr_read_b32 v74, a82
v_mfma_f32_16x16x32_bf16 v[132:135], a[196:199], a[144:147], v[132:135]
v_add_u32 v66, s59, v58
v_add_u32 v67, s59, v59
v_accvgpr_read_b32 v75, a83
v_mfma_f32_16x16x32_bf16 v[164:167], a[196:199], a[148:151], v[164:167]
s_add_u32 s58, s58, 16384
s_cmp_ge_u32 s58, 49152
v_accvgpr_read_b32 v76, a84
v_mfma_f32_16x16x32_bf16 v[196:199], a[196:199], a[152:155], v[196:199]
s_cselect_b32 s58, 0, s58
v_add_u32 v50, s58, v48
v_accvgpr_read_b32 v77, a85
v_mfma_f32_16x16x32_bf16 v[228:231], a[196:199], a[156:159], v[228:231]
v_add_u32 v51, s58, v49
s_waitcnt vmcnt(16)
v_accvgpr_read_b32 v78, a86
v_mfma_f32_16x16x32_bf16 v[136:139], a[200:203], a[144:147], v[136:139]
s_barrier
ds_read_b64_tr_b16 a[160:161], v60 offset:0
v_mfma_f32_16x16x32_bf16 v[168:171], a[200:203], a[148:151], v[168:171]
ds_read_b64_tr_b16 a[162:163], v60 offset:2048
ds_read_b128 a[128:131], v50 offset:0
v_accvgpr_read_b32 v79, a87
v_mfma_f32_16x16x32_bf16 v[200:203], a[200:203], a[152:155], v[200:203]
ds_read_b128 a[132:135], v50 offset:2048
s_add_u32 m0, s57, s63
v_cvt_pk_bf16_f32 v72, v72, v73
v_mfma_f32_16x16x32_bf16 v[232:235], a[200:203], a[156:159], v[232:235]
ds_read_b128 a[136:139], v50 offset:4096
buffer_load_dwordx4 v44, s[80:83], s55 offen offset:0 lds
v_cvt_pk_bf16_f32 v73, v74, v75
v_mfma_f32_16x16x32_bf16 v[140:143], a[204:207], a[144:147], v[140:143]
ds_read_b128 a[140:143], v50 offset:6144
v_cvt_pk_bf16_f32 v74, v76, v77
v_mfma_f32_16x16x32_bf16 v[172:175], a[204:207], a[148:151], v[172:175]
ds_read_b64_tr_b16 a[164:165], v61 offset:0
ds_read_b64_tr_b16 a[166:167], v61 offset:2048
v_cvt_pk_bf16_f32 v75, v78, v79
v_mfma_f32_16x16x32_bf16 v[204:207], a[204:207], a[152:155], v[204:207]
ds_read_b64_tr_b16 a[168:169], v62 offset:0
buffer_load_dwordx4 v45, s[80:83], s55 offen offset:1024 lds
buffer_store_dwordx4 v[72:75], v14, s[36:39], s42 offen offset:2048 sc1
v_mfma_f32_16x16x32_bf16 v[236:239], a[204:207], a[156:159], v[236:239]
ds_read_b64_tr_b16 a[170:171], v62 offset:2048
v_mfma_f32_16x16x32_bf16 v[144:147], a[208:211], a[144:147], v[144:147]
ds_read_b64_tr_b16 a[172:173], v63 offset:0
buffer_load_dwordx4 v46, s[80:83], s55 offen offset:2048 lds
v_accvgpr_read_b32 v80, a88
v_mfma_f32_16x16x32_bf16 v[176:179], a[208:211], a[148:151], v[176:179]
ds_read_b64_tr_b16 a[174:175], v63 offset:2048
v_accvgpr_read_b32 v81, a89
v_mfma_f32_16x16x32_bf16 v[208:211], a[208:211], a[152:155], v[208:211]
ds_read_b64_tr_b16 a[176:177], v64 offset:0
v_accvgpr_read_b32 v82, a90
v_mfma_f32_16x16x32_bf16 v[240:243], a[208:211], a[156:159], v[240:243]
ds_read_b64_tr_b16 a[178:179], v64 offset:2048
ds_read_b64_tr_b16 a[180:181], v65 offset:0
v_accvgpr_read_b32 v83, a91
v_mfma_f32_16x16x32_bf16 v[148:151], a[212:215], a[144:147], v[148:151]
ds_read_b64_tr_b16 a[182:183], v65 offset:2048
buffer_load_dwordx4 v47, s[80:83], s55 offen offset:3072 lds
v_accvgpr_read_b32 v84, a92
v_mfma_f32_16x16x32_bf16 v[180:183], a[212:215], a[148:151], v[180:183]
ds_read_b64_tr_b16 a[184:185], v66 offset:0
s_add_u32 s57, s57, 16384
v_accvgpr_read_b32 v85, a93
v_mfma_f32_16x16x32_bf16 v[212:215], a[212:215], a[152:155], v[212:215]
ds_read_b64_tr_b16 a[186:187], v66 offset:2048
s_and_b32 s57, s57, 0xffff
v_accvgpr_read_b32 v86, a94
v_mfma_f32_16x16x32_bf16 v[244:247], a[212:215], a[156:159], v[244:247]
ds_read_b64_tr_b16 a[188:189], v67 offset:0
s_add_u32 s55, s55, 262144
v_mfma_f32_16x16x32_bf16 v[152:155], a[216:219], a[144:147], v[152:155]
ds_read_b64_tr_b16 a[190:191], v67 offset:2048
s_mov_b32 m0, s56
v_accvgpr_read_b32 v87, a95
v_mfma_f32_16x16x32_bf16 v[184:187], a[216:219], a[148:151], v[184:187]
buffer_load_dwordx4 v40, s[76:79], s54 offen offset:0 lds
buffer_load_dwordx4 v41, s[76:79], s54 offen offset:1024 lds
v_cvt_pk_bf16_f32 v80, v80, v81
v_mfma_f32_16x16x32_bf16 v[216:219], a[216:219], a[152:155], v[216:219]
v_cvt_pk_bf16_f32 v81, v82, v83
v_mfma_f32_16x16x32_bf16 v[248:251], a[216:219], a[156:159], v[248:251]
v_cvt_pk_bf16_f32 v82, v84, v85
v_mfma_f32_16x16x32_bf16 v[156:159], a[220:223], a[144:147], v[156:159]
v_cvt_pk_bf16_f32 v83, v86, v87
v_mfma_f32_16x16x32_bf16 v[188:191], a[220:223], a[148:151], v[188:191]
buffer_store_dwordx4 v[80:83], v14, s[36:39], s42 offen offset:3072 sc1
v_mfma_f32_16x16x32_bf16 v[220:223], a[220:223], a[152:155], v[220:223]
v_mfma_f32_16x16x32_bf16 v[252:255], a[220:223], a[156:159], v[252:255]
s_waitcnt lgkmcnt(0)
v_mfma_f32_16x16x32_bf16 v[128:131], a[160:163], a[128:131], v[128:131]
s_add_u32 s59, s59, 16384
s_and_b32 s59, s59, 0xffff
v_mfma_f32_16x16x32_bf16 v[160:163], a[160:163], a[132:135], v[160:163]
v_add_u32 v60, s59, v52
v_add_u32 v61, s59, v53
ds_write_b128 v121, v[104:107] offset:0
v_mfma_f32_16x16x32_bf16 v[192:195], a[160:163], a[136:139], v[192:195]
v_add_u32 v62, s59, v54
v_add_u32 v63, s59, v55
ds_write_b128 v121, v[108:111] offset:1152
v_mfma_f32_16x16x32_bf16 v[224:227], a[160:163], a[140:143], v[224:227]
v_add_u32 v64, s59, v56
v_add_u32 v65, s59, v57
ds_write_b128 v121, v[112:115] offset:2304
v_mfma_f32_16x16x32_bf16 v[132:135], a[164:167], a[128:131], v[132:135]
v_add_u32 v66, s59, v58
v_add_u32 v67, s59, v59
ds_write_b128 v121, v[116:119] offset:3456
v_mfma_f32_16x16x32_bf16 v[164:167], a[164:167], a[132:135], v[164:167]
v_mfma_f32_16x16x32_bf16 v[196:199], a[164:167], a[136:139], v[196:199]
v_accvgpr_read_b32 v72, a96
v_accvgpr_read_b32 v73, a97
v_mfma_f32_16x16x32_bf16 v[228:231], a[164:167], a[140:143], v[228:231]
s_waitcnt vmcnt(18)
s_barrier
v_accvgpr_read_b32 v74, a98
v_mfma_f32_16x16x32_bf16 v[136:139], a[168:171], a[128:131], v[136:139]
ds_read_b64_tr_b16 a[192:193], v60 offset:0
ds_read_b64_tr_b16 a[194:195], v60 offset:2048
v_accvgpr_read_b32 v75, a99
v_mfma_f32_16x16x32_bf16 v[168:171], a[168:171], a[132:135], v[168:171]
ds_read_b128 a[144:147], v51 offset:0
s_add_u32 m0, s57, s63
v_accvgpr_read_b32 v76, a100
v_mfma_f32_16x16x32_bf16 v[200:203], a[168:171], a[136:139], v[200:203]
ds_read_b128 a[148:151], v51 offset:2048
buffer_load_dwordx4 v44, s[80:83], s55 offen offset:0 lds
v_accvgpr_read_b32 v77, a101
v_mfma_f32_16x16x32_bf16 v[232:235], a[168:171], a[140:143], v[232:235]
ds_read_b128 a[152:155], v51 offset:4096
v_accvgpr_read_b32 v78, a102
v_mfma_f32_16x16x32_bf16 v[140:143], a[172:175], a[128:131], v[140:143]
ds_read_b128 a[156:159], v51 offset:6144
v_accvgpr_read_b32 v79, a103
v_mfma_f32_16x16x32_bf16 v[172:175], a[172:175], a[132:135], v[172:175]
ds_read_b64_tr_b16 a[196:197], v61 offset:0
ds_read_b64_tr_b16 a[198:199], v61 offset:2048
v_cvt_pk_bf16_f32 v72, v72, v73
v_mfma_f32_16x16x32_bf16 v[204:207], a[172:175], a[136:139], v[204:207]
ds_read_b64_tr_b16 a[200:201], v62 offset:0
buffer_load_dwordx4 v45, s[80:83], s55 offen offset:1024 lds
v_cvt_pk_bf16_f32 v73, v74, v75
v_mfma_f32_16x16x32_bf16 v[236:239], a[172:175], a[140:143], v[236:239]
ds_read_b64_tr_b16 a[202:203], v62 offset:2048
v_cvt_pk_bf16_f32 v74, v76, v77
v_mfma_f32_16x16x32_bf16 v[144:147], a[176:179], a[128:131], v[144:147]
ds_read_b64_tr_b16 a[204:205], v63 offset:0
buffer_load_dwordx4 v46, s[80:83], s55 offen offset:2048 lds
v_cvt_pk_bf16_f32 v75, v78, v79
v_mfma_f32_16x16x32_bf16 v[176:179], a[176:179], a[132:135], v[176:179]
ds_read_b64_tr_b16 a[206:207], v63 offset:2048
buffer_store_dwordx4 v[72:75], v14, s[36:39], s43 offen offset:0 sc1
v_mfma_f32_16x16x32_bf16 v[208:211], a[176:179], a[136:139], v[208:211]
ds_read_b64_tr_b16 a[208:209], v64 offset:0
v_accvgpr_read_b32 v80, a104
v_mfma_f32_16x16x32_bf16 v[240:243], a[176:179], a[140:143], v[240:243]
ds_read_b64_tr_b16 a[210:211], v64 offset:2048
ds_read_b64_tr_b16 a[212:213], v65 offset:0
v_accvgpr_read_b32 v81, a105
v_mfma_f32_16x16x32_bf16 v[148:151], a[180:183], a[128:131], v[148:151]
ds_read_b64_tr_b16 a[214:215], v65 offset:2048
buffer_load_dwordx4 v47, s[80:83], s55 offen offset:3072 lds
v_accvgpr_read_b32 v82, a106
v_mfma_f32_16x16x32_bf16 v[180:183], a[180:183], a[132:135], v[180:183]
ds_read_b64_tr_b16 a[216:217], v66 offset:0
s_add_u32 s57, s57, 16384
v_accvgpr_read_b32 v83, a107
v_mfma_f32_16x16x32_bf16 v[212:215], a[180:183], a[136:139], v[212:215]
ds_read_b64_tr_b16 a[218:219], v66 offset:2048
s_and_b32 s57, s57, 0xffff
v_accvgpr_read_b32 v84, a108
v_mfma_f32_16x16x32_bf16 v[244:247], a[180:183], a[140:143], v[244:247]
ds_read_b64_tr_b16 a[220:221], v67 offset:0
s_add_u32 s55, s55, 262144
v_accvgpr_read_b32 v85, a109
v_mfma_f32_16x16x32_bf16 v[152:155], a[184:187], a[128:131], v[152:155]
ds_read_b64_tr_b16 a[222:223], v67 offset:2048
s_mov_b32 m0, s56
v_accvgpr_read_b32 v86, a110
v_mfma_f32_16x16x32_bf16 v[184:187], a[184:187], a[132:135], v[184:187]
buffer_load_dwordx4 v42, s[76:79], s54 offen offset:2048 lds
buffer_load_dwordx4 v43, s[76:79], s54 offen offset:3072 lds
v_accvgpr_read_b32 v87, a111
v_mfma_f32_16x16x32_bf16 v[216:219], a[184:187], a[136:139], v[216:219]
s_add_u32 s56, s56, 16384
s_cmp_ge_u32 s56, s44
v_cvt_pk_bf16_f32 v80, v80, v81
v_mfma_f32_16x16x32_bf16 v[248:251], a[184:187], a[140:143], v[248:251]
s_cselect_b32 s50, 49152, 0
s_sub_u32 s56, s56, s50
v_cvt_pk_bf16_f32 v81, v82, v83
v_mfma_f32_16x16x32_bf16 v[156:159], a[188:191], a[128:131], v[156:159]
s_add_u32 s54, s54, 128
v_cvt_pk_bf16_f32 v82, v84, v85
v_mfma_f32_16x16x32_bf16 v[188:191], a[188:191], a[132:135], v[188:191]
v_cvt_pk_bf16_f32 v83, v86, v87
v_mfma_f32_16x16x32_bf16 v[220:223], a[188:191], a[136:139], v[220:223]
buffer_store_dwordx4 v[80:83], v14, s[36:39], s43 offen offset:1024 sc1
v_mfma_f32_16x16x32_bf16 v[252:255], a[188:191], a[140:143], v[252:255]
s_waitcnt lgkmcnt(0)
v_mfma_f32_16x16x32_bf16 v[128:131], a[192:195], a[144:147], v[128:131]
s_add_u32 s59, s59, 16384
s_and_b32 s59, s59, 0xffff
v_mfma_f32_16x16x32_bf16 v[160:163], a[192:195], a[148:151], v[160:163]
v_add_u32 v60, s59, v52
v_add_u32 v61, s59, v53
ds_read_b64_tr_b16 v[88:89], v122 offset:0
v_mfma_f32_16x16x32_bf16 v[192:195], a[192:195], a[152:155], v[192:195]
v_add_u32 v62, s59, v54
v_add_u32 v63, s59, v55
ds_read_b64_tr_b16 v[90:91], v122 offset:2304
v_mfma_f32_16x16x32_bf16 v[224:227], a[192:195], a[156:159], v[224:227]
v_add_u32 v64, s59, v56
v_add_u32 v65, s59, v57
ds_read_b64_tr_b16 v[92:93], v122 offset:32
v_mfma_f32_16x16x32_bf16 v[132:135], a[196:199], a[144:147], v[132:135]
v_add_u32 v66, s59, v58
v_add_u32 v67, s59, v59
ds_read_b64_tr_b16 v[94:95], v122 offset:2336
v_mfma_f32_16x16x32_bf16 v[164:167], a[196:199], a[148:151], v[164:167]
s_add_u32 s58, s58, 16384
s_cmp_ge_u32 s58, 49152
ds_read_b64_tr_b16 v[96:97], v122 offset:64
v_mfma_f32_16x16x32_bf16 v[196:199], a[196:199], a[152:155], v[196:199]
s_cselect_b32 s58, 0, s58
v_add_u32 v50, s58, v48
ds_read_b64_tr_b16 v[98:99], v122 offset:2368
v_mfma_f32_16x16x32_bf16 v[228:231], a[196:199], a[156:159], v[228:231]
v_add_u32 v51, s58, v49
s_waitcnt vmcnt(16)
ds_read_b64_tr_b16 v[100:101], v122 offset:96
v_mfma_f32_16x16x32_bf16 v[136:139], a[200:203], a[144:147], v[136:139]
s_barrier
ds_read_b64_tr_b16 a[160:161], v60 offset:0
ds_read_b64_tr_b16 v[102:103], v122 offset:2400
v_mfma_f32_16x16x32_bf16 v[168:171], a[200:203], a[148:151], v[168:171]
ds_read_b64_tr_b16 a[162:163], v60 offset:2048
ds_read_b128 a[128:131], v50 offset:0
v_accvgpr_read_b32 v72, a112
v_mfma_f32_16x16x32_bf16 v[200:203], a[200:203], a[152:155], v[200:203]
ds_read_b128 a[132:135], v50 offset:2048
s_add_u32 m0, s57, s63
v_accvgpr_read_b32 v73, a113
v_mfma_f32_16x16x32_bf16 v[232:235], a[200:203], a[156:159], v[232:235]
ds_read_b128 a[136:139], v50 offset:4096
buffer_load_dwordx4 v44, s[80:83], s55 offen offset:0 lds
v_accvgpr_read_b32 v74, a114
v_mfma_f32_16x16x32_bf16 v[140:143], a[204:207], a[144:147], v[140:143]
ds_read_b128 a[140:143], v50 offset:6144
v_accvgpr_read_b32 v75, a115
v_accvgpr_read_b32 v76, a116
v_mfma_f32_16x16x32_bf16 v[172:175], a[204:207], a[148:151], v[172:175]
ds_read_b64_tr_b16 a[164:165], v61 offset:0
ds_read_b64_tr_b16 a[166:167], v61 offset:2048
v_accvgpr_read_b32 v77, a117
v_mfma_f32_16x16x32_bf16 v[204:207], a[204:207], a[152:155], v[204:207]
ds_read_b64_tr_b16 a[168:169], v62 offset:0
buffer_load_dwordx4 v45, s[80:83], s55 offen offset:1024 lds
v_accvgpr_read_b32 v78, a118
v_mfma_f32_16x16x32_bf16 v[236:239], a[204:207], a[156:159], v[236:239]
ds_read_b64_tr_b16 a[170:171], v62 offset:2048
v_accvgpr_read_b32 v79, a119
v_cvt_pk_bf16_f32 v72, v72, v73
v_mfma_f32_16x16x32_bf16 v[144:147], a[208:211], a[144:147], v[144:147]
ds_read_b64_tr_b16 a[172:173], v63 offset:0
buffer_load_dwordx4 v46, s[80:83], s55 offen offset:2048 lds
v_cvt_pk_bf16_f32 v73, v74, v75
v_mfma_f32_16x16x32_bf16 v[176:179], a[208:211], a[148:151], v[176:179]
ds_read_b64_tr_b16 a[174:175], v63 offset:2048
v_cvt_pk_bf16_f32 v74, v76, v77
v_cvt_pk_bf16_f32 v75, v78, v79
v_mfma_f32_16x16x32_bf16 v[208:211], a[208:211], a[152:155], v[208:211]
ds_read_b64_tr_b16 a[176:177], v64 offset:0
buffer_store_dwordx4 v[72:75], v14, s[36:39], s43 offen offset:2048 sc1
v_accvgpr_read_b32 v80, a120
v_mfma_f32_16x16x32_bf16 v[240:243], a[208:211], a[156:159], v[240:243]
ds_read_b64_tr_b16 a[178:179], v64 offset:2048
ds_read_b64_tr_b16 a[180:181], v65 offset:0
v_accvgpr_read_b32 v81, a121
v_mfma_f32_16x16x32_bf16 v[148:151], a[212:215], a[144:147], v[148:151]
ds_read_b64_tr_b16 a[182:183], v65 offset:2048
buffer_load_dwordx4 v47, s[80:83], s55 offen offset:3072 lds
v_accvgpr_read_b32 v82, a122
v_mfma_f32_16x16x32_bf16 v[180:183], a[212:215], a[148:151], v[180:183]
ds_read_b64_tr_b16 a[184:185], v66 offset:0
s_add_u32 s57, s57, 16384
v_accvgpr_read_b32 v83, a123
v_mfma_f32_16x16x32_bf16 v[212:215], a[212:215], a[152:155], v[212:215]
ds_read_b64_tr_b16 a[186:187], v66 offset:2048
s_and_b32 s57, s57, 0xffff
v_accvgpr_read_b32 v84, a124
v_mfma_f32_16x16x32_bf16 v[244:247], a[212:215], a[156:159], v[244:247]
ds_read_b64_tr_b16 a[188:189], v67 offset:0
s_add_u32 s55, s55, 262144
v_accvgpr_read_b32 v85, a125
v_mfma_f32_16x16x32_bf16 v[152:155], a[216:219], a[144:147], v[152:155]
ds_read_b64_tr_b16 a[190:191], v67 offset:2048
s_mov_b32 m0, s56
v_accvgpr_read_b32 v86, a126
v_mfma_f32_16x16x32_bf16 v[184:187], a[216:219], a[148:151], v[184:187]
buffer_load_dwordx4 v40, s[76:79], s54 offen offset:0 lds
buffer_load_dwordx4 v41, s[76:79], s54 offen offset:1024 lds
v_accvgpr_read_b32 v87, a127
v_mfma_f32_16x16x32_bf16 v[216:219], a[216:219], a[152:155], v[216:219]
v_cvt_pk_bf16_f32 v80, v80, v81
v_mfma_f32_16x16x32_bf16 v[248:251], a[216:219], a[156:159], v[248:251]
v_cvt_pk_bf16_f32 v81, v82, v83
v_mfma_f32_16x16x32_bf16 v[156:159], a[220:223], a[144:147], v[156:159]
v_cvt_pk_bf16_f32 v82, v84, v85
v_mfma_f32_16x16x32_bf16 v[188:191], a[220:223], a[148:151], v[188:191]
v_cvt_pk_bf16_f32 v83, v86, v87
v_mfma_f32_16x16x32_bf16 v[220:223], a[220:223], a[152:155], v[220:223]
buffer_store_dwordx4 v[80:83], v14, s[36:39], s43 offen offset:3072 sc1
v_mfma_f32_16x16x32_bf16 v[252:255], a[220:223], a[156:159], v[252:255]
s_waitcnt lgkmcnt(0)
v_mfma_f32_16x16x32_bf16 v[128:131], a[160:163], a[128:131], v[128:131]
s_add_u32 s59, s59, 16384
s_and_b32 s59, s59, 0xffff
v_mfma_f32_16x16x32_bf16 v[160:163], a[160:163], a[132:135], v[160:163]
v_add_u32 v60, s59, v52
v_add_u32 v61, s59, v53
v_mfma_f32_16x16x32_bf16 v[192:195], a[160:163], a[136:139], v[192:195]
v_add_u32 v62, s59, v54
v_add_u32 v63, s59, v55
v_mfma_f32_16x16x32_bf16 v[224:227], a[160:163], a[140:143], v[224:227]
v_add_u32 v64, s59, v56
v_add_u32 v65, s59, v57
v_mfma_f32_16x16x32_bf16 v[132:135], a[164:167], a[128:131], v[132:135]
v_add_u32 v66, s59, v58
v_add_u32 v67, s59, v59
v_mfma_f32_16x16x32_bf16 v[164:167], a[164:167], a[132:135], v[164:167]
v_mfma_f32_16x16x32_bf16 v[196:199], a[164:167], a[136:139], v[196:199]
v_mfma_f32_16x16x32_bf16 v[228:231], a[164:167], a[140:143], v[228:231]
s_waitcnt vmcnt(18)
s_barrier
v_mfma_f32_16x16x32_bf16 v[136:139], a[168:171], a[128:131], v[136:139]
ds_read_b64_tr_b16 a[192:193], v60 offset:0
ds_read_b64_tr_b16 a[194:195], v60 offset:2048
v_mfma_f32_16x16x32_bf16 v[168:171], a[168:171], a[132:135], v[168:171]
ds_read_b128 a[144:147], v51 offset:0
s_add_u32 m0, s57, s63
v_mfma_f32_16x16x32_bf16 v[200:203], a[168:171], a[136:139], v[200:203]
ds_read_b128 a[148:151], v51 offset:2048
buffer_load_dwordx4 v44, s[80:83], s55 offen offset:0 lds
v_mfma_f32_16x16x32_bf16 v[232:235], a[168:171], a[140:143], v[232:235]
ds_read_b128 a[152:155], v51 offset:4096
v_mfma_f32_16x16x32_bf16 v[140:143], a[172:175], a[128:131], v[140:143]
ds_read_b128 a[156:159], v51 offset:6144
v_mfma_f32_16x16x32_bf16 v[172:175], a[172:175], a[132:135], v[172:175]
ds_read_b64_tr_b16 a[196:197], v61 offset:0
ds_read_b64_tr_b16 a[198:199], v61 offset:2048
v_mfma_f32_16x16x32_bf16 v[204:207], a[172:175], a[136:139], v[204:207]
ds_read_b64_tr_b16 a[200:201], v62 offset:0
buffer_load_dwordx4 v45, s[80:83], s55 offen offset:1024 lds
v_mfma_f32_16x16x32_bf16 v[236:239], a[172:175], a[140:143], v[236:239]
ds_read_b64_tr_b16 a[202:203], v62 offset:2048
v_mfma_f32_16x16x32_bf16 v[144:147], a[176:179], a[128:131], v[144:147]
ds_read_b64_tr_b16 a[204:205], v63 offset:0
buffer_load_dwordx4 v46, s[80:83], s55 offen offset:2048 lds
v_mfma_f32_16x16x32_bf16 v[176:179], a[176:179], a[132:135], v[176:179]
ds_read_b64_tr_b16 a[206:207], v63 offset:2048
v_mfma_f32_16x16x32_bf16 v[208:211], a[176:179], a[136:139], v[208:211]
ds_read_b64_tr_b16 a[208:209], v64 offset:0
v_mfma_f32_16x16x32_bf16 v[240:243], a[176:179], a[140:143], v[240:243]
ds_read_b64_tr_b16 a[210:211], v64 offset:2048
ds_read_b64_tr_b16 a[212:213], v65 offset:0
v_mfma_f32_16x16x32_bf16 v[148:151], a[180:183], a[128:131], v[148:151]
ds_read_b64_tr_b16 a[214:215], v65 offset:2048
buffer_load_dwordx4 v47, s[80:83], s55 offen offset:3072 lds
buffer_store_dwordx4 v[88:91], v14, s[36:39], s24 offen offset:0 sc1
v_mfma_f32_16x16x32_bf16 v[180:183], a[180:183], a[132:135], v[180:183]
ds_read_b64_tr_b16 a[216:217], v66 offset:0
s_add_u32 s57, s57, 16384
v_mfma_f32_16x16x32_bf16 v[212:215], a[180:183], a[136:139], v[212:215]
ds_read_b64_tr_b16 a[218:219], v66 offset:2048
s_and_b32 s57, s57, 0xffff
v_mfma_f32_16x16x32_bf16 v[244:247], a[180:183], a[140:143], v[244:247]
ds_read_b64_tr_b16 a[220:221], v67 offset:0
s_add_u32 s55, s55, 262144
buffer_store_dwordx4 v[92:95], v14, s[36:39], s24 offen offset:1024 sc1
v_mfma_f32_16x16x32_bf16 v[152:155], a[184:187], a[128:131], v[152:155]
ds_read_b64_tr_b16 a[222:223], v67 offset:2048
s_mov_b32 m0, s56
v_mfma_f32_16x16x32_bf16 v[184:187], a[184:187], a[132:135], v[184:187]
buffer_load_dwordx4 v42, s[76:79], s54 offen offset:2048 lds
buffer_load_dwordx4 v43, s[76:79], s54 offen offset:3072 lds
v_mfma_f32_16x16x32_bf16 v[216:219], a[184:187], a[136:139], v[216:219]
s_add_u32 s56, s56, 16384
s_cmp_ge_u32 s56, s44
buffer_store_dwordx4 v[96:99], v14, s[36:39], s24 offen offset:2048 sc1
v_mfma_f32_16x16x32_bf16 v[248:251], a[184:187], a[140:143], v[248:251]
s_cselect_b32 s50, 49152, 0
s_sub_u32 s56, s56, s50
v_mfma_f32_16x16x32_bf16 v[156:159], a[188:191], a[128:131], v[156:159]
s_add_u32 s54, s54, 128
v_mfma_f32_16x16x32_bf16 v[188:191], a[188:191], a[132:135], v[188:191]
buffer_store_dwordx4 v[100:103], v14, s[36:39], s24 offen offset:3072 sc1
v_mfma_f32_16x16x32_bf16 v[220:223], a[188:191], a[136:139], v[220:223]
v_mfma_f32_16x16x32_bf16 v[252:255], a[188:191], a[140:143], v[252:255]
v_min_u32 v118, 223, %5
v_mul_u32_u24 v119, 9363, v118
v_lshrrev_b32 v119, 16, v119
v_mul_u32_u24 v120, 7, v119
v_sub_u32 v120, v118, v120
s_and_b32 s50, s66, 7
s_mul_i32 s50, s50, 112
s_lshr_b32 s51, s66, 3
s_mul_i32 s51, s51, 57344
s_add_u32 s51, s51, s50
s_sub_u32 s51, s51, 2688
v_mul_u32_u24 v116, 1792, v119
v_lshl_add_u32 v116, v120, 4, v116
v_add_u32 v116, s51, v116
s_lshl_b32 s50, s50, 2
v_lshlrev_b32 v117, 6, v120
v_add_u32 v117, s50, v117
s_mov_b64 s[24:25], %9
s_and_b32 s25, s25, 0xffff
s_mov_b32 s26, 1835008
s_mov_b32 s27, 0x20000
buffer_load_dwordx4 v[72:75], v116, s[24:27], 0 offen offset:0
buffer_load_dwordx4 v[76:79], v116, s[24:27], 0 offen offset:896
buffer_load_dwordx4 v[80:83], v116, s[24:27], 0 offen offset:1792
buffer_load_dwordx4 v[84:87], v116, s[24:27], 0 offen offset:2688
buffer_load_dwordx4 v[88:91], v116, s[24:27], 0 offen offset:3584
s_mov_b64 s[24:25], %10
s_and_b32 s25, s25, 0xffff
s_mov_b32 s26, 1835008
s_mov_b32 s27, 0x20000
buffer_load_dwordx4 v[92:95], v116, s[24:27], 0 offen offset:2688
buffer_load_dwordx4 v[96:99], v116, s[24:27], 0 offen offset:3584
s_mov_b64 s[24:25], %11
s_and_b32 s25, s25, 0xffff
s_mov_b32 s26, 3584
s_mov_b32 s27, 0x20000
buffer_load_dwordx4 v[100:103], v117, s[24:27], 0 offen offset:0
buffer_load_dwordx4 v[104:107], v117, s[24:27], 0 offen offset:16
buffer_load_dwordx4 v[108:111], v117, s[24:27], 0 offen offset:32
buffer_load_dwordx4 v[112:115], v117, s[24:27], 0 offen offset:48
s_waitcnt lgkmcnt(0)
v_mfma_f32_16x16x32_bf16 v[128:131], a[192:195], a[144:147], v[128:131]
s_add_u32 s59, s59, 16384
s_and_b32 s59, s59, 0xffff
v_mfma_f32_16x16x32_bf16 v[160:163], a[192:195], a[148:151], v[160:163]
v_add_u32 v60, s59, v52
v_add_u32 v61, s59, v53
v_mfma_f32_16x16x32_bf16 v[192:195], a[192:195], a[152:155], v[192:195]
v_add_u32 v62, s59, v54
v_add_u32 v63, s59, v55
v_mfma_f32_16x16x32_bf16 v[224:227], a[192:195], a[156:159], v[224:227]
v_add_u32 v64, s59, v56
v_add_u32 v65, s59, v57
v_mfma_f32_16x16x32_bf16 v[132:135], a[196:199], a[144:147], v[132:135]
v_add_u32 v66, s59, v58
v_add_u32 v67, s59, v59
v_mfma_f32_16x16x32_bf16 v[164:167], a[196:199], a[148:151], v[164:167]
s_add_u32 s58, s58, 16384
s_cmp_ge_u32 s58, 49152
v_mfma_f32_16x16x32_bf16 v[196:199], a[196:199], a[152:155], v[196:199]
s_cselect_b32 s58, 0, s58
v_add_u32 v50, s58, v48
v_mfma_f32_16x16x32_bf16 v[228:231], a[196:199], a[156:159], v[228:231]
v_add_u32 v51, s58, v49
s_waitcnt vmcnt(29)
v_mfma_f32_16x16x32_bf16 v[136:139], a[200:203], a[144:147], v[136:139]
s_barrier
ds_read_b64_tr_b16 a[160:161], v60 offset:0
v_mfma_f32_16x16x32_bf16 v[168:171], a[200:203], a[148:151], v[168:171]
ds_read_b64_tr_b16 a[162:163], v60 offset:2048
ds_read_b128 a[128:131], v50 offset:0
v_mfma_f32_16x16x32_bf16 v[200:203], a[200:203], a[152:155], v[200:203]
ds_read_b128 a[132:135], v50 offset:2048
s_add_u32 m0, s57, s63
v_mfma_f32_16x16x32_bf16 v[232:235], a[200:203], a[156:159], v[232:235]
ds_read_b128 a[136:139], v50 offset:4096
buffer_load_dwordx4 v44, s[80:83], s55 offen offset:0 lds
v_mfma_f32_16x16x32_bf16 v[140:143], a[204:207], a[144:147], v[140:143]
ds_read_b128 a[140:143], v50 offset:6144
v_mfma_f32_16x16x32_bf16 v[172:175], a[204:207], a[148:151], v[172:175]
ds_read_b64_tr_b16 a[164:165], v61 offset:0
ds_read_b64_tr_b16 a[166:167], v61 offset:2048
v_mfma_f32_16x16x32_bf16 v[204:207], a[204:207], a[152:155], v[204:207]
ds_read_b64_tr_b16 a[168:169], v62 offset:0
buffer_load_dwordx4 v45, s[80:83], s55 offen offset:1024 lds
v_mfma_f32_16x16x32_bf16 v[236:239], a[204:207], a[156:159], v[236:239]
ds_read_b64_tr_b16 a[170:171], v62 offset:2048
v_mfma_f32_16x16x32_bf16 v[144:147], a[208:211], a[144:147], v[144:147]
ds_read_b64_tr_b16 a[172:173], v63 offset:0
buffer_load_dwordx4 v46, s[80:83], s55 offen offset:2048 lds
v_mfma_f32_16x16x32_bf16 v[176:179], a[208:211], a[148:151], v[176:179]
ds_read_b64_tr_b16 a[174:175], v63 offset:2048
v_mfma_f32_16x16x32_bf16 v[208:211], a[208:211], a[152:155], v[208:211]
ds_read_b64_tr_b16 a[176:177], v64 offset:0
v_mfma_f32_16x16x32_bf16 v[240:243], a[208:211], a[156:159], v[240:243]
ds_read_b64_tr_b16 a[178:179], v64 offset:2048
ds_read_b64_tr_b16 a[180:181], v65 offset:0
v_mfma_f32_16x16x32_bf16 v[148:151], a[212:215], a[144:147], v[148:151]
ds_read_b64_tr_b16 a[182:183], v65 offset:2048
buffer_load_dwordx4 v47, s[80:83], s55 offen offset:3072 lds
v_mfma_f32_16x16x32_bf16 v[180:183], a[212:215], a[148:151], v[180:183]
ds_read_b64_tr_b16 a[184:185], v66 offset:0
s_add_u32 s57, s57, 16384
v_mfma_f32_16x16x32_bf16 v[212:215], a[212:215], a[152:155], v[212:215]
ds_read_b64_tr_b16 a[186:187], v66 offset:2048
s_and_b32 s57, s57, 0xffff
v_mfma_f32_16x16x32_bf16 v[244:247], a[212:215], a[156:159], v[244:247]
ds_read_b64_tr_b16 a[188:189], v67 offset:0
s_add_u32 s55, s55, 262144
v_mfma_f32_16x16x32_bf16 v[152:155], a[216:219], a[144:147], v[152:155]
ds_read_b64_tr_b16 a[190:191], v67 offset:2048
v_mfma_f32_16x16x32_bf16 v[184:187], a[216:219], a[148:151], v[184:187]
v_mfma_f32_16x16x32_bf16 v[216:219], a[216:219], a[152:155], v[216:219]
v_mfma_f32_16x16x32_bf16 v[248:251], a[216:219], a[156:159], v[248:251]
v_mfma_f32_16x16x32_bf16 v[156:159], a[220:223], a[144:147], v[156:159]
v_mfma_f32_16x16x32_bf16 v[188:191], a[220:223], a[148:151], v[188:191]
v_mfma_f32_16x16x32_bf16 v[220:223], a[220:223], a[152:155], v[220:223]
v_mfma_f32_16x16x32_bf16 v[252:255], a[220:223], a[156:159], v[252:255]
s_waitcnt lgkmcnt(0)
v_mfma_f32_16x16x32_bf16 v[128:131], a[160:163], a[128:131], v[128:131]
s_add_u32 s59, s59, 16384
s_and_b32 s59, s59, 0xffff
v_mfma_f32_16x16x32_bf16 v[160:163], a[160:163], a[132:135], v[160:163]
v_add_u32 v60, s59, v52
v_add_u32 v61, s59, v53
v_mfma_f32_16x16x32_bf16 v[192:195], a[160:163], a[136:139], v[192:195]
v_add_u32 v62, s59, v54
v_add_u32 v63, s59, v55
v_mfma_f32_16x16x32_bf16 v[224:227], a[160:163], a[140:143], v[224:227]
v_add_u32 v64, s59, v56
v_add_u32 v65, s59, v57
v_mfma_f32_16x16x32_bf16 v[132:135], a[164:167], a[128:131], v[132:135]
v_add_u32 v66, s59, v58
v_add_u32 v67, s59, v59
v_mfma_f32_16x16x32_bf16 v[164:167], a[164:167], a[132:135], v[164:167]
v_mfma_f32_16x16x32_bf16 v[196:199], a[164:167], a[136:139], v[196:199]
v_mfma_f32_16x16x32_bf16 v[228:231], a[164:167], a[140:143], v[228:231]
s_waitcnt vmcnt(27)
s_barrier
v_mfma_f32_16x16x32_bf16 v[136:139], a[168:171], a[128:131], v[136:139]
ds_read_b64_tr_b16 a[192:193], v60 offset:0
ds_read_b64_tr_b16 a[194:195], v60 offset:2048
v_mfma_f32_16x16x32_bf16 v[168:171], a[168:171], a[132:135], v[168:171]
ds_read_b128 a[144:147], v51 offset:0
v_mfma_f32_16x16x32_bf16 v[200:203], a[168:171], a[136:139], v[200:203]
ds_read_b128 a[148:151], v51 offset:2048
v_mfma_f32_16x16x32_bf16 v[232:235], a[168:171], a[140:143], v[232:235]
ds_read_b128 a[152:155], v51 offset:4096
v_mfma_f32_16x16x32_bf16 v[140:143], a[172:175], a[128:131], v[140:143]
ds_read_b128 a[156:159], v51 offset:6144
v_mfma_f32_16x16x32_bf16 v[172:175], a[172:175], a[132:135], v[172:175]
ds_read_b64_tr_b16 a[196:197], v61 offset:0
ds_read_b64_tr_b16 a[198:199], v61 offset:2048
v_mfma_f32_16x16x32_bf16 v[204:207], a[172:175], a[136:139], v[204:207]
ds_read_b64_tr_b16 a[200:201], v62 offset:0
v_mfma_f32_16x16x32_bf16 v[236:239], a[172:175], a[140:143], v[236:239]
ds_read_b64_tr_b16 a[202:203], v62 offset:2048
v_mfma_f32_16x16x32_bf16 v[144:147], a[176:179], a[128:131], v[144:147]
ds_read_b64_tr_b16 a[204:205], v63 offset:0
v_mfma_f32_16x16x32_bf16 v[176:179], a[176:179], a[132:135], v[176:179]
ds_read_b64_tr_b16 a[206:207], v63 offset:2048
v_mfma_f32_16x16x32_bf16 v[208:211], a[176:179], a[136:139], v[208:211]
ds_read_b64_tr_b16 a[208:209], v64 offset:0
v_mfma_f32_16x16x32_bf16 v[240:243], a[176:179], a[140:143], v[240:243]
ds_read_b64_tr_b16 a[210:211], v64 offset:2048
ds_read_b64_tr_b16 a[212:213], v65 offset:0
v_mfma_f32_16x16x32_bf16 v[148:151], a[180:183], a[128:131], v[148:151]
ds_read_b64_tr_b16 a[214:215], v65 offset:2048
v_mfma_f32_16x16x32_bf16 v[180:183], a[180:183], a[132:135], v[180:183]
ds_read_b64_tr_b16 a[216:217], v66 offset:0
v_mfma_f32_16x16x32_bf16 v[212:215], a[180:183], a[136:139], v[212:215]
ds_read_b64_tr_b16 a[218:219], v66 offset:2048
v_mfma_f32_16x16x32_bf16 v[244:247], a[180:183], a[140:143], v[244:247]
ds_read_b64_tr_b16 a[220:221], v67 offset:0
v_mfma_f32_16x16x32_bf16 v[152:155], a[184:187], a[128:131], v[152:155]
ds_read_b64_tr_b16 a[222:223], v67 offset:2048
v_mfma_f32_16x16x32_bf16 v[184:187], a[184:187], a[132:135], v[184:187]
v_mfma_f32_16x16x32_bf16 v[216:219], a[184:187], a[136:139], v[216:219]
v_mfma_f32_16x16x32_bf16 v[248:251], a[184:187], a[140:143], v[248:251]
v_mfma_f32_16x16x32_bf16 v[156:159], a[188:191], a[128:131], v[156:159]
v_mfma_f32_16x16x32_bf16 v[188:191], a[188:191], a[132:135], v[188:191]
v_mfma_f32_16x16x32_bf16 v[220:223], a[188:191], a[136:139], v[220:223]
v_mfma_f32_16x16x32_bf16 v[252:255], a[188:191], a[140:143], v[252:255]
s_waitcnt lgkmcnt(0)
v_mfma_f32_16x16x32_bf16 v[128:131], a[192:195], a[144:147], v[128:131]
s_add_u32 s59, s59, 16384
s_and_b32 s59, s59, 0xffff
v_mfma_f32_16x16x32_bf16 v[160:163], a[192:195], a[148:151], v[160:163]
v_add_u32 v60, s59, v52
v_add_u32 v61, s59, v53
v_mfma_f32_16x16x32_bf16 v[192:195], a[192:195], a[152:155], v[192:195]
v_add_u32 v62, s59, v54
v_add_u32 v63, s59, v55
v_mfma_f32_16x16x32_bf16 v[224:227], a[192:195], a[156:159], v[224:227]
v_add_u32 v64, s59, v56
v_add_u32 v65, s59, v57
v_mfma_f32_16x16x32_bf16 v[132:135], a[196:199], a[144:147], v[132:135]
v_add_u32 v66, s59, v58
v_add_u32 v67, s59, v59
v_mfma_f32_16x16x32_bf16 v[164:167], a[196:199], a[148:151], v[164:167]
s_add_u32 s58, s58, 16384
s_cmp_ge_u32 s58, 49152
v_mfma_f32_16x16x32_bf16 v[196:199], a[196:199], a[152:155], v[196:199]
s_cselect_b32 s58, 0, s58
v_add_u32 v50, s58, v48
v_mfma_f32_16x16x32_bf16 v[228:231], a[196:199], a[156:159], v[228:231]
v_add_u32 v51, s58, v49
s_waitcnt vmcnt(15)
v_mfma_f32_16x16x32_bf16 v[136:139], a[200:203], a[144:147], v[136:139]
s_barrier
ds_read_b64_tr_b16 a[160:161], v60 offset:0
v_mfma_f32_16x16x32_bf16 v[168:171], a[200:203], a[148:151], v[168:171]
ds_read_b64_tr_b16 a[162:163], v60 offset:2048
ds_read_b128 a[128:131], v50 offset:0
v_mfma_f32_16x16x32_bf16 v[200:203], a[200:203], a[152:155], v[200:203]
ds_read_b128 a[132:135], v50 offset:2048
v_mfma_f32_16x16x32_bf16 v[232:235], a[200:203], a[156:159], v[232:235]
ds_read_b128 a[136:139], v50 offset:4096
v_mfma_f32_16x16x32_bf16 v[140:143], a[204:207], a[144:147], v[140:143]
ds_read_b128 a[140:143], v50 offset:6144
v_mfma_f32_16x16x32_bf16 v[172:175], a[204:207], a[148:151], v[172:175]
ds_read_b64_tr_b16 a[164:165], v61 offset:0
ds_read_b64_tr_b16 a[166:167], v61 offset:2048
v_mfma_f32_16x16x32_bf16 v[204:207], a[204:207], a[152:155], v[204:207]
ds_read_b64_tr_b16 a[168:169], v62 offset:0
v_mfma_f32_16x16x32_bf16 v[236:239], a[204:207], a[156:159], v[236:239]
ds_read_b64_tr_b16 a[170:171], v62 offset:2048
v_mfma_f32_16x16x32_bf16 v[144:147], a[208:211], a[144:147], v[144:147]
ds_read_b64_tr_b16 a[172:173], v63 offset:0
v_mfma_f32_16x16x32_bf16 v[176:179], a[208:211], a[148:151], v[176:179]
ds_read_b64_tr_b16 a[174:175], v63 offset:2048
v_mfma_f32_16x16x32_bf16 v[208:211], a[208:211], a[152:155], v[208:211]
ds_read_b64_tr_b16 a[176:177], v64 offset:0
v_mfma_f32_16x16x32_bf16 v[240:243], a[208:211], a[156:159], v[240:243]
ds_read_b64_tr_b16 a[178:179], v64 offset:2048
ds_read_b64_tr_b16 a[180:181], v65 offset:0
v_mfma_f32_16x16x32_bf16 v[148:151], a[212:215], a[144:147], v[148:151]
ds_read_b64_tr_b16 a[182:183], v65 offset:2048
v_mfma_f32_16x16x32_bf16 v[180:183], a[212:215], a[148:151], v[180:183]
ds_read_b64_tr_b16 a[184:185], v66 offset:0
v_mfma_f32_16x16x32_bf16 v[212:215], a[212:215], a[152:155], v[212:215]
ds_read_b64_tr_b16 a[186:187], v66 offset:2048
v_mfma_f32_16x16x32_bf16 v[244:247], a[212:215], a[156:159], v[244:247]
ds_read_b64_tr_b16 a[188:189], v67 offset:0
v_mfma_f32_16x16x32_bf16 v[152:155], a[216:219], a[144:147], v[152:155]
ds_read_b64_tr_b16 a[190:191], v67 offset:2048
v_mfma_f32_16x16x32_bf16 v[184:187], a[216:219], a[148:151], v[184:187]
v_mfma_f32_16x16x32_bf16 v[216:219], a[216:219], a[152:155], v[216:219]
v_mfma_f32_16x16x32_bf16 v[248:251], a[216:219], a[156:159], v[248:251]
v_mfma_f32_16x16x32_bf16 v[156:159], a[220:223], a[144:147], v[156:159]
v_mfma_f32_16x16x32_bf16 v[188:191], a[220:223], a[148:151], v[188:191]
v_mfma_f32_16x16x32_bf16 v[220:223], a[220:223], a[152:155], v[220:223]
v_mfma_f32_16x16x32_bf16 v[252:255], a[220:223], a[156:159], v[252:255]
s_waitcnt lgkmcnt(0)
v_mfma_f32_16x16x32_bf16 v[128:131], a[160:163], a[128:131], v[128:131]
s_add_u32 s59, s59, 16384
s_and_b32 s59, s59, 0xffff
v_mfma_f32_16x16x32_bf16 v[160:163], a[160:163], a[132:135], v[160:163]
v_add_u32 v60, s59, v52
v_add_u32 v61, s59, v53
v_mfma_f32_16x16x32_bf16 v[192:195], a[160:163], a[136:139], v[192:195]
v_add_u32 v62, s59, v54
v_add_u32 v63, s59, v55
v_mfma_f32_16x16x32_bf16 v[224:227], a[160:163], a[140:143], v[224:227]
v_add_u32 v64, s59, v56
v_add_u32 v65, s59, v57
v_mfma_f32_16x16x32_bf16 v[132:135], a[164:167], a[128:131], v[132:135]
v_add_u32 v66, s59, v58
v_add_u32 v67, s59, v59
v_mfma_f32_16x16x32_bf16 v[164:167], a[164:167], a[132:135], v[164:167]
v_mfma_f32_16x16x32_bf16 v[196:199], a[164:167], a[136:139], v[196:199]
v_mfma_f32_16x16x32_bf16 v[228:231], a[164:167], a[140:143], v[228:231]
s_waitcnt vmcnt(0)
s_barrier
v_mfma_f32_16x16x32_bf16 v[136:139], a[168:171], a[128:131], v[136:139]
ds_read_b64_tr_b16 a[192:193], v60 offset:0
ds_read_b64_tr_b16 a[194:195], v60 offset:2048
v_mfma_f32_16x16x32_bf16 v[168:171], a[168:171], a[132:135], v[168:171]
ds_read_b128 a[144:147], v51 offset:0
v_mfma_f32_16x16x32_bf16 v[200:203], a[168:171], a[136:139], v[200:203]
ds_read_b128 a[148:151], v51 offset:2048
v_mfma_f32_16x16x32_bf16 v[232:235], a[168:171], a[140:143], v[232:235]
ds_read_b128 a[152:155], v51 offset:4096
v_mfma_f32_16x16x32_bf16 v[140:143], a[172:175], a[128:131], v[140:143]
ds_read_b128 a[156:159], v51 offset:6144
v_mfma_f32_16x16x32_bf16 v[172:175], a[172:175], a[132:135], v[172:175]
ds_read_b64_tr_b16 a[196:197], v61 offset:0
ds_read_b64_tr_b16 a[198:199], v61 offset:2048
v_mfma_f32_16x16x32_bf16 v[204:207], a[172:175], a[136:139], v[204:207]
ds_read_b64_tr_b16 a[200:201], v62 offset:0
v_mfma_f32_16x16x32_bf16 v[236:239], a[172:175], a[140:143], v[236:239]
ds_read_b64_tr_b16 a[202:203], v62 offset:2048
v_mfma_f32_16x16x32_bf16 v[144:147], a[176:179], a[128:131], v[144:147]
ds_read_b64_tr_b16 a[204:205], v63 offset:0
v_mfma_f32_16x16x32_bf16 v[176:179], a[176:179], a[132:135], v[176:179]
ds_read_b64_tr_b16 a[206:207], v63 offset:2048
v_mfma_f32_16x16x32_bf16 v[208:211], a[176:179], a[136:139], v[208:211]
ds_read_b64_tr_b16 a[208:209], v64 offset:0
v_mfma_f32_16x16x32_bf16 v[240:243], a[176:179], a[140:143], v[240:243]
ds_read_b64_tr_b16 a[210:211], v64 offset:2048
ds_read_b64_tr_b16 a[212:213], v65 offset:0
v_mfma_f32_16x16x32_bf16 v[148:151], a[180:183], a[128:131], v[148:151]
ds_read_b64_tr_b16 a[214:215], v65 offset:2048
v_mfma_f32_16x16x32_bf16 v[180:183], a[180:183], a[132:135], v[180:183]
ds_read_b64_tr_b16 a[216:217], v66 offset:0
v_mfma_f32_16x16x32_bf16 v[212:215], a[180:183], a[136:139], v[212:215]
ds_read_b64_tr_b16 a[218:219], v66 offset:2048
v_mfma_f32_16x16x32_bf16 v[244:247], a[180:183], a[140:143], v[244:247]
ds_read_b64_tr_b16 a[220:221], v67 offset:0
v_mfma_f32_16x16x32_bf16 v[152:155], a[184:187], a[128:131], v[152:155]
ds_read_b64_tr_b16 a[222:223], v67 offset:2048
v_mfma_f32_16x16x32_bf16 v[184:187], a[184:187], a[132:135], v[184:187]
v_mfma_f32_16x16x32_bf16 v[216:219], a[184:187], a[136:139], v[216:219]
v_mfma_f32_16x16x32_bf16 v[248:251], a[184:187], a[140:143], v[248:251]
v_mfma_f32_16x16x32_bf16 v[156:159], a[188:191], a[128:131], v[156:159]
v_mfma_f32_16x16x32_bf16 v[188:191], a[188:191], a[132:135], v[188:191]
v_mfma_f32_16x16x32_bf16 v[220:223], a[188:191], a[136:139], v[220:223]
v_mfma_f32_16x16x32_bf16 v[252:255], a[188:191], a[140:143], v[252:255]
s_waitcnt lgkmcnt(0)
v_mfma_f32_16x16x32_bf16 v[128:131], a[192:195], a[144:147], v[128:131]
v_mfma_f32_16x16x32_bf16 v[160:163], a[192:195], a[148:151], v[160:163]
v_mfma_f32_16x16x32_bf16 v[192:195], a[192:195], a[152:155], v[192:195]
v_mfma_f32_16x16x32_bf16 v[224:227], a[192:195], a[156:159], v[224:227]
v_mfma_f32_16x16x32_bf16 v[132:135], a[196:199], a[144:147], v[132:135]
v_mfma_f32_16x16x32_bf16 v[164:167], a[196:199], a[148:151], v[164:167]
v_mfma_f32_16x16x32_bf16 v[196:199], a[196:199], a[152:155], v[196:199]
v_mfma_f32_16x16x32_bf16 v[228:231], a[196:199], a[156:159], v[228:231]
v_mfma_f32_16x16x32_bf16 v[136:139], a[200:203], a[144:147], v[136:139]
v_mfma_f32_16x16x32_bf16 v[168:171], a[200:203], a[148:151], v[168:171]
v_mfma_f32_16x16x32_bf16 v[200:203], a[200:203], a[152:155], v[200:203]
v_mfma_f32_16x16x32_bf16 v[232:235], a[200:203], a[156:159], v[232:235]
v_mfma_f32_16x16x32_bf16 v[140:143], a[204:207], a[144:147], v[140:143]
v_mfma_f32_16x16x32_bf16 v[172:175], a[204:207], a[148:151], v[172:175]
v_mfma_f32_16x16x32_bf16 v[204:207], a[204:207], a[152:155], v[204:207]
v_mfma_f32_16x16x32_bf16 v[236:239], a[204:207], a[156:159], v[236:239]
v_mfma_f32_16x16x32_bf16 v[144:147], a[208:211], a[144:147], v[144:147]
v_mfma_f32_16x16x32_bf16 v[176:179], a[208:211], a[148:151], v[176:179]
v_mfma_f32_16x16x32_bf16 v[208:211], a[208:211], a[152:155], v[208:211]
v_mfma_f32_16x16x32_bf16 v[240:243], a[208:211], a[156:159], v[240:243]
v_mfma_f32_16x16x32_bf16 v[148:151], a[212:215], a[144:147], v[148:151]
v_mfma_f32_16x16x32_bf16 v[180:183], a[212:215], a[148:151], v[180:183]
v_mfma_f32_16x16x32_bf16 v[212:215], a[212:215], a[152:155], v[212:215]
v_mfma_f32_16x16x32_bf16 v[244:247], a[212:215], a[156:159], v[244:247]
v_mfma_f32_16x16x32_bf16 v[152:155], a[216:219], a[144:147], v[152:155]
v_mfma_f32_16x16x32_bf16 v[184:187], a[216:219], a[148:151], v[184:187]
v_mfma_f32_16x16x32_bf16 v[216:219], a[216:219], a[152:155], v[216:219]
v_mfma_f32_16x16x32_bf16 v[248:251], a[216:219], a[156:159], v[248:251]
v_mfma_f32_16x16x32_bf16 v[156:159], a[220:223], a[144:147], v[156:159]
v_mfma_f32_16x16x32_bf16 v[188:191], a[220:223], a[148:151], v[188:191]
v_mfma_f32_16x16x32_bf16 v[220:223], a[220:223], a[152:155], v[220:223]
v_mfma_f32_16x16x32_bf16 v[252:255], a[220:223], a[156:159], v[252:255]
s_waitcnt vmcnt(0)
s_barrier
s_lshl_b32 s50, s68, 4
s_bfe_u32 s51, s66, 0x10004
s_lshl_b32 s51, s51, 3
s_add_u32 s50, s50, s51
s_lshr_b32 s51, s66, 5
s_add_u32 s50, s50, s51
s_lshl_b32 s50, s50, 2
s_add_u32 s50, s50, 79691776
v_mov_b32 v27, 1
s_cmp_eq_u32 s49, 0
s_cselect_b32 exec_lo, 1, 0
s_mov_b32 exec_hi, 0
buffer_store_dword v27, off, s[36:39], s50 sc1
s_mov_b64 exec, -1
s_lshl_b32 s50, s45, 15
s_lshl_b32 s51, s46, 12
s_add_u32 s50, s50, s51
s_add_u32 s50, s50, 32768
v_add_u32 v68, s50, v8
s_mov_b32 s51, 0x3db8aa3b
v_mul_f32 v128, s51, v128
v_mul_f32 v129, s51, v129
v_mul_f32 v130, s51, v130
v_mul_f32 v131, s51, v131
v_mul_f32 v132, s51, v132
v_mul_f32 v133, s51, v133
v_mul_f32 v134, s51, v134
v_mul_f32 v135, s51, v135
v_cvt_pk_bf16_f32 v40, v128, v129
v_cvt_pk_bf16_f32 v41, v130, v131
v_cvt_pk_bf16_f32 v42, v132, v133
v_cvt_pk_bf16_f32 v43, v134, v135
ds_write_b128 v68, v[40:43] offset:0
v_mul_f32 v136, s51, v136
v_mul_f32 v137, s51, v137
v_mul_f32 v138, s51, v138
v_mul_f32 v139, s51, v139
v_mul_f32 v140, s51, v140
v_mul_f32 v141, s51, v141
v_mul_f32 v142, s51, v142
v_mul_f32 v143, s51, v143
v_cvt_pk_bf16_f32 v44, v136, v137
v_cvt_pk_bf16_f32 v45, v138, v139
v_cvt_pk_bf16_f32 v46, v140, v141
v_cvt_pk_bf16_f32 v47, v142, v143
ds_write_b128 v68, v[44:47] offset:1024
v_mul_f32 v144, s51, v144
v_mul_f32 v145, s51, v145
v_mul_f32 v146, s51, v146
v_mul_f32 v147, s51, v147
v_mul_f32 v148, s51, v148
v_mul_f32 v149, s51, v149
v_mul_f32 v150, s51, v150
v_mul_f32 v151, s51, v151
v_cvt_pk_bf16_f32 v48, v144, v145
v_cvt_pk_bf16_f32 v49, v146, v147
v_cvt_pk_bf16_f32 v50, v148, v149
v_cvt_pk_bf16_f32 v51, v150, v151
ds_write_b128 v68, v[48:51] offset:2048
v_mul_f32 v152, s51, v152
v_mul_f32 v153, s51, v153
v_mul_f32 v154, s51, v154
v_mul_f32 v155, s51, v155
v_mul_f32 v156, s51, v156
v_mul_f32 v157, s51, v157
v_mul_f32 v158, s51, v158
v_mul_f32 v159, s51, v159
v_cvt_pk_bf16_f32 v52, v152, v153
v_cvt_pk_bf16_f32 v53, v154, v155
v_cvt_pk_bf16_f32 v54, v156, v157
v_cvt_pk_bf16_f32 v55, v158, v159
ds_write_b128 v68, v[52:55] offset:3072
v_mul_f32 v160, s51, v160
v_mul_f32 v161, s51, v161
v_mul_f32 v162, s51, v162
v_mul_f32 v163, s51, v163
v_mul_f32 v164, s51, v164
v_mul_f32 v165, s51, v165
v_mul_f32 v166, s51, v166
v_mul_f32 v167, s51, v167
v_cvt_pk_bf16_f32 v56, v160, v161
v_cvt_pk_bf16_f32 v57, v162, v163
v_cvt_pk_bf16_f32 v58, v164, v165
v_cvt_pk_bf16_f32 v59, v166, v167
ds_write_b128 v68, v[56:59] offset:8192
v_mul_f32 v168, s51, v168
v_mul_f32 v169, s51, v169
v_mul_f32 v170, s51, v170
v_mul_f32 v171, s51, v171
v_mul_f32 v172, s51, v172
v_mul_f32 v173, s51, v173
v_mul_f32 v174, s51, v174
v_mul_f32 v175, s51, v175
v_cvt_pk_bf16_f32 v60, v168, v169
v_cvt_pk_bf16_f32 v61, v170, v171
v_cvt_pk_bf16_f32 v62, v172, v173
v_cvt_pk_bf16_f32 v63, v174, v175
ds_write_b128 v68, v[60:63] offset:9216
v_mul_f32 v176, s51, v176
v_mul_f32 v177, s51, v177
v_mul_f32 v178, s51, v178
v_mul_f32 v179, s51, v179
v_mul_f32 v180, s51, v180
v_mul_f32 v181, s51, v181
v_mul_f32 v182, s51, v182
v_mul_f32 v183, s51, v183
v_cvt_pk_bf16_f32 v40, v176, v177
v_cvt_pk_bf16_f32 v41, v178, v179
v_cvt_pk_bf16_f32 v42, v180, v181
v_cvt_pk_bf16_f32 v43, v182, v183
ds_write_b128 v68, v[40:43] offset:10240
v_mul_f32 v184, s51, v184
v_mul_f32 v185, s51, v185
v_mul_f32 v186, s51, v186
v_mul_f32 v187, s51, v187
v_mul_f32 v188, s51, v188
v_mul_f32 v189, s51, v189
v_mul_f32 v190, s51, v190
v_mul_f32 v191, s51, v191
v_cvt_pk_bf16_f32 v44, v184, v185
v_cvt_pk_bf16_f32 v45, v186, v187
v_cvt_pk_bf16_f32 v46, v188, v189
v_cvt_pk_bf16_f32 v47, v190, v191
ds_write_b128 v68, v[44:47] offset:11264
v_mul_f32 v192, s51, v192
v_mul_f32 v193, s51, v193
v_mul_f32 v194, s51, v194
v_mul_f32 v195, s51, v195
v_mul_f32 v196, s51, v196
v_mul_f32 v197, s51, v197
v_mul_f32 v198, s51, v198
v_mul_f32 v199, s51, v199
v_cvt_pk_bf16_f32 v48, v192, v193
v_cvt_pk_bf16_f32 v49, v194, v195
v_cvt_pk_bf16_f32 v50, v196, v197
v_cvt_pk_bf16_f32 v51, v198, v199
ds_write_b128 v68, v[48:51] offset:16384
v_mul_f32 v200, s51, v200
v_mul_f32 v201, s51, v201
v_mul_f32 v202, s51, v202
v_mul_f32 v203, s51, v203
v_mul_f32 v204, s51, v204
v_mul_f32 v205, s51, v205
v_mul_f32 v206, s51, v206
v_mul_f32 v207, s51, v207
v_cvt_pk_bf16_f32 v52, v200, v201
v_cvt_pk_bf16_f32 v53, v202, v203
v_cvt_pk_bf16_f32 v54, v204, v205
v_cvt_pk_bf16_f32 v55, v206, v207
ds_write_b128 v68, v[52:55] offset:17408
v_mul_f32 v208, s51, v208
v_mul_f32 v209, s51, v209
v_mul_f32 v210, s51, v210
v_mul_f32 v211, s51, v211
v_mul_f32 v212, s51, v212
v_mul_f32 v213, s51, v213
v_mul_f32 v214, s51, v214
v_mul_f32 v215, s51, v215
v_cvt_pk_bf16_f32 v56, v208, v209
v_cvt_pk_bf16_f32 v57, v210, v211
v_cvt_pk_bf16_f32 v58, v212, v213
v_cvt_pk_bf16_f32 v59, v214, v215
ds_write_b128 v68, v[56:59] offset:18432
v_mul_f32 v216, s51, v216
v_mul_f32 v217, s51, v217
v_mul_f32 v218, s51, v218
v_mul_f32 v219, s51, v219
v_mul_f32 v220, s51, v220
v_mul_f32 v221, s51, v221
v_mul_f32 v222, s51, v222
v_mul_f32 v223, s51, v223
v_cvt_pk_bf16_f32 v60, v216, v217
v_cvt_pk_bf16_f32 v61, v218, v219
v_cvt_pk_bf16_f32 v62, v220, v221
v_cvt_pk_bf16_f32 v63, v222, v223
ds_write_b128 v68, v[60:63] offset:19456
v_mul_f32 v224, s51, v224
v_mul_f32 v225, s51, v225
v_mul_f32 v226, s51, v226
v_mul_f32 v227, s51, v227
v_mul_f32 v228, s51, v228
v_mul_f32 v229, s51, v229
v_mul_f32 v230, s51, v230
v_mul_f32 v231, s51, v231
v_cvt_pk_bf16_f32 v40, v224, v225
v_cvt_pk_bf16_f32 v41, v226, v227
v_cvt_pk_bf16_f32 v42, v228, v229
v_cvt_pk_bf16_f32 v43, v230, v231
ds_write_b128 v68, v[40:43] offset:24576
v_mul_f32 v232, s51, v232
v_mul_f32 v233, s51, v233
v_mul_f32 v234, s51, v234
v_mul_f32 v235, s51, v235
v_mul_f32 v236, s51, v236
v_mul_f32 v237, s51, v237
v_mul_f32 v238, s51, v238
v_mul_f32 v239, s51, v239
v_cvt_pk_bf16_f32 v44, v232, v233
v_cvt_pk_bf16_f32 v45, v234, v235
v_cvt_pk_bf16_f32 v46, v236, v237
v_cvt_pk_bf16_f32 v47, v238, v239
ds_write_b128 v68, v[44:47] offset:25600
v_mul_f32 v240, s51, v240
v_mul_f32 v241, s51, v241
v_mul_f32 v242, s51, v242
v_mul_f32 v243, s51, v243
v_mul_f32 v244, s51, v244
v_mul_f32 v245, s51, v245
v_mul_f32 v246, s51, v246
v_mul_f32 v247, s51, v247
v_cvt_pk_bf16_f32 v48, v240, v241
v_cvt_pk_bf16_f32 v49, v242, v243
v_cvt_pk_bf16_f32 v50, v244, v245
v_cvt_pk_bf16_f32 v51, v246, v247
ds_write_b128 v68, v[48:51] offset:26624
v_mul_f32 v248, s51, v248
v_mul_f32 v249, s51, v249
v_mul_f32 v250, s51, v250
v_mul_f32 v251, s51, v251
v_mul_f32 v252, s51, v252
v_mul_f32 v253, s51, v253
v_mul_f32 v254, s51, v254
v_mul_f32 v255, s51, v255
v_cvt_pk_bf16_f32 v52, v248, v249
v_cvt_pk_bf16_f32 v53, v250, v251
v_cvt_pk_bf16_f32 v54, v252, v253
v_cvt_pk_bf16_f32 v55, v254, v255
ds_write_b128 v68, v[52:55] offset:27648
s_lshl_b32 s50, s49, 14
s_add_u32 s50, s50, 32768
v_add_u32 v68, s50, v8
s_waitcnt lgkmcnt(0)
s_barrier
ds_read_b128 a[0:3], v68 offset:0
ds_read_b128 a[4:7], v68 offset:1024
ds_read_b128 a[8:11], v68 offset:2048
ds_read_b128 a[12:15], v68 offset:3072
ds_read_b128 a[16:19], v68 offset:4096
ds_read_b128 a[20:23], v68 offset:5120
ds_read_b128 a[24:27], v68 offset:6144
ds_read_b128 a[28:31], v68 offset:7168
ds_read_b128 a[32:35], v68 offset:8192
ds_read_b128 a[36:39], v68 offset:9216
ds_read_b128 a[40:43], v68 offset:10240
ds_read_b128 a[44:47], v68 offset:11264
ds_read_b128 a[48:51], v68 offset:12288
ds_read_b128 a[52:55], v68 offset:13312
ds_read_b128 a[56:59], v68 offset:14336
ds_read_b128 a[60:63], v68 offset:15360
v_mul_u32_u24 v116, 1152, v120
v_lshl_add_u32 v116, v119, 2, v116
s_add_u32 s52, s67, 98304
v_add_u32 v116, s52, v116
s_mov_b32 s50, 0xffff0000
v_lshlrev_b32 v128, 16, v100
v_and_b32 v129, s50, v100
v_lshlrev_b32 v130, 16, v101
v_and_b32 v131, s50, v101
v_lshlrev_b32 v132, 16, v102
v_and_b32 v133, s50, v102
v_lshlrev_b32 v134, 16, v103
v_and_b32 v135, s50, v103
v_lshlrev_b32 v136, 16, v72
v_and_b32 v141, s50, v72
v_lshlrev_b32 v137, 16, v76
v_and_b32 v142, s50, v76
v_lshlrev_b32 v138, 16, v80
v_and_b32 v143, s50, v80
v_lshlrev_b32 v139, 16, v84
v_and_b32 v144, s50, v84
v_lshlrev_b32 v140, 16, v88
v_and_b32 v145, s50, v88
v_mul_f32 v146, v136, v131
v_mul_f32 v147, v141, v135
v_mul_f32 v148, v137, v130
v_mul_f32 v149, v142, v134
v_mul_f32 v150, v138, v129
v_mul_f32 v151, v143, v133
v_mul_f32 v152, v139, v128
v_mul_f32 v153, v144, v132
v_cvt_pk_bf16_f32 v146, v146, v147
v_cvt_pk_bf16_f32 v148, v148, v149
v_cvt_pk_bf16_f32 v150, v150, v151
v_cvt_pk_bf16_f32 v152, v152, v153
v_lshlrev_b32 v154, 16, v146
v_and_b32 v155, s50, v146
v_lshlrev_b32 v156, 16, v148
v_and_b32 v157, s50, v148
v_add_f32 v154, v154, v156
v_add_f32 v155, v155, v157
v_cvt_pk_bf16_f32 v146, v154, v155
v_lshlrev_b32 v154, 16, v146
v_and_b32 v155, s50, v146
v_lshlrev_b32 v156, 16, v150
v_and_b32 v157, s50, v150
v_add_f32 v154, v154, v156
v_add_f32 v155, v155, v157
v_cvt_pk_bf16_f32 v146, v154, v155
v_lshlrev_b32 v154, 16, v146
v_and_b32 v155, s50, v146
v_lshlrev_b32 v156, 16, v152
v_and_b32 v157, s50, v152
v_add_f32 v154, v154, v156
v_add_f32 v155, v155, v157
v_cvt_pk_bf16_f32 v146, v154, v155
v_lshlrev_b32 v154, 16, v146
v_and_b32 v155, s50, v146
v_lshlrev_b32 v156, 16, v92
v_and_b32 v157, s50, v92
v_mul_f32 v154, v154, v156
v_mul_f32 v155, v155, v157
v_cvt_pk_bf16_f32 v158, v154, v155
v_mul_f32 v146, v137, v131
v_mul_f32 v147, v142, v135
v_mul_f32 v148, v138, v130
v_mul_f32 v149, v143, v134
v_mul_f32 v150, v139, v129
v_mul_f32 v151, v144, v133
v_mul_f32 v152, v140, v128
v_mul_f32 v153, v145, v132
v_cvt_pk_bf16_f32 v146, v146, v147
v_cvt_pk_bf16_f32 v148, v148, v149
v_cvt_pk_bf16_f32 v150, v150, v151
v_cvt_pk_bf16_f32 v152, v152, v153
v_lshlrev_b32 v154, 16, v146
v_and_b32 v155, s50, v146
v_lshlrev_b32 v156, 16, v148
v_and_b32 v157, s50, v148
v_add_f32 v154, v154, v156
v_add_f32 v155, v155, v157
v_cvt_pk_bf16_f32 v146, v154, v155
v_lshlrev_b32 v154, 16, v146
v_and_b32 v155, s50, v146
v_lshlrev_b32 v156, 16, v150
v_and_b32 v157, s50, v150
v_add_f32 v154, v154, v156
v_add_f32 v155, v155, v157
v_cvt_pk_bf16_f32 v146, v154, v155
v_lshlrev_b32 v154, 16, v146
v_and_b32 v155, s50, v146
v_lshlrev_b32 v156, 16, v152
v_and_b32 v157, s50, v152
v_add_f32 v154, v154, v156
v_add_f32 v155, v155, v157
v_cvt_pk_bf16_f32 v146, v154, v155
v_lshlrev_b32 v154, 16, v146
v_and_b32 v155, s50, v146
v_lshlrev_b32 v156, 16, v96
v_and_b32 v157, s50, v96
v_mul_f32 v154, v154, v156
v_mul_f32 v155, v155, v157
v_cvt_pk_bf16_f32 v159, v154, v155
v_lshlrev_b32 v160, 16, v159
v_bfi_b32 v160, s50, v160, v158
v_lshrrev_b32 v161, 16, v158
v_bfi_b32 v161, s50, v159, v161
ds_write_b32 v116, v160 offset:0
ds_write_b32 v116, v161 offset:144
v_lshlrev_b32 v128, 16, v104
v_and_b32 v129, s50, v104
v_lshlrev_b32 v130, 16, v105
v_and_b32 v131, s50, v105
v_lshlrev_b32 v132, 16, v106
v_and_b32 v133, s50, v106
v_lshlrev_b32 v134, 16, v107
v_and_b32 v135, s50, v107
v_lshlrev_b32 v136, 16, v73
v_and_b32 v141, s50, v73
v_lshlrev_b32 v137, 16, v77
v_and_b32 v142, s50, v77
v_lshlrev_b32 v138, 16, v81
v_and_b32 v143, s50, v81
v_lshlrev_b32 v139, 16, v85
v_and_b32 v144, s50, v85
v_lshlrev_b32 v140, 16, v89
v_and_b32 v145, s50, v89
v_mul_f32 v146, v136, v131
v_mul_f32 v147, v141, v135
v_mul_f32 v148, v137, v130
v_mul_f32 v149, v142, v134
v_mul_f32 v150, v138, v129
v_mul_f32 v151, v143, v133
v_mul_f32 v152, v139, v128
v_mul_f32 v153, v144, v132
v_cvt_pk_bf16_f32 v146, v146, v147
v_cvt_pk_bf16_f32 v148, v148, v149
v_cvt_pk_bf16_f32 v150, v150, v151
v_cvt_pk_bf16_f32 v152, v152, v153
v_lshlrev_b32 v154, 16, v146
v_and_b32 v155, s50, v146
v_lshlrev_b32 v156, 16, v148
v_and_b32 v157, s50, v148
v_add_f32 v154, v154, v156
v_add_f32 v155, v155, v157
v_cvt_pk_bf16_f32 v146, v154, v155
v_lshlrev_b32 v154, 16, v146
v_and_b32 v155, s50, v146
v_lshlrev_b32 v156, 16, v150
v_and_b32 v157, s50, v150
v_add_f32 v154, v154, v156
v_add_f32 v155, v155, v157
v_cvt_pk_bf16_f32 v146, v154, v155
v_lshlrev_b32 v154, 16, v146
v_and_b32 v155, s50, v146
v_lshlrev_b32 v156, 16, v152
v_and_b32 v157, s50, v152
v_add_f32 v154, v154, v156
v_add_f32 v155, v155, v157
v_cvt_pk_bf16_f32 v146, v154, v155
v_lshlrev_b32 v154, 16, v146
v_and_b32 v155, s50, v146
v_lshlrev_b32 v156, 16, v93
v_and_b32 v157, s50, v93
v_mul_f32 v154, v154, v156
v_mul_f32 v155, v155, v157
v_cvt_pk_bf16_f32 v158, v154, v155
v_mul_f32 v146, v137, v131
v_mul_f32 v147, v142, v135
v_mul_f32 v148, v138, v130
v_mul_f32 v149, v143, v134
v_mul_f32 v150, v139, v129
v_mul_f32 v151, v144, v133
v_mul_f32 v152, v140, v128
v_mul_f32 v153, v145, v132
v_cvt_pk_bf16_f32 v146, v146, v147
v_cvt_pk_bf16_f32 v148, v148, v149
v_cvt_pk_bf16_f32 v150, v150, v151
v_cvt_pk_bf16_f32 v152, v152, v153
v_lshlrev_b32 v154, 16, v146
v_and_b32 v155, s50, v146
v_lshlrev_b32 v156, 16, v148
v_and_b32 v157, s50, v148
v_add_f32 v154, v154, v156
v_add_f32 v155, v155, v157
v_cvt_pk_bf16_f32 v146, v154, v155
v_lshlrev_b32 v154, 16, v146
v_and_b32 v155, s50, v146
v_lshlrev_b32 v156, 16, v150
v_and_b32 v157, s50, v150
v_add_f32 v154, v154, v156
v_add_f32 v155, v155, v157
v_cvt_pk_bf16_f32 v146, v154, v155
v_lshlrev_b32 v154, 16, v146
v_and_b32 v155, s50, v146
v_lshlrev_b32 v156, 16, v152
v_and_b32 v157, s50, v152
v_add_f32 v154, v154, v156
v_add_f32 v155, v155, v157
v_cvt_pk_bf16_f32 v146, v154, v155
v_lshlrev_b32 v154, 16, v146
v_and_b32 v155, s50, v146
v_lshlrev_b32 v156, 16, v97
v_and_b32 v157, s50, v97
v_mul_f32 v154, v154, v156
v_mul_f32 v155, v155, v157
v_cvt_pk_bf16_f32 v159, v154, v155
v_lshlrev_b32 v162, 16, v159
v_bfi_b32 v162, s50, v162, v158
v_lshrrev_b32 v163, 16, v158
v_bfi_b32 v163, s50, v159, v163
ds_write_b32 v116, v162 offset:288
ds_write_b32 v116, v163 offset:432
v_lshlrev_b32 v128, 16, v108
v_and_b32 v129, s50, v108
v_lshlrev_b32 v130, 16, v109
v_and_b32 v131, s50, v109
v_lshlrev_b32 v132, 16, v110
v_and_b32 v133, s50, v110
v_lshlrev_b32 v134, 16, v111
v_and_b32 v135, s50, v111
v_lshlrev_b32 v136, 16, v74
v_and_b32 v141, s50, v74
v_lshlrev_b32 v137, 16, v78
v_and_b32 v142, s50, v78
v_lshlrev_b32 v138, 16, v82
v_and_b32 v143, s50, v82
v_lshlrev_b32 v139, 16, v86
v_and_b32 v144, s50, v86
v_lshlrev_b32 v140, 16, v90
v_and_b32 v145, s50, v90
v_mul_f32 v146, v136, v131
v_mul_f32 v147, v141, v135
v_mul_f32 v148, v137, v130
v_mul_f32 v149, v142, v134
v_mul_f32 v150, v138, v129
v_mul_f32 v151, v143, v133
v_mul_f32 v152, v139, v128
v_mul_f32 v153, v144, v132
v_cvt_pk_bf16_f32 v146, v146, v147
v_cvt_pk_bf16_f32 v148, v148, v149
v_cvt_pk_bf16_f32 v150, v150, v151
v_cvt_pk_bf16_f32 v152, v152, v153
v_lshlrev_b32 v154, 16, v146
v_and_b32 v155, s50, v146
v_lshlrev_b32 v156, 16, v148
v_and_b32 v157, s50, v148
v_add_f32 v154, v154, v156
v_add_f32 v155, v155, v157
v_cvt_pk_bf16_f32 v146, v154, v155
v_lshlrev_b32 v154, 16, v146
v_and_b32 v155, s50, v146
v_lshlrev_b32 v156, 16, v150
v_and_b32 v157, s50, v150
v_add_f32 v154, v154, v156
v_add_f32 v155, v155, v157
v_cvt_pk_bf16_f32 v146, v154, v155
v_lshlrev_b32 v154, 16, v146
v_and_b32 v155, s50, v146
v_lshlrev_b32 v156, 16, v152
v_and_b32 v157, s50, v152
v_add_f32 v154, v154, v156
v_add_f32 v155, v155, v157
v_cvt_pk_bf16_f32 v146, v154, v155
v_lshlrev_b32 v154, 16, v146
v_and_b32 v155, s50, v146
v_lshlrev_b32 v156, 16, v94
v_and_b32 v157, s50, v94
v_mul_f32 v154, v154, v156
v_mul_f32 v155, v155, v157
v_cvt_pk_bf16_f32 v158, v154, v155
v_mul_f32 v146, v137, v131
v_mul_f32 v147, v142, v135
v_mul_f32 v148, v138, v130
v_mul_f32 v149, v143, v134
v_mul_f32 v150, v139, v129
v_mul_f32 v151, v144, v133
v_mul_f32 v152, v140, v128
v_mul_f32 v153, v145, v132
v_cvt_pk_bf16_f32 v146, v146, v147
v_cvt_pk_bf16_f32 v148, v148, v149
v_cvt_pk_bf16_f32 v150, v150, v151
v_cvt_pk_bf16_f32 v152, v152, v153
v_lshlrev_b32 v154, 16, v146
v_and_b32 v155, s50, v146
v_lshlrev_b32 v156, 16, v148
v_and_b32 v157, s50, v148
v_add_f32 v154, v154, v156
v_add_f32 v155, v155, v157
v_cvt_pk_bf16_f32 v146, v154, v155
v_lshlrev_b32 v154, 16, v146
v_and_b32 v155, s50, v146
v_lshlrev_b32 v156, 16, v150
v_and_b32 v157, s50, v150
v_add_f32 v154, v154, v156
v_add_f32 v155, v155, v157
v_cvt_pk_bf16_f32 v146, v154, v155
v_lshlrev_b32 v154, 16, v146
v_and_b32 v155, s50, v146
v_lshlrev_b32 v156, 16, v152
v_and_b32 v157, s50, v152
v_add_f32 v154, v154, v156
v_add_f32 v155, v155, v157
v_cvt_pk_bf16_f32 v146, v154, v155
v_lshlrev_b32 v154, 16, v146
v_and_b32 v155, s50, v146
v_lshlrev_b32 v156, 16, v98
v_and_b32 v157, s50, v98
v_mul_f32 v154, v154, v156
v_mul_f32 v155, v155, v157
v_cvt_pk_bf16_f32 v159, v154, v155
v_lshlrev_b32 v160, 16, v159
v_bfi_b32 v160, s50, v160, v158
v_lshrrev_b32 v161, 16, v158
v_bfi_b32 v161, s50, v159, v161
ds_write_b32 v116, v160 offset:576
ds_write_b32 v116, v161 offset:720
v_lshlrev_b32 v128, 16, v112
v_and_b32 v129, s50, v112
v_lshlrev_b32 v130, 16, v113
v_and_b32 v131, s50, v113
v_lshlrev_b32 v132, 16, v114
v_and_b32 v133, s50, v114
v_lshlrev_b32 v134, 16, v115
v_and_b32 v135, s50, v115
v_lshlrev_b32 v136, 16, v75
v_and_b32 v141, s50, v75
v_lshlrev_b32 v137, 16, v79
v_and_b32 v142, s50, v79
v_lshlrev_b32 v138, 16, v83
v_and_b32 v143, s50, v83
v_lshlrev_b32 v139, 16, v87
v_and_b32 v144, s50, v87
v_lshlrev_b32 v140, 16, v91
v_and_b32 v145, s50, v91
v_mul_f32 v146, v136, v131
v_mul_f32 v147, v141, v135
v_mul_f32 v148, v137, v130
v_mul_f32 v149, v142, v134
v_mul_f32 v150, v138, v129
v_mul_f32 v151, v143, v133
v_mul_f32 v152, v139, v128
v_mul_f32 v153, v144, v132
v_cvt_pk_bf16_f32 v146, v146, v147
v_cvt_pk_bf16_f32 v148, v148, v149
v_cvt_pk_bf16_f32 v150, v150, v151
v_cvt_pk_bf16_f32 v152, v152, v153
v_lshlrev_b32 v154, 16, v146
v_and_b32 v155, s50, v146
v_lshlrev_b32 v156, 16, v148
v_and_b32 v157, s50, v148
v_add_f32 v154, v154, v156
v_add_f32 v155, v155, v157
v_cvt_pk_bf16_f32 v146, v154, v155
v_lshlrev_b32 v154, 16, v146
v_and_b32 v155, s50, v146
v_lshlrev_b32 v156, 16, v150
v_and_b32 v157, s50, v150
v_add_f32 v154, v154, v156
v_add_f32 v155, v155, v157
v_cvt_pk_bf16_f32 v146, v154, v155
v_lshlrev_b32 v154, 16, v146
v_and_b32 v155, s50, v146
v_lshlrev_b32 v156, 16, v152
v_and_b32 v157, s50, v152
v_add_f32 v154, v154, v156
v_add_f32 v155, v155, v157
v_cvt_pk_bf16_f32 v146, v154, v155
v_lshlrev_b32 v154, 16, v146
v_and_b32 v155, s50, v146
v_lshlrev_b32 v156, 16, v95
v_and_b32 v157, s50, v95
v_mul_f32 v154, v154, v156
v_mul_f32 v155, v155, v157
v_cvt_pk_bf16_f32 v158, v154, v155
v_mul_f32 v146, v137, v131
v_mul_f32 v147, v142, v135
v_mul_f32 v148, v138, v130
v_mul_f32 v149, v143, v134
v_mul_f32 v150, v139, v129
v_mul_f32 v151, v144, v133
v_mul_f32 v152, v140, v128
v_mul_f32 v153, v145, v132
v_cvt_pk_bf16_f32 v146, v146, v147
v_cvt_pk_bf16_f32 v148, v148, v149
v_cvt_pk_bf16_f32 v150, v150, v151
v_cvt_pk_bf16_f32 v152, v152, v153
v_lshlrev_b32 v154, 16, v146
v_and_b32 v155, s50, v146
v_lshlrev_b32 v156, 16, v148
v_and_b32 v157, s50, v148
v_add_f32 v154, v154, v156
v_add_f32 v155, v155, v157
v_cvt_pk_bf16_f32 v146, v154, v155
v_lshlrev_b32 v154, 16, v146
v_and_b32 v155, s50, v146
v_lshlrev_b32 v156, 16, v150
v_and_b32 v157, s50, v150
v_add_f32 v154, v154, v156
v_add_f32 v155, v155, v157
v_cvt_pk_bf16_f32 v146, v154, v155
v_lshlrev_b32 v154, 16, v146
v_and_b32 v155, s50, v146
v_lshlrev_b32 v156, 16, v152
v_and_b32 v157, s50, v152
v_add_f32 v154, v154, v156
v_add_f32 v155, v155, v157
v_cvt_pk_bf16_f32 v146, v154, v155
v_lshlrev_b32 v154, 16, v146
v_and_b32 v155, s50, v146
v_lshlrev_b32 v156, 16, v99
v_and_b32 v157, s50, v99
v_mul_f32 v154, v154, v156
v_mul_f32 v155, v155, v157
v_cvt_pk_bf16_f32 v159, v154, v155
v_lshlrev_b32 v162, 16, v159
v_bfi_b32 v162, s50, v162, v158
v_lshrrev_b32 v163, 16, v158
v_bfi_b32 v163, s50, v159, v163
ds_write_b32 v116, v162 offset:864
ds_write_b32 v116, v163 offset:1008
s_and_b32 s50, s66, 7
s_mul_i32 s50, s50, 229376
s_lshr_b32 s51, s66, 3
s_lshl_b32 s51, s51, 7
s_add_u32 s50, s50, s51
s_add_u32 s50, s50, 75497472
v_lshrrev_b32 v128, 3, v118
v_and_b32 v129, 7, v118
v_lshlrev_b32 v129, 4, v129
v_mul_u32_u24 v130, 144, v128
v_add3_u32 v130, v130, v129, s52
v_lshl_add_u32 v131, v128, 12, v129
s_waitcnt lgkmcnt(0)
s_barrier
ds_read_b128 v[132:135], v130
ds_read_b128 v[136:139], v130 offset:4032
s_add_u32 s51, s50, 114688
s_waitcnt lgkmcnt(1)
buffer_store_dwordx4 v[132:135], v131, s[36:39], s50 offen sc1
s_waitcnt lgkmcnt(0)
buffer_store_dwordx4 v[136:139], v131, s[36:39], s51 offen sc1
s_lshl_b32 s50, s68, 20
s_add_u32 s50, s50, 16777216
s_add_u32 s24, s64, s50
s_addc_u32 s25, s65, 0
s_and_b32 s25, s25, 0xffff
s_mov_b32 s26, 1048576
s_mov_b32 s27, 0x20000
s_lshl_b32 s50, s68, 20
s_add_u32 s50, s50, 33554432
s_add_u32 s28, s64, s50
s_addc_u32 s29, s65, 0
s_and_b32 s29, s29, 0xffff
s_mov_b32 s30, 1048576
s_mov_b32 s31, 0x20000
s_lshl_b32 s50, s49, 11
s_add_u32 s42, s50, s67
s_add_u32 s43, s42, 0x10000
s_lshl_b32 s40, s70, 15
s_add_u32 s40, s40, s50
s_lshr_b32 s77, s34, 2
s_lshl_b32 s77, s77, 6
s_mov_b32 s76, 79693824
s_bitcmp1_b32 s66, 7
s_cselect_b32 s50, 0, 8192
s_add_u32 s77, s77, s50
s_add_u32 s77, s77, 79695872
s_lshl_b32 s78, s66, 2
s_add_u32 s78, s78, 79693824
s_lshl_b32 s51, s68, 6
s_add_u32 s51, s51, 79691776
s_mov_b32 s53, 0x8000
v_lshrrev_b32 v26, 2, v14
v_min_u32 v26, 60, v26
L_pollKV:
buffer_load_dword v27, v26, s[36:39], s51 offen sc1
s_waitcnt vmcnt(0)
v_cmp_eq_u32 vcc, 0, v27
s_sub_u32 s53, s53, 1
s_cbranch_scc1 L_pollokKV
s_cbranch_vccz L_pollokKV
s_sleep 1
s_branch L_pollKV
L_pollokKV:
s_add_u32 m0, s42, 0
s_nop 0
buffer_load_dwordx4 v10, s[24:27], s40 offen sc1 lds
buffer_load_dwordx4 v10, s[24:27], s40 offen offset:1024 sc1 lds
s_add_u32 m0, s42, 8192
s_nop 0
buffer_load_dwordx4 v11, s[24:27], s40 offen sc1 lds
buffer_load_dwordx4 v11, s[24:27], s40 offen offset:1024 sc1 lds
s_add_u32 m0, s42, 16384
s_nop 0
buffer_load_dwordx4 v12, s[24:27], s40 offen sc1 lds
buffer_load_dwordx4 v12, s[24:27], s40 offen offset:1024 sc1 lds
s_add_u32 m0, s42, 24576
s_nop 0
buffer_load_dwordx4 v13, s[24:27], s40 offen sc1 lds
buffer_load_dwordx4 v13, s[24:27], s40 offen offset:1024 sc1 lds
s_add_u32 s40, s40, 32768
s_waitcnt lgkmcnt(0)
s_barrier
s_cmp_eq_u32 s35, 0
s_cbranch_scc1 L_seg
s_lshl_b32 s50, s34, 14
buffer_store_dwordx4 a[0:3], v14, s[36:39], s50 offen offset:0 sc1
buffer_store_dwordx4 a[4:7], v14, s[36:39], s50 offen offset:1024 sc1
buffer_store_dwordx4 a[8:11], v14, s[36:39], s50 offen offset:2048 sc1
buffer_store_dwordx4 a[12:15], v14, s[36:39], s50 offen offset:3072 sc1
s_add_u32 s50, s50, 4096
buffer_store_dwordx4 a[16:19], v14, s[36:39], s50 offen offset:0 sc1
buffer_store_dwordx4 a[20:23], v14, s[36:39], s50 offen offset:1024 sc1
buffer_store_dwordx4 a[24:27], v14, s[36:39], s50 offen offset:2048 sc1
buffer_store_dwordx4 a[28:31], v14, s[36:39], s50 offen offset:3072 sc1
s_add_u32 s50, s50, 4096
buffer_store_dwordx4 a[32:35], v14, s[36:39], s50 offen offset:0 sc1
buffer_store_dwordx4 a[36:39], v14, s[36:39], s50 offen offset:1024 sc1
buffer_store_dwordx4 a[40:43], v14, s[36:39], s50 offen offset:2048 sc1
buffer_store_dwordx4 a[44:47], v14, s[36:39], s50 offen offset:3072 sc1
s_add_u32 s50, s50, 4096
buffer_store_dwordx4 a[48:51], v14, s[36:39], s50 offen offset:0 sc1
buffer_store_dwordx4 a[52:55], v14, s[36:39], s50 offen offset:1024 sc1
buffer_store_dwordx4 a[56:59], v14, s[36:39], s50 offen offset:2048 sc1
buffer_store_dwordx4 a[60:63], v14, s[36:39], s50 offen offset:3072 sc1
L_seg:
s_sub_u32 s41, s40, 32768
s_add_u32 m0, s42, 32768
s_nop 0
buffer_load_dwordx4 v10, s[24:27], s40 offen sc1 lds
buffer_load_dwordx4 v10, s[24:27], s40 offen offset:1024 sc1 lds
s_add_u32 m0, s42, 40960
s_nop 0
buffer_load_dwordx4 v11, s[24:27], s40 offen sc1 lds
buffer_load_dwordx4 v11, s[24:27], s40 offen offset:1024 sc1 lds
s_add_u32 m0, s42, 49152
s_nop 0
buffer_load_dwordx4 v12, s[24:27], s40 offen sc1 lds
buffer_load_dwordx4 v12, s[24:27], s40 offen offset:1024 sc1 lds
s_add_u32 m0, s42, 57344
s_nop 0
buffer_load_dwordx4 v13, s[24:27], s40 offen sc1 lds
buffer_load_dwordx4 v13, s[24:27], s40 offen offset:1024 sc1 lds
s_add_u32 s40, s40, 32768
L_seg2:
v_mov_b32 v128, 0
v_mov_b32 v129, 0
v_mov_b32 v130, 0
v_mov_b32 v131, 0
v_mov_b32 v132, 0
v_mov_b32 v133, 0
v_mov_b32 v134, 0
v_mov_b32 v135, 0
v_mov_b32 v136, 0
v_mov_b32 v137, 0
v_mov_b32 v138, 0
v_mov_b32 v139, 0
v_mov_b32 v140, 0
v_mov_b32 v141, 0
v_mov_b32 v142, 0
v_mov_b32 v143, 0
v_mov_b32 v144, 0
v_mov_b32 v145, 0
v_mov_b32 v146, 0
v_mov_b32 v147, 0
v_mov_b32 v148, 0
v_mov_b32 v149, 0
v_mov_b32 v150, 0
v_mov_b32 v151, 0
v_mov_b32 v152, 0
v_mov_b32 v153, 0
v_mov_b32 v154, 0
v_mov_b32 v155, 0
v_mov_b32 v156, 0
v_mov_b32 v157, 0
v_mov_b32 v158, 0
v_mov_b32 v159, 0
v_mov_b32 v160, 0
v_mov_b32 v161, 0
v_mov_b32 v162, 0
v_mov_b32 v163, 0
v_mov_b32 v164, 0
v_mov_b32 v165, 0
v_mov_b32 v166, 0
v_mov_b32 v167, 0
v_mov_b32 v168, 0
v_mov_b32 v169, 0
v_mov_b32 v170, 0
v_mov_b32 v171, 0
v_mov_b32 v172, 0
v_mov_b32 v173, 0
v_mov_b32 v174, 0
v_mov_b32 v175, 0
v_mov_b32 v176, 0
v_mov_b32 v177, 0
v_mov_b32 v178, 0
v_mov_b32 v179, 0
v_mov_b32 v180, 0
v_mov_b32 v181, 0
v_mov_b32 v182, 0
v_mov_b32 v183, 0
v_mov_b32 v184, 0
v_mov_b32 v185, 0
v_mov_b32 v186, 0
v_mov_b32 v187, 0
v_mov_b32 v188, 0
v_mov_b32 v189, 0
v_mov_b32 v190, 0
v_mov_b32 v191, 0
v_mov_b32 v192, 0
v_mov_b32 v193, 0
v_mov_b32 v194, 0
v_mov_b32 v195, 0
v_mov_b32 v196, 0
v_mov_b32 v197, 0
v_mov_b32 v198, 0
v_mov_b32 v199, 0
v_mov_b32 v200, 0
v_mov_b32 v201, 0
v_mov_b32 v202, 0
v_mov_b32 v203, 0
v_mov_b32 v204, 0
v_mov_b32 v205, 0
v_mov_b32 v206, 0
v_mov_b32 v207, 0
v_mov_b32 v208, 0
v_mov_b32 v209, 0
v_mov_b32 v210, 0
v_mov_b32 v211, 0
v_mov_b32 v212, 0
v_mov_b32 v213, 0
v_mov_b32 v214, 0
v_mov_b32 v215, 0
v_mov_b32 v216, 0
v_mov_b32 v217, 0
v_mov_b32 v218, 0
v_mov_b32 v219, 0
v_mov_b32 v220, 0
v_mov_b32 v221, 0
v_mov_b32 v222, 0
v_mov_b32 v223, 0
v_mov_b32 v224, 0
v_mov_b32 v225, 0
v_mov_b32 v226, 0
v_mov_b32 v227, 0
v_mov_b32 v228, 0
v_mov_b32 v229, 0
v_mov_b32 v230, 0
v_mov_b32 v231, 0
v_mov_b32 v232, 0
v_mov_b32 v233, 0
v_mov_b32 v234, 0
v_mov_b32 v235, 0
v_mov_b32 v236, 0
v_mov_b32 v237, 0
v_mov_b32 v238, 0
v_mov_b32 v239, 0
v_mov_b32 v240, 0
v_mov_b32 v241, 0
v_mov_b32 v242, 0
v_mov_b32 v243, 0
v_mov_b32 v244, 0
v_mov_b32 v245, 0
v_mov_b32 v246, 0
v_mov_b32 v247, 0
v_mov_b32 v248, 0
v_mov_b32 v249, 0
v_mov_b32 v250, 0
v_mov_b32 v251, 0
v_mov_b32 v252, 0
v_mov_b32 v253, 0
v_mov_b32 v254, 0
v_mov_b32 v255, 0
v_mov_b32 v40, 0
v_mov_b32 v41, 0
v_mov_b32 v42, 0
v_mov_b32 v43, 0
v_mov_b32 v44, 0
v_mov_b32 v45, 0
v_mov_b32 v46, 0
v_mov_b32 v47, 0
v_mov_b32 v16, 0
v_mov_b32 v17, 0
v_mov_b32 v18, 0
v_mov_b32 v19, 0
s_mov_b32 s46, 0xff800000
s_mov_b32 s48, 0xff7fffff
s_mov_b32 s47, 0
s_mov_b32 s44, s71
s_lshl_b32 s45, s69, 1
s_sub_u32 s45, s45, s70
s_cmp_eq_u32 s74, 2
s_cselect_b32 s50, 1, s35
s_cmp_eq_u32 s50, 0
s_cbranch_scc1 L_w8
s_waitcnt vmcnt(24)
s_branch L_w8d
L_w8:
s_waitcnt vmcnt(8)
L_w8d:
s_barrier
ds_read_b128 a[64:67], v8 offset:0
ds_read_b128 a[68:71], v8 offset:1024
ds_read_b128 a[72:75], v8 offset:2048
ds_read_b128 a[76:79], v8 offset:3072
ds_read_b128 a[80:83], v8 offset:4096
ds_read_b128 a[84:87], v8 offset:5120
ds_read_b128 a[88:91], v8 offset:6144
ds_read_b128 a[92:95], v8 offset:7168
s_waitcnt lgkmcnt(4)
v_mfma_f32_16x16x32_bf16 v[64:67], a[64:67], a[0:3], v[40:43]
ds_read_b128 a[96:99], v8 offset:8192
v_mfma_f32_16x16x32_bf16 v[68:71], a[64:67], a[32:35], v[44:47]
s_add_u32 m0, s43, 0
v_mfma_f32_16x16x32_bf16 v[64:67], a[68:71], a[4:7], v[64:67]
ds_read_b128 a[100:103], v8 offset:9216
v_mfma_f32_16x16x32_bf16 v[68:71], a[68:71], a[36:39], v[68:71]
buffer_load_dwordx4 v10, s[28:31], s41 offen sc1 lds
v_mfma_f32_16x16x32_bf16 v[64:67], a[72:75], a[8:11], v[64:67]
ds_read_b128 a[104:107], v8 offset:10240
v_mfma_f32_16x16x32_bf16 v[68:71], a[72:75], a[40:43], v[68:71]
v_mfma_f32_16x16x32_bf16 v[64:67], a[76:79], a[12:15], v[64:67]
ds_read_b128 a[108:111], v8 offset:11264
v_mfma_f32_16x16x32_bf16 v[68:71], a[76:79], a[44:47], v[68:71]
s_waitcnt lgkmcnt(4)
v_mfma_f32_16x16x32_bf16 v[64:67], a[80:83], a[16:19], v[64:67]
ds_read_b128 a[112:115], v8 offset:12288
v_mfma_f32_16x16x32_bf16 v[68:71], a[80:83], a[48:51], v[68:71]
v_mfma_f32_16x16x32_bf16 v[64:67], a[84:87], a[20:23], v[64:67]
ds_read_b128 a[116:119], v8 offset:13312
v_mfma_f32_16x16x32_bf16 v[68:71], a[84:87], a[52:55], v[68:71]
buffer_load_dwordx4 v10, s[28:31], s41 offen offset:1024 sc1 lds
v_mfma_f32_16x16x32_bf16 v[64:67], a[88:91], a[24:27], v[64:67]
ds_read_b128 a[120:123], v8 offset:14336
v_mfma_f32_16x16x32_bf16 v[68:71], a[88:91], a[56:59], v[68:71]
v_mfma_f32_16x16x32_bf16 v[64:67], a[92:95], a[28:31], v[64:67]
ds_read_b128 a[124:127], v8 offset:15360
v_mfma_f32_16x16x32_bf16 v[68:71], a[92:95], a[60:63], v[68:71]
s_waitcnt lgkmcnt(4)
v_mfma_f32_16x16x32_bf16 v[72:75], a[96:99], a[0:3], v[40:43]
ds_read_b128 a[64:67], v8 offset:16384
v_mfma_f32_16x16x32_bf16 v[76:79], a[96:99], a[32:35], v[44:47]
s_add_u32 m0, s43, 8192
v_mfma_f32_16x16x32_bf16 v[72:75], a[100:103], a[4:7], v[72:75]
ds_read_b128 a[68:71], v8 offset:17408
v_mfma_f32_16x16x32_bf16 v[76:79], a[100:103], a[36:39], v[76:79]
buffer_load_dwordx4 v11, s[28:31], s41 offen sc1 lds
v_mfma_f32_16x16x32_bf16 v[72:75], a[104:107], a[8:11], v[72:75]
ds_read_b128 a[72:75], v8 offset:18432
v_mfma_f32_16x16x32_bf16 v[76:79], a[104:107], a[40:43], v[76:79]
v_mfma_f32_16x16x32_bf16 v[72:75], a[108:111], a[12:15], v[72:75]
ds_read_b128 a[76:79], v8 offset:19456
v_mfma_f32_16x16x32_bf16 v[76:79], a[108:111], a[44:47], v[76:79]
s_waitcnt lgkmcnt(4)
v_mfma_f32_16x16x32_bf16 v[72:75], a[112:115], a[16:19], v[72:75]
ds_read_b128 a[80:83], v8 offset:20480
v_mfma_f32_16x16x32_bf16 v[76:79], a[112:115], a[48:51], v[76:79]
v_mfma_f32_16x16x32_bf16 v[72:75], a[116:119], a[20:23], v[72:75]
ds_read_b128 a[84:87], v8 offset:21504
v_mfma_f32_16x16x32_bf16 v[76:79], a[116:119], a[52:55], v[76:79]
buffer_load_dwordx4 v11, s[28:31], s41 offen offset:1024 sc1 lds
v_mfma_f32_16x16x32_bf16 v[72:75], a[120:123], a[24:27], v[72:75]
ds_read_b128 a[88:91], v8 offset:22528
v_mfma_f32_16x16x32_bf16 v[76:79], a[120:123], a[56:59], v[76:79]
v_mfma_f32_16x16x32_bf16 v[72:75], a[124:127], a[28:31], v[72:75]
ds_read_b128 a[92:95], v8 offset:23552
v_mfma_f32_16x16x32_bf16 v[76:79], a[124:127], a[60:63], v[76:79]
s_waitcnt lgkmcnt(4)
v_mfma_f32_16x16x32_bf16 v[80:83], a[64:67], a[0:3], v[40:43]
ds_read_b128 a[96:99], v8 offset:24576
v_mfma_f32_16x16x32_bf16 v[84:87], a[64:67], a[32:35], v[44:47]
s_add_u32 m0, s43, 16384
v_mfma_f32_16x16x32_bf16 v[80:83], a[68:71], a[4:7], v[80:83]
ds_read_b128 a[100:103], v8 offset:25600
v_mfma_f32_16x16x32_bf16 v[84:87], a[68:71], a[36:39], v[84:87]
buffer_load_dwordx4 v12, s[28:31], s41 offen sc1 lds
v_mfma_f32_16x16x32_bf16 v[80:83], a[72:75], a[8:11], v[80:83]
ds_read_b128 a[104:107], v8 offset:26624
v_mfma_f32_16x16x32_bf16 v[84:87], a[72:75], a[40:43], v[84:87]
v_mfma_f32_16x16x32_bf16 v[80:83], a[76:79], a[12:15], v[80:83]
ds_read_b128 a[108:111], v8 offset:27648
v_mfma_f32_16x16x32_bf16 v[84:87], a[76:79], a[44:47], v[84:87]
s_waitcnt lgkmcnt(4)
v_mfma_f32_16x16x32_bf16 v[80:83], a[80:83], a[16:19], v[80:83]
ds_read_b128 a[112:115], v8 offset:28672
v_mfma_f32_16x16x32_bf16 v[84:87], a[80:83], a[48:51], v[84:87]
v_mfma_f32_16x16x32_bf16 v[80:83], a[84:87], a[20:23], v[80:83]
ds_read_b128 a[116:119], v8 offset:29696
v_mfma_f32_16x16x32_bf16 v[84:87], a[84:87], a[52:55], v[84:87]
buffer_load_dwordx4 v12, s[28:31], s41 offen offset:1024 sc1 lds
v_mfma_f32_16x16x32_bf16 v[80:83], a[88:91], a[24:27], v[80:83]
ds_read_b128 a[120:123], v8 offset:30720
v_mfma_f32_16x16x32_bf16 v[84:87], a[88:91], a[56:59], v[84:87]
v_mfma_f32_16x16x32_bf16 v[80:83], a[92:95], a[28:31], v[80:83]
ds_read_b128 a[124:127], v8 offset:31744
v_mfma_f32_16x16x32_bf16 v[84:87], a[92:95], a[60:63], v[84:87]
s_waitcnt lgkmcnt(4)
v_mfma_f32_16x16x32_bf16 v[88:91], a[96:99], a[0:3], v[40:43]
s_waitcnt vmcnt(14)
s_barrier
v_mfma_f32_16x16x32_bf16 v[92:95], a[96:99], a[32:35], v[44:47]
s_add_u32 m0, s43, 24576
v_mfma_f32_16x16x32_bf16 v[88:91], a[100:103], a[4:7], v[88:91]
v_mfma_f32_16x16x32_bf16 v[92:95], a[100:103], a[36:39], v[92:95]
buffer_load_dwordx4 v13, s[28:31], s41 offen sc1 lds
v_mfma_f32_16x16x32_bf16 v[88:91], a[104:107], a[8:11], v[88:91]
v_mfma_f32_16x16x32_bf16 v[92:95], a[104:107], a[40:43], v[92:95]
v_mfma_f32_16x16x32_bf16 v[88:91], a[108:111], a[12:15], v[88:91]
v_mfma_f32_16x16x32_bf16 v[92:95], a[108:111], a[44:47], v[92:95]
s_waitcnt lgkmcnt(0)
v_mfma_f32_16x16x32_bf16 v[88:91], a[112:115], a[16:19], v[88:91]
v_mfma_f32_16x16x32_bf16 v[92:95], a[112:115], a[48:51], v[92:95]
v_mfma_f32_16x16x32_bf16 v[88:91], a[116:119], a[20:23], v[88:91]
v_mfma_f32_16x16x32_bf16 v[92:95], a[116:119], a[52:55], v[92:95]
buffer_load_dwordx4 v13, s[28:31], s41 offen offset:1024 sc1 lds
v_mfma_f32_16x16x32_bf16 v[88:91], a[120:123], a[24:27], v[88:91]
v_mfma_f32_16x16x32_bf16 v[92:95], a[120:123], a[56:59], v[92:95]
s_add_u32 s41, s41, 32768
v_mfma_f32_16x16x32_bf16 v[88:91], a[124:127], a[28:31], v[88:91]
v_mfma_f32_16x16x32_bf16 v[92:95], a[124:127], a[60:63], v[92:95]
s_cmp_eq_u32 s35, 0
s_cbranch_scc1 L_nopub
s_sub_u32 s50, s77, 8192
v_mov_b32 v27, 1
s_cmp_eq_u32 s49, 0
s_cselect_b32 exec_lo, 1, 0
s_mov_b32 exec_hi, 0
buffer_store_dword v27, off, s[36:39], s50 sc1
s_mov_b64 exec, -1
s_mov_b32 s35, 0
L_nopub:
s_add_u32 m0, s42, 0
s_cmp_le_i32 s45, 0
s_cbranch_scc1 L_maskF
L_maskretF:
s_nop 7
buffer_load_dwordx4 v10, s[24:27], s40 offen sc1 lds
v_max3_f32 v20, v64, v65, v66
v_max3_f32 v21, v67, v68, v69
v_max3_f32 v20, v20, v70, v71
v_max3_f32 v21, v21, v72, v73
v_max3_f32 v20, v20, v74, v75
v_max3_f32 v21, v21, v76, v77
v_max3_f32 v20, v20, v78, v79
v_max3_f32 v21, v21, v80, v81
v_max3_f32 v20, v20, v82, v83
v_max3_f32 v21, v21, v84, v85
v_max3_f32 v20, v20, v86, v87
v_max3_f32 v21, v21, v88, v89
v_max3_f32 v20, v20, v90, v91
v_max3_f32 v21, v21, v92, v93
v_max3_f32 v20, v20, v94, v95
buffer_load_dwordx4 v10, s[24:27], s40 offen offset:1024 sc1 lds
v_max_f32 v20, v20, v21
v_cmp_lt_f32 vcc, s46, v20
s_cbranch_vccnz L_rareF
L_rareretF:
v_exp_f32 v64, v64
v_exp_f32 v65, v65
v_add_f32 v16, v16, v64
s_add_u32 m0, s42, 8192
v_exp_f32 v66, v66
buffer_load_dwordx4 v11, s[24:27], s40 offen sc1 lds
v_add_f32 v17, v17, v65
v_exp_f32 v67, v67
v_add_f32 v16, v16, v66
v_exp_f32 v68, v68
v_add_f32 v17, v17, v67
buffer_load_dwordx4 v11, s[24:27], s40 offen offset:1024 sc1 lds
v_exp_f32 v69, v69
v_add_f32 v18, v18, v68
v_exp_f32 v70, v70
v_add_f32 v19, v19, v69
s_add_u32 m0, s42, 16384
v_exp_f32 v71, v71
buffer_load_dwordx4 v12, s[24:27], s40 offen sc1 lds
v_add_f32 v18, v18, v70
v_exp_f32 v72, v72
v_add_f32 v19, v19, v71
v_exp_f32 v73, v73
v_add_f32 v16, v16, v72
v_exp_f32 v74, v74
buffer_load_dwordx4 v12, s[24:27], s40 offen offset:1024 sc1 lds
v_add_f32 v17, v17, v73
v_exp_f32 v75, v75
v_add_f32 v16, v16, v74
s_waitcnt vmcnt(14)
s_barrier
ds_read_b128 a[64:67], v8 offset:32768
s_add_u32 m0, s42, 24576
v_exp_f32 v76, v76
ds_read_b128 a[68:71], v8 offset:33792
buffer_load_dwordx4 v13, s[24:27], s40 offen sc1 lds
v_add_f32 v17, v17, v75
ds_read_b128 a[72:75], v8 offset:34816
v_exp_f32 v77, v77
ds_read_b128 a[76:79], v8 offset:35840
v_add_f32 v18, v18, v76
v_exp_f32 v78, v78
ds_read_b128 a[80:83], v8 offset:36864
v_add_f32 v19, v19, v77
v_exp_f32 v79, v79
ds_read_b128 a[84:87], v8 offset:37888
buffer_load_dwordx4 v13, s[24:27], s40 offen offset:1024 sc1 lds
v_add_f32 v18, v18, v78
s_nop 0
ds_read_b128 a[88:91], v8 offset:38912
s_add_u32 s40, s40, 32768
v_add_f32 v19, v19, v79
s_sub_u32 s45, s45, 1
ds_read_b128 a[92:95], v8 offset:39936
v_mov_b32 v27, 1
s_cmp_eq_u32 s49, 0
s_cselect_b32 exec_lo, 1, 0
s_mov_b32 exec_hi, 0
buffer_store_dword v27, off, s[36:39], s78 sc1
s_mov_b64 exec, -1
s_sub_u32 s44, s44, 1
s_cmp_eq_u32 s44, 0
s_cbranch_scc1 L_end0
L_loop:
s_waitcnt lgkmcnt(4)
v_mfma_f32_16x16x32_bf16 v[96:99], a[64:67], a[0:3], v[40:43]
ds_read_b128 a[96:99], v8 offset:40960
v_mfma_f32_16x16x32_bf16 v[100:103], a[64:67], a[32:35], v[44:47]
s_add_u32 m0, s43, 32768
v_cvt_pk_bf16_f32 v48, v64, v65
v_mfma_f32_16x16x32_bf16 v[96:99], a[68:71], a[4:7], v[96:99]
ds_read_b128 a[100:103], v8 offset:41984
v_mfma_f32_16x16x32_bf16 v[100:103], a[68:71], a[36:39], v[100:103]
buffer_load_dwordx4 v10, s[28:31], s41 offen sc1 lds
v_cvt_pk_bf16_f32 v49, v66, v67
v_mfma_f32_16x16x32_bf16 v[96:99], a[72:75], a[8:11], v[96:99]
ds_read_b128 a[104:107], v8 offset:43008
v_mfma_f32_16x16x32_bf16 v[100:103], a[72:75], a[40:43], v[100:103]
v_exp_f32 v80, v80
v_mfma_f32_16x16x32_bf16 v[96:99], a[76:79], a[12:15], v[96:99]
ds_read_b128 a[108:111], v8 offset:44032
v_cvt_pk_bf16_f32 v50, v72, v73
v_mfma_f32_16x16x32_bf16 v[100:103], a[76:79], a[44:47], v[100:103]
v_exp_f32 v81, v81
s_waitcnt lgkmcnt(4)
v_mfma_f32_16x16x32_bf16 v[96:99], a[80:83], a[16:19], v[96:99]
ds_read_b128 a[112:115], v8 offset:45056
v_add_f32 v16, v16, v80
v_mfma_f32_16x16x32_bf16 v[100:103], a[80:83], a[48:51], v[100:103]
v_exp_f32 v82, v82
v_mfma_f32_16x16x32_bf16 v[96:99], a[84:87], a[20:23], v[96:99]
ds_read_b128 a[116:119], v8 offset:46080
v_add_f32 v17, v17, v81
v_mfma_f32_16x16x32_bf16 v[100:103], a[84:87], a[52:55], v[100:103]
buffer_load_dwordx4 v10, s[28:31], s41 offen offset:1024 sc1 lds
v_cvt_pk_bf16_f32 v51, v74, v75
v_mfma_f32_16x16x32_bf16 v[96:99], a[88:91], a[24:27], v[96:99]
ds_read_b128 a[120:123], v8 offset:47104
v_cvt_pk_bf16_f32 v52, v68, v69
v_mfma_f32_16x16x32_bf16 v[100:103], a[88:91], a[56:59], v[100:103]
v_exp_f32 v83, v83
v_mfma_f32_16x16x32_bf16 v[96:99], a[92:95], a[28:31], v[96:99]
ds_read_b128 a[124:127], v8 offset:48128
v_add_f32 v16, v16, v82
v_mfma_f32_16x16x32_bf16 v[100:103], a[92:95], a[60:63], v[100:103]
v_exp_f32 v84, v84
s_waitcnt lgkmcnt(4)
v_mfma_f32_16x16x32_bf16 v[104:107], a[96:99], a[0:3], v[40:43]
ds_read_b128 a[64:67], v8 offset:49152
v_add_f32 v17, v17, v83
v_mfma_f32_16x16x32_bf16 v[108:111], a[96:99], a[32:35], v[44:47]
s_add_u32 m0, s43, 40960
v_cvt_pk_bf16_f32 v53, v70, v71
v_mfma_f32_16x16x32_bf16 v[104:107], a[100:103], a[4:7], v[104:107]
ds_read_b128 a[68:71], v8 offset:50176
v_cvt_pk_bf16_f32 v54, v76, v77
v_mfma_f32_16x16x32_bf16 v[108:111], a[100:103], a[36:39], v[108:111]
buffer_load_dwordx4 v11, s[28:31], s41 offen sc1 lds
v_cvt_pk_bf16_f32 v55, v78, v79
v_mfma_f32_16x16x32_bf16 v[104:107], a[104:107], a[8:11], v[104:107]
ds_read_b128 a[72:75], v8 offset:51200
v_mfma_f32_16x16x32_bf16 v[108:111], a[104:107], a[40:43], v[108:111]
v_exp_f32 v85, v85
v_mfma_f32_16x16x32_bf16 v[104:107], a[108:111], a[12:15], v[104:107]
ds_read_b128 a[76:79], v8 offset:52224
v_add_f32 v18, v18, v84
v_mfma_f32_16x16x32_bf16 v[108:111], a[108:111], a[44:47], v[108:111]
v_exp_f32 v86, v86
s_waitcnt lgkmcnt(4)
v_mfma_f32_16x16x32_bf16 v[104:107], a[112:115], a[16:19], v[104:107]
ds_read_b128 a[80:83], v8 offset:53248
v_add_f32 v19, v19, v85
v_mfma_f32_16x16x32_bf16 v[108:111], a[112:115], a[48:51], v[108:111]
v_exp_f32 v87, v87
v_mfma_f32_16x16x32_bf16 v[104:107], a[116:119], a[20:23], v[104:107]
ds_read_b128 a[84:87], v8 offset:54272
v_add_f32 v18, v18, v86
v_mfma_f32_16x16x32_bf16 v[108:111], a[116:119], a[52:55], v[108:111]
buffer_load_dwordx4 v11, s[28:31], s41 offen offset:1024 sc1 lds
v_mfma_f32_16x16x32_bf16 v[104:107], a[120:123], a[24:27], v[104:107]
ds_read_b128 a[88:91], v8 offset:55296
v_mfma_f32_16x16x32_bf16 v[108:111], a[120:123], a[56:59], v[108:111]
v_exp_f32 v88, v88
v_mfma_f32_16x16x32_bf16 v[104:107], a[124:127], a[28:31], v[104:107]
ds_read_b128 a[92:95], v8 offset:56320
v_add_f32 v19, v19, v87
v_mfma_f32_16x16x32_bf16 v[108:111], a[124:127], a[60:63], v[108:111]
v_exp_f32 v89, v89
s_waitcnt lgkmcnt(4)
v_mfma_f32_16x16x32_bf16 v[112:115], a[64:67], a[0:3], v[40:43]
ds_read_b128 a[96:99], v8 offset:57344
v_add_f32 v16, v16, v88
v_mfma_f32_16x16x32_bf16 v[116:119], a[64:67], a[32:35], v[44:47]
s_add_u32 m0, s43, 49152
v_mfma_f32_16x16x32_bf16 v[112:115], a[68:71], a[4:7], v[112:115]
ds_read_b128 a[100:103], v8 offset:58368
v_mfma_f32_16x16x32_bf16 v[116:119], a[68:71], a[36:39], v[116:119]
buffer_load_dwordx4 v12, s[28:31], s41 offen sc1 lds
v_mfma_f32_16x16x32_bf16 v[112:115], a[72:75], a[8:11], v[112:115]
ds_read_b128 a[104:107], v8 offset:59392
v_mfma_f32_16x16x32_bf16 v[116:119], a[72:75], a[40:43], v[116:119]
v_exp_f32 v90, v90
v_mfma_f32_16x16x32_bf16 v[112:115], a[76:79], a[12:15], v[112:115]
ds_read_b128 a[108:111], v8 offset:60416
v_add_f32 v17, v17, v89
v_mfma_f32_16x16x32_bf16 v[116:119], a[76:79], a[44:47], v[116:119]
v_exp_f32 v91, v91
s_waitcnt lgkmcnt(4)
v_mfma_f32_16x16x32_bf16 v[112:115], a[80:83], a[16:19], v[112:115]
ds_read_b128 a[112:115], v8 offset:61440
v_add_f32 v16, v16, v90
v_mfma_f32_16x16x32_bf16 v[116:119], a[80:83], a[48:51], v[116:119]
v_exp_f32 v92, v92
v_mfma_f32_16x16x32_bf16 v[112:115], a[84:87], a[20:23], v[112:115]
ds_read_b128 a[116:119], v8 offset:62464
v_add_f32 v17, v17, v91
v_mfma_f32_16x16x32_bf16 v[116:119], a[84:87], a[52:55], v[116:119]
buffer_load_dwordx4 v12, s[28:31], s41 offen offset:1024 sc1 lds
v_mfma_f32_16x16x32_bf16 v[112:115], a[88:91], a[24:27], v[112:115]
ds_read_b128 a[120:123], v8 offset:63488
v_mfma_f32_16x16x32_bf16 v[116:119], a[88:91], a[56:59], v[116:119]
v_exp_f32 v93, v93
v_mfma_f32_16x16x32_bf16 v[112:115], a[92:95], a[28:31], v[112:115]
ds_read_b128 a[124:127], v8 offset:64512
v_add_f32 v18, v18, v92
v_mfma_f32_16x16x32_bf16 v[116:119], a[92:95], a[60:63], v[116:119]
v_exp_f32 v94, v94
s_waitcnt lgkmcnt(4)
v_mfma_f32_16x16x32_bf16 v[120:123], a[96:99], a[0:3], v[40:43]
s_waitcnt vmcnt(14)
s_barrier
v_mfma_f32_16x16x32_bf16 v[124:127], a[96:99], a[32:35], v[44:47]
ds_read_b128 a[64:67], v9 offset:0
s_add_u32 m0, s43, 57344
v_mfma_f32_16x16x32_bf16 v[120:123], a[100:103], a[4:7], v[120:123]
v_add_f32 v19, v19, v93
v_mfma_f32_16x16x32_bf16 v[124:127], a[100:103], a[36:39], v[124:127]
ds_read_b128 a[68:71], v9 offset:1024
buffer_load_dwordx4 v13, s[28:31], s41 offen sc1 lds
v_mfma_f32_16x16x32_bf16 v[120:123], a[104:107], a[8:11], v[120:123]
v_exp_f32 v95, v95
v_mfma_f32_16x16x32_bf16 v[124:127], a[104:107], a[40:43], v[124:127]
ds_read_b128 a[72:75], v9 offset:2048
v_add_f32 v18, v18, v94
v_mfma_f32_16x16x32_bf16 v[120:123], a[108:111], a[12:15], v[120:123]
s_nop 0
v_add_f32 v19, v19, v95
v_mfma_f32_16x16x32_bf16 v[124:127], a[108:111], a[44:47], v[124:127]
ds_read_b128 a[76:79], v9 offset:3072
v_cvt_pk_bf16_f32 v56, v80, v81
s_waitcnt lgkmcnt(4)
v_mfma_f32_16x16x32_bf16 v[120:123], a[112:115], a[16:19], v[120:123]
v_cvt_pk_bf16_f32 v57, v82, v83
v_cvt_pk_bf16_f32 v58, v88, v89
v_mfma_f32_16x16x32_bf16 v[124:127], a[112:115], a[48:51], v[124:127]
ds_read_b128 a[80:83], v9 offset:4096
v_cvt_pk_bf16_f32 v59, v90, v91
v_mfma_f32_16x16x32_bf16 v[120:123], a[116:119], a[20:23], v[120:123]
v_cvt_pk_bf16_f32 v60, v84, v85
v_cvt_pk_bf16_f32 v61, v86, v87
v_mfma_f32_16x16x32_bf16 v[124:127], a[116:119], a[52:55], v[124:127]
ds_read_b128 a[84:87], v9 offset:5120
buffer_load_dwordx4 v13, s[28:31], s41 offen offset:1024 sc1 lds
v_mfma_f32_16x16x32_bf16 v[120:123], a[120:123], a[24:27], v[120:123]
v_cvt_pk_bf16_f32 v62, v92, v93
v_cvt_pk_bf16_f32 v63, v94, v95
v_mfma_f32_16x16x32_bf16 v[124:127], a[120:123], a[56:59], v[124:127]
ds_read_b128 a[88:91], v9 offset:6144
s_add_u32 s41, s41, 32768
v_mfma_f32_16x16x32_bf16 v[120:123], a[124:127], a[28:31], v[120:123]
v_mfma_f32_16x16x32_bf16 v[124:127], a[124:127], a[60:63], v[124:127]
ds_read_b128 a[92:95], v9 offset:7168
s_waitcnt lgkmcnt(4)
v_mfma_f32_16x16x32_bf16 v[128:131], a[64:67], v[48:51], v[128:131]
ds_read_b128 a[96:99], v9 offset:8192
v_mfma_f32_16x16x32_bf16 v[132:135], a[64:67], v[52:55], v[132:135]
s_add_u32 m0, s42, 32768
s_cmp_le_i32 s45, 0
v_mfma_f32_16x16x32_bf16 v[136:139], a[68:71], v[48:51], v[136:139]
ds_read_b128 a[100:103], v9 offset:9216
s_cbranch_scc1 L_mask1
L_maskret1:
v_mfma_f32_16x16x32_bf16 v[140:143], a[68:71], v[52:55], v[140:143]
buffer_load_dwordx4 v10, s[24:27], s40 offen sc1 lds
v_max3_f32 v20, v96, v97, v98
v_mfma_f32_16x16x32_bf16 v[144:147], a[72:75], v[48:51], v[144:147]
ds_read_b128 a[104:107], v9 offset:10240
v_max3_f32 v21, v99, v100, v101
v_mfma_f32_16x16x32_bf16 v[148:151], a[72:75], v[52:55], v[148:151]
v_max3_f32 v20, v20, v102, v103
v_max3_f32 v21, v21, v104, v105
v_mfma_f32_16x16x32_bf16 v[152:155], a[76:79], v[48:51], v[152:155]
ds_read_b128 a[108:111], v9 offset:11264
v_max3_f32 v20, v20, v106, v107
v_mfma_f32_16x16x32_bf16 v[156:159], a[76:79], v[52:55], v[156:159]
v_max3_f32 v21, v21, v108, v109
v_max3_f32 v20, v20, v110, v111
s_waitcnt lgkmcnt(4)
v_mfma_f32_16x16x32_bf16 v[160:163], a[80:83], v[48:51], v[160:163]
ds_read_b128 a[112:115], v9 offset:12288
v_max3_f32 v21, v21, v112, v113
v_mfma_f32_16x16x32_bf16 v[164:167], a[80:83], v[52:55], v[164:167]
v_max3_f32 v20, v20, v114, v115
v_max3_f32 v21, v21, v116, v117
v_mfma_f32_16x16x32_bf16 v[168:171], a[84:87], v[48:51], v[168:171]
ds_read_b128 a[116:119], v9 offset:13312
v_max3_f32 v20, v20, v118, v119
v_mfma_f32_16x16x32_bf16 v[172:175], a[84:87], v[52:55], v[172:175]
buffer_load_dwordx4 v10, s[24:27], s40 offen offset:1024 sc1 lds
v_max3_f32 v21, v21, v120, v121
v_mfma_f32_16x16x32_bf16 v[176:179], a[88:91], v[48:51], v[176:179]
ds_read_b128 a[120:123], v9 offset:14336
v_max3_f32 v20, v20, v122, v123
v_mfma_f32_16x16x32_bf16 v[180:183], a[88:91], v[52:55], v[180:183]
v_max3_f32 v21, v21, v124, v125
v_max3_f32 v20, v20, v126, v127
v_mfma_f32_16x16x32_bf16 v[184:187], a[92:95], v[48:51], v[184:187]
ds_read_b128 a[124:127], v9 offset:15360
v_max_f32 v20, v20, v21
v_mfma_f32_16x16x32_bf16 v[188:191], a[92:95], v[52:55], v[188:191]
v_cmp_lt_f32 vcc, s46, v20
s_cbranch_vccnz L_rare1
L_rareret1:
s_waitcnt lgkmcnt(4)
v_mfma_f32_16x16x32_bf16 v[192:195], a[96:99], v[48:51], v[192:195]
ds_read_b128 a[64:67], v9 offset:16384
v_mfma_f32_16x16x32_bf16 v[196:199], a[96:99], v[52:55], v[196:199]
s_add_u32 m0, s42, 40960
v_mfma_f32_16x16x32_bf16 v[200:203], a[100:103], v[48:51], v[200:203]
ds_read_b128 a[68:71], v9 offset:17408
v_mfma_f32_16x16x32_bf16 v[204:207], a[100:103], v[52:55], v[204:207]
buffer_load_dwordx4 v11, s[24:27], s40 offen sc1 lds
v_mfma_f32_16x16x32_bf16 v[208:211], a[104:107], v[48:51], v[208:211]
ds_read_b128 a[72:75], v9 offset:18432
v_mfma_f32_16x16x32_bf16 v[212:215], a[104:107], v[52:55], v[212:215]
v_exp_f32 v96, v96
v_mfma_f32_16x16x32_bf16 v[216:219], a[108:111], v[48:51], v[216:219]
ds_read_b128 a[76:79], v9 offset:19456
v_mfma_f32_16x16x32_bf16 v[220:223], a[108:111], v[52:55], v[220:223]
v_exp_f32 v97, v97
s_waitcnt lgkmcnt(4)
v_mfma_f32_16x16x32_bf16 v[224:227], a[112:115], v[48:51], v[224:227]
ds_read_b128 a[80:83], v9 offset:20480
v_add_f32 v16, v16, v96
v_mfma_f32_16x16x32_bf16 v[228:231], a[112:115], v[52:55], v[228:231]
v_exp_f32 v98, v98
v_mfma_f32_16x16x32_bf16 v[232:235], a[116:119], v[48:51], v[232:235]
ds_read_b128 a[84:87], v9 offset:21504
v_add_f32 v17, v17, v97
v_mfma_f32_16x16x32_bf16 v[236:239], a[116:119], v[52:55], v[236:239]
buffer_load_dwordx4 v11, s[24:27], s40 offen offset:1024 sc1 lds
v_mfma_f32_16x16x32_bf16 v[240:243], a[120:123], v[48:51], v[240:243]
ds_read_b128 a[88:91], v9 offset:22528
v_mfma_f32_16x16x32_bf16 v[244:247], a[120:123], v[52:55], v[244:247]
v_exp_f32 v99, v99
v_mfma_f32_16x16x32_bf16 v[248:251], a[124:127], v[48:51], v[248:251]
ds_read_b128 a[92:95], v9 offset:23552
v_add_f32 v16, v16, v98
v_mfma_f32_16x16x32_bf16 v[252:255], a[124:127], v[52:55], v[252:255]
v_exp_f32 v100, v100
s_waitcnt lgkmcnt(4)
v_mfma_f32_16x16x32_bf16 v[128:131], a[64:67], v[56:59], v[128:131]
ds_read_b128 a[96:99], v9 offset:24576
v_add_f32 v17, v17, v99
v_mfma_f32_16x16x32_bf16 v[132:135], a[64:67], v[60:63], v[132:135]
s_add_u32 m0, s42, 49152
v_mfma_f32_16x16x32_bf16 v[136:139], a[68:71], v[56:59], v[136:139]
ds_read_b128 a[100:103], v9 offset:25600
v_mfma_f32_16x16x32_bf16 v[140:143], a[68:71], v[60:63], v[140:143]
buffer_load_dwordx4 v12, s[24:27], s40 offen sc1 lds
v_mfma_f32_16x16x32_bf16 v[144:147], a[72:75], v[56:59], v[144:147]
ds_read_b128 a[104:107], v9 offset:26624
v_mfma_f32_16x16x32_bf16 v[148:151], a[72:75], v[60:63], v[148:151]
v_exp_f32 v101, v101
v_mfma_f32_16x16x32_bf16 v[152:155], a[76:79], v[56:59], v[152:155]
ds_read_b128 a[108:111], v9 offset:27648
v_add_f32 v18, v18, v100
v_mfma_f32_16x16x32_bf16 v[156:159], a[76:79], v[60:63], v[156:159]
v_exp_f32 v102, v102
s_waitcnt lgkmcnt(4)
v_mfma_f32_16x16x32_bf16 v[160:163], a[80:83], v[56:59], v[160:163]
ds_read_b128 a[112:115], v9 offset:28672
v_add_f32 v19, v19, v101
v_mfma_f32_16x16x32_bf16 v[164:167], a[80:83], v[60:63], v[164:167]
v_exp_f32 v103, v103
v_mfma_f32_16x16x32_bf16 v[168:171], a[84:87], v[56:59], v[168:171]
ds_read_b128 a[116:119], v9 offset:29696
v_add_f32 v18, v18, v102
v_mfma_f32_16x16x32_bf16 v[172:175], a[84:87], v[60:63], v[172:175]
buffer_load_dwordx4 v12, s[24:27], s40 offen offset:1024 sc1 lds
v_mfma_f32_16x16x32_bf16 v[176:179], a[88:91], v[56:59], v[176:179]
ds_read_b128 a[120:123], v9 offset:30720
v_mfma_f32_16x16x32_bf16 v[180:183], a[88:91], v[60:63], v[180:183]
v_exp_f32 v104, v104
v_mfma_f32_16x16x32_bf16 v[184:187], a[92:95], v[56:59], v[184:187]
ds_read_b128 a[124:127], v9 offset:31744
v_add_f32 v19, v19, v103
v_mfma_f32_16x16x32_bf16 v[188:191], a[92:95], v[60:63], v[188:191]
v_exp_f32 v105, v105
s_waitcnt lgkmcnt(4)
v_mfma_f32_16x16x32_bf16 v[192:195], a[96:99], v[56:59], v[192:195]
s_waitcnt vmcnt(14)
s_barrier
v_mfma_f32_16x16x32_bf16 v[196:199], a[96:99], v[60:63], v[196:199]
ds_read_b128 a[64:67], v8 offset:0
s_add_u32 m0, s42, 57344
v_mfma_f32_16x16x32_bf16 v[200:203], a[100:103], v[56:59], v[200:203]
v_add_f32 v16, v16, v104
v_mfma_f32_16x16x32_bf16 v[204:207], a[100:103], v[60:63], v[204:207]
ds_read_b128 a[68:71], v8 offset:1024
buffer_load_dwordx4 v13, s[24:27], s40 offen sc1 lds
v_mfma_f32_16x16x32_bf16 v[208:211], a[104:107], v[56:59], v[208:211]
v_exp_f32 v106, v106
v_mfma_f32_16x16x32_bf16 v[212:215], a[104:107], v[60:63], v[212:215]
ds_read_b128 a[72:75], v8 offset:2048
v_add_f32 v17, v17, v105
v_mfma_f32_16x16x32_bf16 v[216:219], a[108:111], v[56:59], v[216:219]
v_exp_f32 v107, v107
v_mfma_f32_16x16x32_bf16 v[220:223], a[108:111], v[60:63], v[220:223]
ds_read_b128 a[76:79], v8 offset:3072
v_add_f32 v16, v16, v106
s_waitcnt lgkmcnt(4)
v_mfma_f32_16x16x32_bf16 v[224:227], a[112:115], v[56:59], v[224:227]
v_exp_f32 v108, v108
v_mfma_f32_16x16x32_bf16 v[228:231], a[112:115], v[60:63], v[228:231]
ds_read_b128 a[80:83], v8 offset:4096
v_add_f32 v17, v17, v107
v_mfma_f32_16x16x32_bf16 v[232:235], a[116:119], v[56:59], v[232:235]
v_exp_f32 v109, v109
v_mfma_f32_16x16x32_bf16 v[236:239], a[116:119], v[60:63], v[236:239]
ds_read_b128 a[84:87], v8 offset:5120
buffer_load_dwordx4 v13, s[24:27], s40 offen offset:1024 sc1 lds
v_mfma_f32_16x16x32_bf16 v[240:243], a[120:123], v[56:59], v[240:243]
v_add_f32 v18, v18, v108
v_mfma_f32_16x16x32_bf16 v[244:247], a[120:123], v[60:63], v[244:247]
ds_read_b128 a[88:91], v8 offset:6144
s_add_u32 s40, s40, 32768
v_mfma_f32_16x16x32_bf16 v[248:251], a[124:127], v[56:59], v[248:251]
v_exp_f32 v110, v110
v_mfma_f32_16x16x32_bf16 v[252:255], a[124:127], v[60:63], v[252:255]
ds_read_b128 a[92:95], v8 offset:7168
v_add_f32 v19, v19, v109
v_exp_f32 v111, v111
v_add_f32 v18, v18, v110
s_nop 0
v_add_f32 v19, v19, v111
s_sub_u32 s45, s45, 1
s_cmp_lg_u32 s47, 0
s_cbranch_scc1 L_resc1
L_rescret1:
s_sub_u32 s44, s44, 1
s_cmp_eq_u32 s44, 0
s_cbranch_scc1 L_end1
s_waitcnt lgkmcnt(4)
v_mfma_f32_16x16x32_bf16 v[64:67], a[64:67], a[0:3], v[40:43]
ds_read_b128 a[96:99], v8 offset:8192
v_mfma_f32_16x16x32_bf16 v[68:71], a[64:67], a[32:35], v[44:47]
s_add_u32 m0, s43, 0
v_cvt_pk_bf16_f32 v48, v96, v97
v_mfma_f32_16x16x32_bf16 v[64:67], a[68:71], a[4:7], v[64:67]
ds_read_b128 a[100:103], v8 offset:9216
v_mfma_f32_16x16x32_bf16 v[68:71], a[68:71], a[36:39], v[68:71]
buffer_load_dwordx4 v10, s[28:31], s41 offen sc1 lds
v_cvt_pk_bf16_f32 v49, v98, v99
v_mfma_f32_16x16x32_bf16 v[64:67], a[72:75], a[8:11], v[64:67]
ds_read_b128 a[104:107], v8 offset:10240
v_mfma_f32_16x16x32_bf16 v[68:71], a[72:75], a[40:43], v[68:71]
v_exp_f32 v112, v112
v_mfma_f32_16x16x32_bf16 v[64:67], a[76:79], a[12:15], v[64:67]
ds_read_b128 a[108:111], v8 offset:11264
v_cvt_pk_bf16_f32 v50, v104, v105
v_mfma_f32_16x16x32_bf16 v[68:71], a[76:79], a[44:47], v[68:71]
v_exp_f32 v113, v113
s_waitcnt lgkmcnt(4)
v_mfma_f32_16x16x32_bf16 v[64:67], a[80:83], a[16:19], v[64:67]
ds_read_b128 a[112:115], v8 offset:12288
v_add_f32 v16, v16, v112
v_mfma_f32_16x16x32_bf16 v[68:71], a[80:83], a[48:51], v[68:71]
v_exp_f32 v114, v114
v_mfma_f32_16x16x32_bf16 v[64:67], a[84:87], a[20:23], v[64:67]
ds_read_b128 a[116:119], v8 offset:13312
v_add_f32 v17, v17, v113
v_mfma_f32_16x16x32_bf16 v[68:71], a[84:87], a[52:55], v[68:71]
buffer_load_dwordx4 v10, s[28:31], s41 offen offset:1024 sc1 lds
v_cvt_pk_bf16_f32 v51, v106, v107
v_mfma_f32_16x16x32_bf16 v[64:67], a[88:91], a[24:27], v[64:67]
ds_read_b128 a[120:123], v8 offset:14336
v_cvt_pk_bf16_f32 v52, v100, v101
v_mfma_f32_16x16x32_bf16 v[68:71], a[88:91], a[56:59], v[68:71]
v_exp_f32 v115, v115
v_mfma_f32_16x16x32_bf16 v[64:67], a[92:95], a[28:31], v[64:67]
ds_read_b128 a[124:127], v8 offset:15360
v_add_f32 v16, v16, v114
v_mfma_f32_16x16x32_bf16 v[68:71], a[92:95], a[60:63], v[68:71]
v_exp_f32 v116, v116
s_waitcnt lgkmcnt(4)
v_mfma_f32_16x16x32_bf16 v[72:75], a[96:99], a[0:3], v[40:43]
ds_read_b128 a[64:67], v8 offset:16384
v_add_f32 v17, v17, v115
v_mfma_f32_16x16x32_bf16 v[76:79], a[96:99], a[32:35], v[44:47]
s_add_u32 m0, s43, 8192
v_cvt_pk_bf16_f32 v53, v102, v103
v_mfma_f32_16x16x32_bf16 v[72:75], a[100:103], a[4:7], v[72:75]
ds_read_b128 a[68:71], v8 offset:17408
v_cvt_pk_bf16_f32 v54, v108, v109
v_mfma_f32_16x16x32_bf16 v[76:79], a[100:103], a[36:39], v[76:79]
buffer_load_dwordx4 v11, s[28:31], s41 offen sc1 lds
v_cvt_pk_bf16_f32 v55, v110, v111
v_mfma_f32_16x16x32_bf16 v[72:75], a[104:107], a[8:11], v[72:75]
ds_read_b128 a[72:75], v8 offset:18432
v_mfma_f32_16x16x32_bf16 v[76:79], a[104:107], a[40:43], v[76:79]
v_exp_f32 v117, v117
v_mfma_f32_16x16x32_bf16 v[72:75], a[108:111], a[12:15], v[72:75]
ds_read_b128 a[76:79], v8 offset:19456
v_add_f32 v18, v18, v116
v_mfma_f32_16x16x32_bf16 v[76:79], a[108:111], a[44:47], v[76:79]
v_exp_f32 v118, v118
s_waitcnt lgkmcnt(4)
v_mfma_f32_16x16x32_bf16 v[72:75], a[112:115], a[16:19], v[72:75]
ds_read_b128 a[80:83], v8 offset:20480
v_add_f32 v19, v19, v117
v_mfma_f32_16x16x32_bf16 v[76:79], a[112:115], a[48:51], v[76:79]
v_exp_f32 v119, v119
v_mfma_f32_16x16x32_bf16 v[72:75], a[116:119], a[20:23], v[72:75]
ds_read_b128 a[84:87], v8 offset:21504
v_add_f32 v18, v18, v118
v_mfma_f32_16x16x32_bf16 v[76:79], a[116:119], a[52:55], v[76:79]
buffer_load_dwordx4 v11, s[28:31], s41 offen offset:1024 sc1 lds
v_mfma_f32_16x16x32_bf16 v[72:75], a[120:123], a[24:27], v[72:75]
ds_read_b128 a[88:91], v8 offset:22528
v_mfma_f32_16x16x32_bf16 v[76:79], a[120:123], a[56:59], v[76:79]
v_exp_f32 v120, v120
v_mfma_f32_16x16x32_bf16 v[72:75], a[124:127], a[28:31], v[72:75]
ds_read_b128 a[92:95], v8 offset:23552
v_add_f32 v19, v19, v119
v_mfma_f32_16x16x32_bf16 v[76:79], a[124:127], a[60:63], v[76:79]
v_exp_f32 v121, v121
s_waitcnt lgkmcnt(4)
v_mfma_f32_16x16x32_bf16 v[80:83], a[64:67], a[0:3], v[40:43]
ds_read_b128 a[96:99], v8 offset:24576
v_add_f32 v16, v16, v120
v_mfma_f32_16x16x32_bf16 v[84:87], a[64:67], a[32:35], v[44:47]
s_add_u32 m0, s43, 16384
v_mfma_f32_16x16x32_bf16 v[80:83], a[68:71], a[4:7], v[80:83]
ds_read_b128 a[100:103], v8 offset:25600
v_mfma_f32_16x16x32_bf16 v[84:87], a[68:71], a[36:39], v[84:87]
buffer_load_dwordx4 v12, s[28:31], s41 offen sc1 lds
v_mfma_f32_16x16x32_bf16 v[80:83], a[72:75], a[8:11], v[80:83]
ds_read_b128 a[104:107], v8 offset:26624
v_mfma_f32_16x16x32_bf16 v[84:87], a[72:75], a[40:43], v[84:87]
v_exp_f32 v122, v122
v_mfma_f32_16x16x32_bf16 v[80:83], a[76:79], a[12:15], v[80:83]
ds_read_b128 a[108:111], v8 offset:27648
v_add_f32 v17, v17, v121
v_mfma_f32_16x16x32_bf16 v[84:87], a[76:79], a[44:47], v[84:87]
v_exp_f32 v123, v123
s_waitcnt lgkmcnt(4)
v_mfma_f32_16x16x32_bf16 v[80:83], a[80:83], a[16:19], v[80:83]
ds_read_b128 a[112:115], v8 offset:28672
v_add_f32 v16, v16, v122
v_mfma_f32_16x16x32_bf16 v[84:87], a[80:83], a[48:51], v[84:87]
v_exp_f32 v124, v124
v_mfma_f32_16x16x32_bf16 v[80:83], a[84:87], a[20:23], v[80:83]
ds_read_b128 a[116:119], v8 offset:29696
v_add_f32 v17, v17, v123
v_mfma_f32_16x16x32_bf16 v[84:87], a[84:87], a[52:55], v[84:87]
buffer_load_dwordx4 v12, s[28:31], s41 offen offset:1024 sc1 lds
v_mfma_f32_16x16x32_bf16 v[80:83], a[88:91], a[24:27], v[80:83]
ds_read_b128 a[120:123], v8 offset:30720
v_mfma_f32_16x16x32_bf16 v[84:87], a[88:91], a[56:59], v[84:87]
v_exp_f32 v125, v125
v_mfma_f32_16x16x32_bf16 v[80:83], a[92:95], a[28:31], v[80:83]
ds_read_b128 a[124:127], v8 offset:31744
v_add_f32 v18, v18, v124
v_mfma_f32_16x16x32_bf16 v[84:87], a[92:95], a[60:63], v[84:87]
v_exp_f32 v126, v126
s_waitcnt lgkmcnt(4)
v_mfma_f32_16x16x32_bf16 v[88:91], a[96:99], a[0:3], v[40:43]
s_waitcnt vmcnt(14)
s_barrier
v_mfma_f32_16x16x32_bf16 v[92:95], a[96:99], a[32:35], v[44:47]
ds_read_b128 a[64:67], v9 offset:32768
s_add_u32 m0, s43, 24576
v_mfma_f32_16x16x32_bf16 v[88:91], a[100:103], a[4:7], v[88:91]
v_add_f32 v19, v19, v125
v_mfma_f32_16x16x32_bf16 v[92:95], a[100:103], a[36:39], v[92:95]
ds_read_b128 a[68:71], v9 offset:33792
buffer_load_dwordx4 v13, s[28:31], s41 offen sc1 lds
v_mfma_f32_16x16x32_bf16 v[88:91], a[104:107], a[8:11], v[88:91]
v_exp_f32 v127, v127
v_mfma_f32_16x16x32_bf16 v[92:95], a[104:107], a[40:43], v[92:95]
ds_read_b128 a[72:75], v9 offset:34816
v_add_f32 v18, v18, v126
v_mfma_f32_16x16x32_bf16 v[88:91], a[108:111], a[12:15], v[88:91]
s_nop 0
v_add_f32 v19, v19, v127
v_mfma_f32_16x16x32_bf16 v[92:95], a[108:111], a[44:47], v[92:95]
ds_read_b128 a[76:79], v9 offset:35840
v_cvt_pk_bf16_f32 v56, v112, v113
s_waitcnt lgkmcnt(4)
v_mfma_f32_16x16x32_bf16 v[88:91], a[112:115], a[16:19], v[88:91]
v_cvt_pk_bf16_f32 v57, v114, v115
v_cvt_pk_bf16_f32 v58, v120, v121
v_mfma_f32_16x16x32_bf16 v[92:95], a[112:115], a[48:51], v[92:95]
ds_read_b128 a[80:83], v9 offset:36864
v_cvt_pk_bf16_f32 v59, v122, v123
v_mfma_f32_16x16x32_bf16 v[88:91], a[116:119], a[20:23], v[88:91]
v_cvt_pk_bf16_f32 v60, v116, v117
v_cvt_pk_bf16_f32 v61, v118, v119
v_mfma_f32_16x16x32_bf16 v[92:95], a[116:119], a[52:55], v[92:95]
ds_read_b128 a[84:87], v9 offset:37888
buffer_load_dwordx4 v13, s[28:31], s41 offen offset:1024 sc1 lds
v_mfma_f32_16x16x32_bf16 v[88:91], a[120:123], a[24:27], v[88:91]
v_cvt_pk_bf16_f32 v62, v124, v125
v_cvt_pk_bf16_f32 v63, v126, v127
v_mfma_f32_16x16x32_bf16 v[92:95], a[120:123], a[56:59], v[92:95]
ds_read_b128 a[88:91], v9 offset:38912
s_add_u32 s41, s41, 32768
v_mfma_f32_16x16x32_bf16 v[88:91], a[124:127], a[28:31], v[88:91]
v_mfma_f32_16x16x32_bf16 v[92:95], a[124:127], a[60:63], v[92:95]
ds_read_b128 a[92:95], v9 offset:39936
s_waitcnt lgkmcnt(4)
v_mfma_f32_16x16x32_bf16 v[128:131], a[64:67], v[48:51], v[128:131]
ds_read_b128 a[96:99], v9 offset:40960
v_mfma_f32_16x16x32_bf16 v[132:135], a[64:67], v[52:55], v[132:135]
s_add_u32 m0, s42, 0
s_cmp_le_i32 s45, 0
v_mfma_f32_16x16x32_bf16 v[136:139], a[68:71], v[48:51], v[136:139]
ds_read_b128 a[100:103], v9 offset:41984
s_cbranch_scc1 L_mask0
L_maskret0:
v_mfma_f32_16x16x32_bf16 v[140:143], a[68:71], v[52:55], v[140:143]
buffer_load_dwordx4 v10, s[24:27], s40 offen sc1 lds
v_max3_f32 v20, v64, v65, v66
v_mfma_f32_16x16x32_bf16 v[144:147], a[72:75], v[48:51], v[144:147]
ds_read_b128 a[104:107], v9 offset:43008
v_max3_f32 v21, v67, v68, v69
v_mfma_f32_16x16x32_bf16 v[148:151], a[72:75], v[52:55], v[148:151]
v_max3_f32 v20, v20, v70, v71
v_max3_f32 v21, v21, v72, v73
v_mfma_f32_16x16x32_bf16 v[152:155], a[76:79], v[48:51], v[152:155]
ds_read_b128 a[108:111], v9 offset:44032
v_max3_f32 v20, v20, v74, v75
v_mfma_f32_16x16x32_bf16 v[156:159], a[76:79], v[52:55], v[156:159]
v_max3_f32 v21, v21, v76, v77
v_max3_f32 v20, v20, v78, v79
s_waitcnt lgkmcnt(4)
v_mfma_f32_16x16x32_bf16 v[160:163], a[80:83], v[48:51], v[160:163]
ds_read_b128 a[112:115], v9 offset:45056
v_max3_f32 v21, v21, v80, v81
v_mfma_f32_16x16x32_bf16 v[164:167], a[80:83], v[52:55], v[164:167]
v_max3_f32 v20, v20, v82, v83
v_max3_f32 v21, v21, v84, v85
v_mfma_f32_16x16x32_bf16 v[168:171], a[84:87], v[48:51], v[168:171]
ds_read_b128 a[116:119], v9 offset:46080
v_max3_f32 v20, v20, v86, v87
v_mfma_f32_16x16x32_bf16 v[172:175], a[84:87], v[52:55], v[172:175]
buffer_load_dwordx4 v10, s[24:27], s40 offen offset:1024 sc1 lds
v_max3_f32 v21, v21, v88, v89
v_mfma_f32_16x16x32_bf16 v[176:179], a[88:91], v[48:51], v[176:179]
ds_read_b128 a[120:123], v9 offset:47104
v_max3_f32 v20, v20, v90, v91
v_mfma_f32_16x16x32_bf16 v[180:183], a[88:91], v[52:55], v[180:183]
v_max3_f32 v21, v21, v92, v93
v_max3_f32 v20, v20, v94, v95
v_mfma_f32_16x16x32_bf16 v[184:187], a[92:95], v[48:51], v[184:187]
ds_read_b128 a[124:127], v9 offset:48128
v_max_f32 v20, v20, v21
v_mfma_f32_16x16x32_bf16 v[188:191], a[92:95], v[52:55], v[188:191]
v_cmp_lt_f32 vcc, s46, v20
s_cbranch_vccnz L_rare0
L_rareret0:
s_waitcnt lgkmcnt(4)
v_mfma_f32_16x16x32_bf16 v[192:195], a[96:99], v[48:51], v[192:195]
ds_read_b128 a[64:67], v9 offset:49152
v_mfma_f32_16x16x32_bf16 v[196:199], a[96:99], v[52:55], v[196:199]
s_add_u32 m0, s42, 8192
v_mfma_f32_16x16x32_bf16 v[200:203], a[100:103], v[48:51], v[200:203]
ds_read_b128 a[68:71], v9 offset:50176
v_mfma_f32_16x16x32_bf16 v[204:207], a[100:103], v[52:55], v[204:207]
buffer_load_dwordx4 v11, s[24:27], s40 offen sc1 lds
v_mfma_f32_16x16x32_bf16 v[208:211], a[104:107], v[48:51], v[208:211]
ds_read_b128 a[72:75], v9 offset:51200
v_mfma_f32_16x16x32_bf16 v[212:215], a[104:107], v[52:55], v[212:215]
v_exp_f32 v64, v64
v_mfma_f32_16x16x32_bf16 v[216:219], a[108:111], v[48:51], v[216:219]
ds_read_b128 a[76:79], v9 offset:52224
v_mfma_f32_16x16x32_bf16 v[220:223], a[108:111], v[52:55], v[220:223]
v_exp_f32 v65, v65
s_waitcnt lgkmcnt(4)
v_mfma_f32_16x16x32_bf16 v[224:227], a[112:115], v[48:51], v[224:227]
ds_read_b128 a[80:83], v9 offset:53248
v_add_f32 v16, v16, v64
v_mfma_f32_16x16x32_bf16 v[228:231], a[112:115], v[52:55], v[228:231]
v_exp_f32 v66, v66
v_mfma_f32_16x16x32_bf16 v[232:235], a[116:119], v[48:51], v[232:235]
ds_read_b128 a[84:87], v9 offset:54272
v_add_f32 v17, v17, v65
v_mfma_f32_16x16x32_bf16 v[236:239], a[116:119], v[52:55], v[236:239]
buffer_load_dwordx4 v11, s[24:27], s40 offen offset:1024 sc1 lds
v_mfma_f32_16x16x32_bf16 v[240:243], a[120:123], v[48:51], v[240:243]
ds_read_b128 a[88:91], v9 offset:55296
v_mfma_f32_16x16x32_bf16 v[244:247], a[120:123], v[52:55], v[244:247]
v_exp_f32 v67, v67
v_mfma_f32_16x16x32_bf16 v[248:251], a[124:127], v[48:51], v[248:251]
ds_read_b128 a[92:95], v9 offset:56320
v_add_f32 v16, v16, v66
v_mfma_f32_16x16x32_bf16 v[252:255], a[124:127], v[52:55], v[252:255]
v_exp_f32 v68, v68
s_waitcnt lgkmcnt(4)
v_mfma_f32_16x16x32_bf16 v[128:131], a[64:67], v[56:59], v[128:131]
ds_read_b128 a[96:99], v9 offset:57344
v_add_f32 v17, v17, v67
v_mfma_f32_16x16x32_bf16 v[132:135], a[64:67], v[60:63], v[132:135]
s_add_u32 m0, s42, 16384
v_mfma_f32_16x16x32_bf16 v[136:139], a[68:71], v[56:59], v[136:139]
ds_read_b128 a[100:103], v9 offset:58368
v_mfma_f32_16x16x32_bf16 v[140:143], a[68:71], v[60:63], v[140:143]
buffer_load_dwordx4 v12, s[24:27], s40 offen sc1 lds
v_mfma_f32_16x16x32_bf16 v[144:147], a[72:75], v[56:59], v[144:147]
ds_read_b128 a[104:107], v9 offset:59392
v_mfma_f32_16x16x32_bf16 v[148:151], a[72:75], v[60:63], v[148:151]
v_exp_f32 v69, v69
v_mfma_f32_16x16x32_bf16 v[152:155], a[76:79], v[56:59], v[152:155]
ds_read_b128 a[108:111], v9 offset:60416
v_add_f32 v18, v18, v68
v_mfma_f32_16x16x32_bf16 v[156:159], a[76:79], v[60:63], v[156:159]
v_exp_f32 v70, v70
s_waitcnt lgkmcnt(4)
v_mfma_f32_16x16x32_bf16 v[160:163], a[80:83], v[56:59], v[160:163]
ds_read_b128 a[112:115], v9 offset:61440
v_add_f32 v19, v19, v69
v_mfma_f32_16x16x32_bf16 v[164:167], a[80:83], v[60:63], v[164:167]
v_exp_f32 v71, v71
v_mfma_f32_16x16x32_bf16 v[168:171], a[84:87], v[56:59], v[168:171]
ds_read_b128 a[116:119], v9 offset:62464
v_add_f32 v18, v18, v70
v_mfma_f32_16x16x32_bf16 v[172:175], a[84:87], v[60:63], v[172:175]
buffer_load_dwordx4 v12, s[24:27], s40 offen offset:1024 sc1 lds
v_mfma_f32_16x16x32_bf16 v[176:179], a[88:91], v[56:59], v[176:179]
ds_read_b128 a[120:123], v9 offset:63488
v_mfma_f32_16x16x32_bf16 v[180:183], a[88:91], v[60:63], v[180:183]
v_exp_f32 v72, v72
v_mfma_f32_16x16x32_bf16 v[184:187], a[92:95], v[56:59], v[184:187]
ds_read_b128 a[124:127], v9 offset:64512
v_add_f32 v19, v19, v71
v_mfma_f32_16x16x32_bf16 v[188:191], a[92:95], v[60:63], v[188:191]
v_exp_f32 v73, v73
s_waitcnt lgkmcnt(4)
v_mfma_f32_16x16x32_bf16 v[192:195], a[96:99], v[56:59], v[192:195]
s_waitcnt vmcnt(14)
s_barrier
v_mfma_f32_16x16x32_bf16 v[196:199], a[96:99], v[60:63], v[196:199]
ds_read_b128 a[64:67], v8 offset:32768
s_add_u32 m0, s42, 24576
v_mfma_f32_16x16x32_bf16 v[200:203], a[100:103], v[56:59], v[200:203]
v_add_f32 v16, v16, v72
v_mfma_f32_16x16x32_bf16 v[204:207], a[100:103], v[60:63], v[204:207]
ds_read_b128 a[68:71], v8 offset:33792
buffer_load_dwordx4 v13, s[24:27], s40 offen sc1 lds
v_mfma_f32_16x16x32_bf16 v[208:211], a[104:107], v[56:59], v[208:211]
v_exp_f32 v74, v74
v_mfma_f32_16x16x32_bf16 v[212:215], a[104:107], v[60:63], v[212:215]
ds_read_b128 a[72:75], v8 offset:34816
v_add_f32 v17, v17, v73
v_mfma_f32_16x16x32_bf16 v[216:219], a[108:111], v[56:59], v[216:219]
v_exp_f32 v75, v75
v_mfma_f32_16x16x32_bf16 v[220:223], a[108:111], v[60:63], v[220:223]
ds_read_b128 a[76:79], v8 offset:35840
v_add_f32 v16, v16, v74
s_waitcnt lgkmcnt(4)
v_mfma_f32_16x16x32_bf16 v[224:227], a[112:115], v[56:59], v[224:227]
v_exp_f32 v76, v76
v_mfma_f32_16x16x32_bf16 v[228:231], a[112:115], v[60:63], v[228:231]
ds_read_b128 a[80:83], v8 offset:36864
v_add_f32 v17, v17, v75
v_mfma_f32_16x16x32_bf16 v[232:235], a[116:119], v[56:59], v[232:235]
v_exp_f32 v77, v77
v_mfma_f32_16x16x32_bf16 v[236:239], a[116:119], v[60:63], v[236:239]
ds_read_b128 a[84:87], v8 offset:37888
buffer_load_dwordx4 v13, s[24:27], s40 offen offset:1024 sc1 lds
v_mfma_f32_16x16x32_bf16 v[240:243], a[120:123], v[56:59], v[240:243]
v_add_f32 v18, v18, v76
v_mfma_f32_16x16x32_bf16 v[244:247], a[120:123], v[60:63], v[244:247]
ds_read_b128 a[88:91], v8 offset:38912
s_add_u32 s40, s40, 32768
v_mfma_f32_16x16x32_bf16 v[248:251], a[124:127], v[56:59], v[248:251]
v_exp_f32 v78, v78
v_mfma_f32_16x16x32_bf16 v[252:255], a[124:127], v[60:63], v[252:255]
ds_read_b128 a[92:95], v8 offset:39936
v_add_f32 v19, v19, v77
v_exp_f32 v79, v79
v_add_f32 v18, v18, v78
s_nop 0
v_add_f32 v19, v19, v79
s_sub_u32 s45, s45, 1
s_cmp_lg_u32 s47, 0
s_cbranch_scc1 L_resc0
L_rescret0:
s_sub_u32 s44, s44, 1
s_cmp_lg_u32 s44, 0
s_cbranch_scc1 L_loop
L_end0:
s_waitcnt vmcnt(0)
s_barrier
ds_read_b128 a[64:67], v9 offset:0
ds_read_b128 a[68:71], v9 offset:1024
ds_read_b128 a[72:75], v9 offset:2048
ds_read_b128 a[76:79], v9 offset:3072
ds_read_b128 a[80:83], v9 offset:4096
ds_read_b128 a[84:87], v9 offset:5120
ds_read_b128 a[88:91], v9 offset:6144
ds_read_b128 a[92:95], v9 offset:7168
buffer_load_dword v36, off, s[36:39], s77 sc1
buffer_load_dwordx4 v[32:35], v14, s[36:39], s76 offen sc1
v_cvt_pk_bf16_f32 v48, v64, v65
v_cvt_pk_bf16_f32 v49, v66, v67
v_cvt_pk_bf16_f32 v50, v72, v73
v_cvt_pk_bf16_f32 v51, v74, v75
v_cvt_pk_bf16_f32 v52, v68, v69
v_cvt_pk_bf16_f32 v53, v70, v71
v_cvt_pk_bf16_f32 v54, v76, v77
v_cvt_pk_bf16_f32 v55, v78, v79
v_exp_f32 v80, v80
v_exp_f32 v81, v81
v_add_f32 v16, v16, v80
v_exp_f32 v82, v82
v_add_f32 v17, v17, v81
v_exp_f32 v83, v83
v_add_f32 v16, v16, v82
v_exp_f32 v84, v84
v_add_f32 v17, v17, v83
v_exp_f32 v85, v85
v_add_f32 v18, v18, v84
v_exp_f32 v86, v86
v_add_f32 v19, v19, v85
v_exp_f32 v87, v87
v_add_f32 v18, v18, v86
v_exp_f32 v88, v88
v_add_f32 v19, v19, v87
v_exp_f32 v89, v89
v_add_f32 v16, v16, v88
v_exp_f32 v90, v90
v_add_f32 v17, v17, v89
v_exp_f32 v91, v91
v_add_f32 v16, v16, v90
v_exp_f32 v92, v92
v_add_f32 v17, v17, v91
v_exp_f32 v93, v93
v_add_f32 v18, v18, v92
v_exp_f32 v94, v94
v_add_f32 v19, v19, v93
v_exp_f32 v95, v95
v_add_f32 v18, v18, v94
s_nop 0
v_add_f32 v19, v19, v95
v_cvt_pk_bf16_f32 v56, v80, v81
v_cvt_pk_bf16_f32 v57, v82, v83
v_cvt_pk_bf16_f32 v58, v88, v89
v_cvt_pk_bf16_f32 v59, v90, v91
v_cvt_pk_bf16_f32 v60, v84, v85
v_cvt_pk_bf16_f32 v61, v86, v87
v_cvt_pk_bf16_f32 v62, v92, v93
v_cvt_pk_bf16_f32 v63, v94, v95
s_nop 1
s_waitcnt lgkmcnt(4)
v_mfma_f32_16x16x32_bf16 v[128:131], a[64:67], v[48:51], v[128:131]
ds_read_b128 a[96:99], v9 offset:8192
v_mfma_f32_16x16x32_bf16 v[132:135], a[64:67], v[52:55], v[132:135]
v_mfma_f32_16x16x32_bf16 v[136:139], a[68:71], v[48:51], v[136:139]
ds_read_b128 a[100:103], v9 offset:9216
v_mfma_f32_16x16x32_bf16 v[140:143], a[68:71], v[52:55], v[140:143]
v_mfma_f32_16x16x32_bf16 v[144:147], a[72:75], v[48:51], v[144:147]
ds_read_b128 a[104:107], v9 offset:10240
v_mfma_f32_16x16x32_bf16 v[148:151], a[72:75], v[52:55], v[148:151]
v_mfma_f32_16x16x32_bf16 v[152:155], a[76:79], v[48:51], v[152:155]
ds_read_b128 a[108:111], v9 offset:11264
v_mfma_f32_16x16x32_bf16 v[156:159], a[76:79], v[52:55], v[156:159]
s_waitcnt lgkmcnt(4)
v_mfma_f32_16x16x32_bf16 v[160:163], a[80:83], v[48:51], v[160:163]
ds_read_b128 a[112:115], v9 offset:12288
v_mfma_f32_16x16x32_bf16 v[164:167], a[80:83], v[52:55], v[164:167]
v_mfma_f32_16x16x32_bf16 v[168:171], a[84:87], v[48:51], v[168:171]
ds_read_b128 a[116:119], v9 offset:13312
v_mfma_f32_16x16x32_bf16 v[172:175], a[84:87], v[52:55], v[172:175]
v_mfma_f32_16x16x32_bf16 v[176:179], a[88:91], v[48:51], v[176:179]
ds_read_b128 a[120:123], v9 offset:14336
v_mfma_f32_16x16x32_bf16 v[180:183], a[88:91], v[52:55], v[180:183]
v_mfma_f32_16x16x32_bf16 v[184:187], a[92:95], v[48:51], v[184:187]
ds_read_b128 a[124:127], v9 offset:15360
v_mfma_f32_16x16x32_bf16 v[188:191], a[92:95], v[52:55], v[188:191]
s_waitcnt lgkmcnt(4)
v_mfma_f32_16x16x32_bf16 v[192:195], a[96:99], v[48:51], v[192:195]
ds_read_b128 a[64:67], v9 offset:16384
v_mfma_f32_16x16x32_bf16 v[196:199], a[96:99], v[52:55], v[196:199]
v_mfma_f32_16x16x32_bf16 v[200:203], a[100:103], v[48:51], v[200:203]
ds_read_b128 a[68:71], v9 offset:17408
v_mfma_f32_16x16x32_bf16 v[204:207], a[100:103], v[52:55], v[204:207]
v_mfma_f32_16x16x32_bf16 v[208:211], a[104:107], v[48:51], v[208:211]
ds_read_b128 a[72:75], v9 offset:18432
v_mfma_f32_16x16x32_bf16 v[212:215], a[104:107], v[52:55], v[212:215]
v_mfma_f32_16x16x32_bf16 v[216:219], a[108:111], v[48:51], v[216:219]
ds_read_b128 a[76:79], v9 offset:19456
v_mfma_f32_16x16x32_bf16 v[220:223], a[108:111], v[52:55], v[220:223]
s_waitcnt lgkmcnt(4)
v_mfma_f32_16x16x32_bf16 v[224:227], a[112:115], v[48:51], v[224:227]
ds_read_b128 a[80:83], v9 offset:20480
v_mfma_f32_16x16x32_bf16 v[228:231], a[112:115], v[52:55], v[228:231]
v_mfma_f32_16x16x32_bf16 v[232:235], a[116:119], v[48:51], v[232:235]
ds_read_b128 a[84:87], v9 offset:21504
v_mfma_f32_16x16x32_bf16 v[236:239], a[116:119], v[52:55], v[236:239]
v_mfma_f32_16x16x32_bf16 v[240:243], a[120:123], v[48:51], v[240:243]
ds_read_b128 a[88:91], v9 offset:22528
v_mfma_f32_16x16x32_bf16 v[244:247], a[120:123], v[52:55], v[244:247]
v_mfma_f32_16x16x32_bf16 v[248:251], a[124:127], v[48:51], v[248:251]
ds_read_b128 a[92:95], v9 offset:23552
v_mfma_f32_16x16x32_bf16 v[252:255], a[124:127], v[52:55], v[252:255]
s_waitcnt lgkmcnt(4)
v_mfma_f32_16x16x32_bf16 v[128:131], a[64:67], v[56:59], v[128:131]
ds_read_b128 a[96:99], v9 offset:24576
v_mfma_f32_16x16x32_bf16 v[132:135], a[64:67], v[60:63], v[132:135]
v_mfma_f32_16x16x32_bf16 v[136:139], a[68:71], v[56:59], v[136:139]
ds_read_b128 a[100:103], v9 offset:25600
v_mfma_f32_16x16x32_bf16 v[140:143], a[68:71], v[60:63], v[140:143]
v_mfma_f32_16x16x32_bf16 v[144:147], a[72:75], v[56:59], v[144:147]
ds_read_b128 a[104:107], v9 offset:26624
v_mfma_f32_16x16x32_bf16 v[148:151], a[72:75], v[60:63], v[148:151]
v_mfma_f32_16x16x32_bf16 v[152:155], a[76:79], v[56:59], v[152:155]
ds_read_b128 a[108:111], v9 offset:27648
v_mfma_f32_16x16x32_bf16 v[156:159], a[76:79], v[60:63], v[156:159]
s_waitcnt lgkmcnt(4)
v_mfma_f32_16x16x32_bf16 v[160:163], a[80:83], v[56:59], v[160:163]
ds_read_b128 a[112:115], v9 offset:28672
v_mfma_f32_16x16x32_bf16 v[164:167], a[80:83], v[60:63], v[164:167]
v_mfma_f32_16x16x32_bf16 v[168:171], a[84:87], v[56:59], v[168:171]
ds_read_b128 a[116:119], v9 offset:29696
v_mfma_f32_16x16x32_bf16 v[172:175], a[84:87], v[60:63], v[172:175]
v_mfma_f32_16x16x32_bf16 v[176:179], a[88:91], v[56:59], v[176:179]
ds_read_b128 a[120:123], v9 offset:30720
v_mfma_f32_16x16x32_bf16 v[180:183], a[88:91], v[60:63], v[180:183]
v_mfma_f32_16x16x32_bf16 v[184:187], a[92:95], v[56:59], v[184:187]
ds_read_b128 a[124:127], v9 offset:31744
v_mfma_f32_16x16x32_bf16 v[188:191], a[92:95], v[60:63], v[188:191]
s_waitcnt lgkmcnt(4)
v_mfma_f32_16x16x32_bf16 v[192:195], a[96:99], v[56:59], v[192:195]
v_mfma_f32_16x16x32_bf16 v[196:199], a[96:99], v[60:63], v[196:199]
v_mfma_f32_16x16x32_bf16 v[200:203], a[100:103], v[56:59], v[200:203]
v_mfma_f32_16x16x32_bf16 v[204:207], a[100:103], v[60:63], v[204:207]
v_mfma_f32_16x16x32_bf16 v[208:211], a[104:107], v[56:59], v[208:211]
v_mfma_f32_16x16x32_bf16 v[212:215], a[104:107], v[60:63], v[212:215]
v_mfma_f32_16x16x32_bf16 v[216:219], a[108:111], v[56:59], v[216:219]
v_mfma_f32_16x16x32_bf16 v[220:223], a[108:111], v[60:63], v[220:223]
s_waitcnt lgkmcnt(0)
v_mfma_f32_16x16x32_bf16 v[224:227], a[112:115], v[56:59], v[224:227]
v_mfma_f32_16x16x32_bf16 v[228:231], a[112:115], v[60:63], v[228:231]
v_mfma_f32_16x16x32_bf16 v[232:235], a[116:119], v[56:59], v[232:235]
v_mfma_f32_16x16x32_bf16 v[236:239], a[116:119], v[60:63], v[236:239]
v_mfma_f32_16x16x32_bf16 v[240:243], a[120:123], v[56:59], v[240:243]
v_mfma_f32_16x16x32_bf16 v[244:247], a[120:123], v[60:63], v[244:247]
v_mfma_f32_16x16x32_bf16 v[248:251], a[124:127], v[56:59], v[248:251]
v_mfma_f32_16x16x32_bf16 v[252:255], a[124:127], v[60:63], v[252:255]
s_branch L_epi
L_end1:
s_waitcnt vmcnt(0)
s_barrier
ds_read_b128 a[64:67], v9 offset:32768
ds_read_b128 a[68:71], v9 offset:33792
ds_read_b128 a[72:75], v9 offset:34816
ds_read_b128 a[76:79], v9 offset:35840
ds_read_b128 a[80:83], v9 offset:36864
ds_read_b128 a[84:87], v9 offset:37888
ds_read_b128 a[88:91], v9 offset:38912
ds_read_b128 a[92:95], v9 offset:39936
buffer_load_dword v36, off, s[36:39], s77 sc1
buffer_load_dwordx4 v[32:35], v14, s[36:39], s76 offen sc1
v_cvt_pk_bf16_f32 v48, v96, v97
v_cvt_pk_bf16_f32 v49, v98, v99
v_cvt_pk_bf16_f32 v50, v104, v105
v_cvt_pk_bf16_f32 v51, v106, v107
v_cvt_pk_bf16_f32 v52, v100, v101
v_cvt_pk_bf16_f32 v53, v102, v103
v_cvt_pk_bf16_f32 v54, v108, v109
v_cvt_pk_bf16_f32 v55, v110, v111
v_exp_f32 v112, v112
v_exp_f32 v113, v113
v_add_f32 v16, v16, v112
v_exp_f32 v114, v114
v_add_f32 v17, v17, v113
v_exp_f32 v115, v115
v_add_f32 v16, v16, v114
v_exp_f32 v116, v116
v_add_f32 v17, v17, v115
v_exp_f32 v117, v117
v_add_f32 v18, v18, v116
v_exp_f32 v118, v118
v_add_f32 v19, v19, v117
v_exp_f32 v119, v119
v_add_f32 v18, v18, v118
v_exp_f32 v120, v120
v_add_f32 v19, v19, v119
v_exp_f32 v121, v121
v_add_f32 v16, v16, v120
v_exp_f32 v122, v122
v_add_f32 v17, v17, v121
v_exp_f32 v123, v123
v_add_f32 v16, v16, v122
v_exp_f32 v124, v124
v_add_f32 v17, v17, v123
v_exp_f32 v125, v125
v_add_f32 v18, v18, v124
v_exp_f32 v126, v126
v_add_f32 v19, v19, v125
v_exp_f32 v127, v127
v_add_f32 v18, v18, v126
s_nop 0
v_add_f32 v19, v19, v127
v_cvt_pk_bf16_f32 v56, v112, v113
v_cvt_pk_bf16_f32 v57, v114, v115
v_cvt_pk_bf16_f32 v58, v120, v121
v_cvt_pk_bf16_f32 v59, v122, v123
v_cvt_pk_bf16_f32 v60, v116, v117
v_cvt_pk_bf16_f32 v61, v118, v119
v_cvt_pk_bf16_f32 v62, v124, v125
v_cvt_pk_bf16_f32 v63, v126, v127
s_nop 1
s_waitcnt lgkmcnt(4)
v_mfma_f32_16x16x32_bf16 v[128:131], a[64:67], v[48:51], v[128:131]
ds_read_b128 a[96:99], v9 offset:40960
v_mfma_f32_16x16x32_bf16 v[132:135], a[64:67], v[52:55], v[132:135]
v_mfma_f32_16x16x32_bf16 v[136:139], a[68:71], v[48:51], v[136:139]
ds_read_b128 a[100:103], v9 offset:41984
v_mfma_f32_16x16x32_bf16 v[140:143], a[68:71], v[52:55], v[140:143]
v_mfma_f32_16x16x32_bf16 v[144:147], a[72:75], v[48:51], v[144:147]
ds_read_b128 a[104:107], v9 offset:43008
v_mfma_f32_16x16x32_bf16 v[148:151], a[72:75], v[52:55], v[148:151]
v_mfma_f32_16x16x32_bf16 v[152:155], a[76:79], v[48:51], v[152:155]
ds_read_b128 a[108:111], v9 offset:44032
v_mfma_f32_16x16x32_bf16 v[156:159], a[76:79], v[52:55], v[156:159]
s_waitcnt lgkmcnt(4)
v_mfma_f32_16x16x32_bf16 v[160:163], a[80:83], v[48:51], v[160:163]
ds_read_b128 a[112:115], v9 offset:45056
v_mfma_f32_16x16x32_bf16 v[164:167], a[80:83], v[52:55], v[164:167]
v_mfma_f32_16x16x32_bf16 v[168:171], a[84:87], v[48:51], v[168:171]
ds_read_b128 a[116:119], v9 offset:46080
v_mfma_f32_16x16x32_bf16 v[172:175], a[84:87], v[52:55], v[172:175]
v_mfma_f32_16x16x32_bf16 v[176:179], a[88:91], v[48:51], v[176:179]
ds_read_b128 a[120:123], v9 offset:47104
v_mfma_f32_16x16x32_bf16 v[180:183], a[88:91], v[52:55], v[180:183]
v_mfma_f32_16x16x32_bf16 v[184:187], a[92:95], v[48:51], v[184:187]
ds_read_b128 a[124:127], v9 offset:48128
v_mfma_f32_16x16x32_bf16 v[188:191], a[92:95], v[52:55], v[188:191]
s_waitcnt lgkmcnt(4)
v_mfma_f32_16x16x32_bf16 v[192:195], a[96:99], v[48:51], v[192:195]
ds_read_b128 a[64:67], v9 offset:49152
v_mfma_f32_16x16x32_bf16 v[196:199], a[96:99], v[52:55], v[196:199]
v_mfma_f32_16x16x32_bf16 v[200:203], a[100:103], v[48:51], v[200:203]
ds_read_b128 a[68:71], v9 offset:50176
v_mfma_f32_16x16x32_bf16 v[204:207], a[100:103], v[52:55], v[204:207]
v_mfma_f32_16x16x32_bf16 v[208:211], a[104:107], v[48:51], v[208:211]
ds_read_b128 a[72:75], v9 offset:51200
v_mfma_f32_16x16x32_bf16 v[212:215], a[104:107], v[52:55], v[212:215]
v_mfma_f32_16x16x32_bf16 v[216:219], a[108:111], v[48:51], v[216:219]
ds_read_b128 a[76:79], v9 offset:52224
v_mfma_f32_16x16x32_bf16 v[220:223], a[108:111], v[52:55], v[220:223]
s_waitcnt lgkmcnt(4)
v_mfma_f32_16x16x32_bf16 v[224:227], a[112:115], v[48:51], v[224:227]
ds_read_b128 a[80:83], v9 offset:53248
v_mfma_f32_16x16x32_bf16 v[228:231], a[112:115], v[52:55], v[228:231]
v_mfma_f32_16x16x32_bf16 v[232:235], a[116:119], v[48:51], v[232:235]
ds_read_b128 a[84:87], v9 offset:54272
v_mfma_f32_16x16x32_bf16 v[236:239], a[116:119], v[52:55], v[236:239]
v_mfma_f32_16x16x32_bf16 v[240:243], a[120:123], v[48:51], v[240:243]
ds_read_b128 a[88:91], v9 offset:55296
v_mfma_f32_16x16x32_bf16 v[244:247], a[120:123], v[52:55], v[244:247]
v_mfma_f32_16x16x32_bf16 v[248:251], a[124:127], v[48:51], v[248:251]
ds_read_b128 a[92:95], v9 offset:56320
v_mfma_f32_16x16x32_bf16 v[252:255], a[124:127], v[52:55], v[252:255]
s_waitcnt lgkmcnt(4)
v_mfma_f32_16x16x32_bf16 v[128:131], a[64:67], v[56:59], v[128:131]
ds_read_b128 a[96:99], v9 offset:57344
v_mfma_f32_16x16x32_bf16 v[132:135], a[64:67], v[60:63], v[132:135]
v_mfma_f32_16x16x32_bf16 v[136:139], a[68:71], v[56:59], v[136:139]
ds_read_b128 a[100:103], v9 offset:58368
v_mfma_f32_16x16x32_bf16 v[140:143], a[68:71], v[60:63], v[140:143]
v_mfma_f32_16x16x32_bf16 v[144:147], a[72:75], v[56:59], v[144:147]
ds_read_b128 a[104:107], v9 offset:59392
v_mfma_f32_16x16x32_bf16 v[148:151], a[72:75], v[60:63], v[148:151]
v_mfma_f32_16x16x32_bf16 v[152:155], a[76:79], v[56:59], v[152:155]
ds_read_b128 a[108:111], v9 offset:60416
v_mfma_f32_16x16x32_bf16 v[156:159], a[76:79], v[60:63], v[156:159]
s_waitcnt lgkmcnt(4)
v_mfma_f32_16x16x32_bf16 v[160:163], a[80:83], v[56:59], v[160:163]
ds_read_b128 a[112:115], v9 offset:61440
v_mfma_f32_16x16x32_bf16 v[164:167], a[80:83], v[60:63], v[164:167]
v_mfma_f32_16x16x32_bf16 v[168:171], a[84:87], v[56:59], v[168:171]
ds_read_b128 a[116:119], v9 offset:62464
v_mfma_f32_16x16x32_bf16 v[172:175], a[84:87], v[60:63], v[172:175]
v_mfma_f32_16x16x32_bf16 v[176:179], a[88:91], v[56:59], v[176:179]
ds_read_b128 a[120:123], v9 offset:63488
v_mfma_f32_16x16x32_bf16 v[180:183], a[88:91], v[60:63], v[180:183]
v_mfma_f32_16x16x32_bf16 v[184:187], a[92:95], v[56:59], v[184:187]
ds_read_b128 a[124:127], v9 offset:64512
v_mfma_f32_16x16x32_bf16 v[188:191], a[92:95], v[60:63], v[188:191]
s_waitcnt lgkmcnt(4)
v_mfma_f32_16x16x32_bf16 v[192:195], a[96:99], v[56:59], v[192:195]
v_mfma_f32_16x16x32_bf16 v[196:199], a[96:99], v[60:63], v[196:199]
v_mfma_f32_16x16x32_bf16 v[200:203], a[100:103], v[56:59], v[200:203]
v_mfma_f32_16x16x32_bf16 v[204:207], a[100:103], v[60:63], v[204:207]
v_mfma_f32_16x16x32_bf16 v[208:211], a[104:107], v[56:59], v[208:211]
v_mfma_f32_16x16x32_bf16 v[212:215], a[104:107], v[60:63], v[212:215]
v_mfma_f32_16x16x32_bf16 v[216:219], a[108:111], v[56:59], v[216:219]
v_mfma_f32_16x16x32_bf16 v[220:223], a[108:111], v[60:63], v[220:223]
s_waitcnt lgkmcnt(0)
v_mfma_f32_16x16x32_bf16 v[224:227], a[112:115], v[56:59], v[224:227]
v_mfma_f32_16x16x32_bf16 v[228:231], a[112:115], v[60:63], v[228:231]
v_mfma_f32_16x16x32_bf16 v[232:235], a[116:119], v[56:59], v[232:235]
v_mfma_f32_16x16x32_bf16 v[236:239], a[116:119], v[60:63], v[236:239]
v_mfma_f32_16x16x32_bf16 v[240:243], a[120:123], v[56:59], v[240:243]
v_mfma_f32_16x16x32_bf16 v[244:247], a[120:123], v[60:63], v[244:247]
v_mfma_f32_16x16x32_bf16 v[248:251], a[124:127], v[56:59], v[248:251]
v_mfma_f32_16x16x32_bf16 v[252:255], a[124:127], v[60:63], v[252:255]
s_branch L_epi
L_maskF:
s_nop 7
s_lshl_b32 s50, s45, 6
v_add_u32 v26, s50, v22
v_cmp_le_i32 s[56:57], 0, v26
v_cmp_le_i32 s[58:59], 1, v26
v_cmp_le_i32 s[60:61], 2, v26
v_cmp_le_i32 s[62:63], 3, v26
v_cndmask_b32 v64, v23, v64, s[56:57]
v_cmp_le_i32 s[56:57], -16, v26
v_cndmask_b32 v65, v23, v65, s[58:59]
v_cmp_le_i32 s[58:59], -15, v26
v_cndmask_b32 v66, v23, v66, s[60:61]
v_cmp_le_i32 s[60:61], -14, v26
v_cndmask_b32 v67, v23, v67, s[62:63]
v_cmp_le_i32 s[62:63], -13, v26
v_cndmask_b32 v68, v23, v68, s[56:57]
v_cmp_le_i32 s[56:57], 16, v26
v_cndmask_b32 v69, v23, v69, s[58:59]
v_cmp_le_i32 s[58:59], 17, v26
v_cndmask_b32 v70, v23, v70, s[60:61]
v_cmp_le_i32 s[60:61], 18, v26
v_cndmask_b32 v71, v23, v71, s[62:63]
v_cmp_le_i32 s[62:63], 19, v26
v_cndmask_b32 v72, v23, v72, s[56:57]
v_cmp_le_i32 s[56:57], 0, v26
v_cndmask_b32 v73, v23, v73, s[58:59]
v_cmp_le_i32 s[58:59], 1, v26
v_cndmask_b32 v74, v23, v74, s[60:61]
v_cmp_le_i32 s[60:61], 2, v26
v_cndmask_b32 v75, v23, v75, s[62:63]
v_cmp_le_i32 s[62:63], 3, v26
v_cndmask_b32 v76, v23, v76, s[56:57]
v_cmp_le_i32 s[56:57], 32, v26
v_cndmask_b32 v77, v23, v77, s[58:59]
v_cmp_le_i32 s[58:59], 33, v26
v_cndmask_b32 v78, v23, v78, s[60:61]
v_cmp_le_i32 s[60:61], 34, v26
v_cndmask_b32 v79, v23, v79, s[62:63]
v_cmp_le_i32 s[62:63], 35, v26
v_cndmask_b32 v80, v23, v80, s[56:57]
v_cmp_le_i32 s[56:57], 16, v26
v_cndmask_b32 v81, v23, v81, s[58:59]
v_cmp_le_i32 s[58:59], 17, v26
v_cndmask_b32 v82, v23, v82, s[60:61]
v_cmp_le_i32 s[60:61], 18, v26
v_cndmask_b32 v83, v23, v83, s[62:63]
v_cmp_le_i32 s[62:63], 19, v26
v_cndmask_b32 v84, v23, v84, s[56:57]
v_cmp_le_i32 s[56:57], 48, v26
v_cndmask_b32 v85, v23, v85, s[58:59]
v_cmp_le_i32 s[58:59], 49, v26
v_cndmask_b32 v86, v23, v86, s[60:61]
v_cmp_le_i32 s[60:61], 50, v26
v_cndmask_b32 v87, v23, v87, s[62:63]
v_cmp_le_i32 s[62:63], 51, v26
v_cndmask_b32 v88, v23, v88, s[56:57]
v_cmp_le_i32 s[56:57], 32, v26
v_cndmask_b32 v89, v23, v89, s[58:59]
v_cmp_le_i32 s[58:59], 33, v26
v_cndmask_b32 v90, v23, v90, s[60:61]
v_cmp_le_i32 s[60:61], 34, v26
v_cndmask_b32 v91, v23, v91, s[62:63]
v_cmp_le_i32 s[62:63], 35, v26
v_cndmask_b32 v92, v23, v92, s[56:57]
s_nop 0
v_cndmask_b32 v93, v23, v93, s[58:59]
s_nop 0
v_cndmask_b32 v94, v23, v94, s[60:61]
s_nop 0
v_cndmask_b32 v95, v23, v95, s[62:63]
s_branch L_maskretF
L_rareF:
v_max3_f32 v26, v64, v65, v66
v_max3_f32 v26, v26, v67, v72
v_max3_f32 v26, v26, v73, v74
v_max3_f32 v26, v26, v75, v80
v_max3_f32 v26, v26, v81, v82
v_max3_f32 v26, v26, v83, v88
v_max3_f32 v26, v26, v89, v90
v_max_f32 v26, v26, v91
v_max3_f32 v27, v68, v69, v70
v_max3_f32 v27, v27, v71, v76
v_max3_f32 v27, v27, v77, v78
v_max3_f32 v27, v27, v79, v84
v_max3_f32 v27, v27, v85, v86
v_max3_f32 v27, v27, v87, v92
v_max3_f32 v27, v27, v93, v94
v_max_f32 v27, v27, v95
s_nop 1
v_permlane32_swap_b32 v26, v27
v_max_f32 v26, v26, v27
v_mov_b32 v27, v26
s_nop 1
v_permlane16_swap_b32 v26, v27
v_max_f32 v26, v26, v27
v_mov_b32 v27, v26
s_nop 1
v_permlane32_swap_b32 v26, v27
v_max_f32 v26, s48, v26
v_max_f32 v28, 0, v26
v_exp_f32 v24, -v28
v_max_f32 v27, s48, v27
v_max_f32 v29, 0, v27
v_exp_f32 v25, -v29
v_sub_f32 v64, v64, v26
v_sub_f32 v65, v65, v26
v_sub_f32 v66, v66, v26
v_sub_f32 v67, v67, v26
v_sub_f32 v72, v72, v26
v_sub_f32 v73, v73, v26
v_sub_f32 v74, v74, v26
v_sub_f32 v75, v75, v26
v_sub_f32 v80, v80, v26
v_sub_f32 v81, v81, v26
v_sub_f32 v82, v82, v26
v_sub_f32 v83, v83, v26
v_sub_f32 v88, v88, v26
v_sub_f32 v89, v89, v26
v_sub_f32 v90, v90, v26
v_sub_f32 v91, v91, v26
v_sub_f32 v40, v40, v26
v_sub_f32 v41, v41, v26
v_sub_f32 v42, v42, v26
v_sub_f32 v43, v43, v26
v_mul_f32 v16, v16, v24
v_mul_f32 v17, v17, v24
v_sub_f32 v68, v68, v27
v_sub_f32 v69, v69, v27
v_sub_f32 v70, v70, v27
v_sub_f32 v71, v71, v27
v_sub_f32 v76, v76, v27
v_sub_f32 v77, v77, v27
v_sub_f32 v78, v78, v27
v_sub_f32 v79, v79, v27
v_sub_f32 v84, v84, v27
v_sub_f32 v85, v85, v27
v_sub_f32 v86, v86, v27
v_sub_f32 v87, v87, v27
v_sub_f32 v92, v92, v27
v_sub_f32 v93, v93, v27
v_sub_f32 v94, v94, v27
v_sub_f32 v95, v95, v27
v_sub_f32 v44, v44, v27
v_sub_f32 v45, v45, v27
v_sub_f32 v46, v46, v27
v_sub_f32 v47, v47, v27
v_mul_f32 v18, v18, v25
v_mul_f32 v19, v19, v25
s_mov_b32 s46, 0x41000000
s_mov_b32 s48, 0
s_branch L_rareretF
L_mask0:
s_nop 7
s_lshl_b32 s50, s45, 6
v_add_u32 v26, s50, v22
v_cmp_le_i32 s[56:57], 0, v26
v_cmp_le_i32 s[58:59], 1, v26
v_cmp_le_i32 s[60:61], 2, v26
v_cmp_le_i32 s[62:63], 3, v26
v_cndmask_b32 v64, v23, v64, s[56:57]
v_cmp_le_i32 s[56:57], -16, v26
v_cndmask_b32 v65, v23, v65, s[58:59]
v_cmp_le_i32 s[58:59], -15, v26
v_cndmask_b32 v66, v23, v66, s[60:61]
v_cmp_le_i32 s[60:61], -14, v26
v_cndmask_b32 v67, v23, v67, s[62:63]
v_cmp_le_i32 s[62:63], -13, v26
v_cndmask_b32 v68, v23, v68, s[56:57]
v_cmp_le_i32 s[56:57], 16, v26
v_cndmask_b32 v69, v23, v69, s[58:59]
v_cmp_le_i32 s[58:59], 17, v26
v_cndmask_b32 v70, v23, v70, s[60:61]
v_cmp_le_i32 s[60:61], 18, v26
v_cndmask_b32 v71, v23, v71, s[62:63]
v_cmp_le_i32 s[62:63], 19, v26
v_cndmask_b32 v72, v23, v72, s[56:57]
v_cmp_le_i32 s[56:57], 0, v26
v_cndmask_b32 v73, v23, v73, s[58:59]
v_cmp_le_i32 s[58:59], 1, v26
v_cndmask_b32 v74, v23, v74, s[60:61]
v_cmp_le_i32 s[60:61], 2, v26
v_cndmask_b32 v75, v23, v75, s[62:63]
v_cmp_le_i32 s[62:63], 3, v26
v_cndmask_b32 v76, v23, v76, s[56:57]
v_cmp_le_i32 s[56:57], 32, v26
v_cndmask_b32 v77, v23, v77, s[58:59]
v_cmp_le_i32 s[58:59], 33, v26
v_cndmask_b32 v78, v23, v78, s[60:61]
v_cmp_le_i32 s[60:61], 34, v26
v_cndmask_b32 v79, v23, v79, s[62:63]
v_cmp_le_i32 s[62:63], 35, v26
v_cndmask_b32 v80, v23, v80, s[56:57]
v_cmp_le_i32 s[56:57], 16, v26
v_cndmask_b32 v81, v23, v81, s[58:59]
v_cmp_le_i32 s[58:59], 17, v26
v_cndmask_b32 v82, v23, v82, s[60:61]
v_cmp_le_i32 s[60:61], 18, v26
v_cndmask_b32 v83, v23, v83, s[62:63]
v_cmp_le_i32 s[62:63], 19, v26
v_cndmask_b32 v84, v23, v84, s[56:57]
v_cmp_le_i32 s[56:57], 48, v26
v_cndmask_b32 v85, v23, v85, s[58:59]
v_cmp_le_i32 s[58:59], 49, v26
v_cndmask_b32 v86, v23, v86, s[60:61]
v_cmp_le_i32 s[60:61], 50, v26
v_cndmask_b32 v87, v23, v87, s[62:63]
v_cmp_le_i32 s[62:63], 51, v26
v_cndmask_b32 v88, v23, v88, s[56:57]
v_cmp_le_i32 s[56:57], 32, v26
v_cndmask_b32 v89, v23, v89, s[58:59]
v_cmp_le_i32 s[58:59], 33, v26
v_cndmask_b32 v90, v23, v90, s[60:61]
v_cmp_le_i32 s[60:61], 34, v26
v_cndmask_b32 v91, v23, v91, s[62:63]
v_cmp_le_i32 s[62:63], 35, v26
v_cndmask_b32 v92, v23, v92, s[56:57]
s_nop 0
v_cndmask_b32 v93, v23, v93, s[58:59]
s_nop 0
v_cndmask_b32 v94, v23, v94, s[60:61]
s_nop 0
v_cndmask_b32 v95, v23, v95, s[62:63]
s_branch L_maskret0
L_rare0:
v_max3_f32 v26, v64, v65, v66
v_max3_f32 v26, v26, v67, v72
v_max3_f32 v26, v26, v73, v74
v_max3_f32 v26, v26, v75, v80
v_max3_f32 v26, v26, v81, v82
v_max3_f32 v26, v26, v83, v88
v_max3_f32 v26, v26, v89, v90
v_max_f32 v26, v26, v91
v_max3_f32 v27, v68, v69, v70
v_max3_f32 v27, v27, v71, v76
v_max3_f32 v27, v27, v77, v78
v_max3_f32 v27, v27, v79, v84
v_max3_f32 v27, v27, v85, v86
v_max3_f32 v27, v27, v87, v92
v_max3_f32 v27, v27, v93, v94
v_max_f32 v27, v27, v95
s_nop 1
v_permlane32_swap_b32 v26, v27
v_max_f32 v26, v26, v27
v_mov_b32 v27, v26
s_nop 1
v_permlane16_swap_b32 v26, v27
v_max_f32 v26, v26, v27
v_mov_b32 v27, v26
s_nop 1
v_permlane32_swap_b32 v26, v27
v_max_f32 v26, s48, v26
v_max_f32 v28, 0, v26
v_exp_f32 v24, -v28
v_max_f32 v27, s48, v27
v_max_f32 v29, 0, v27
v_exp_f32 v25, -v29
v_sub_f32 v64, v64, v26
v_sub_f32 v65, v65, v26
v_sub_f32 v66, v66, v26
v_sub_f32 v67, v67, v26
v_sub_f32 v72, v72, v26
v_sub_f32 v73, v73, v26
v_sub_f32 v74, v74, v26
v_sub_f32 v75, v75, v26
v_sub_f32 v80, v80, v26
v_sub_f32 v81, v81, v26
v_sub_f32 v82, v82, v26
v_sub_f32 v83, v83, v26
v_sub_f32 v88, v88, v26
v_sub_f32 v89, v89, v26
v_sub_f32 v90, v90, v26
v_sub_f32 v91, v91, v26
v_sub_f32 v40, v40, v26
v_sub_f32 v41, v41, v26
v_sub_f32 v42, v42, v26
v_sub_f32 v43, v43, v26
v_mul_f32 v16, v16, v24
v_mul_f32 v17, v17, v24
v_sub_f32 v68, v68, v27
v_sub_f32 v69, v69, v27
v_sub_f32 v70, v70, v27
v_sub_f32 v71, v71, v27
v_sub_f32 v76, v76, v27
v_sub_f32 v77, v77, v27
v_sub_f32 v78, v78, v27
v_sub_f32 v79, v79, v27
v_sub_f32 v84, v84, v27
v_sub_f32 v85, v85, v27
v_sub_f32 v86, v86, v27
v_sub_f32 v87, v87, v27
v_sub_f32 v92, v92, v27
v_sub_f32 v93, v93, v27
v_sub_f32 v94, v94, v27
v_sub_f32 v95, v95, v27
v_sub_f32 v44, v44, v27
v_sub_f32 v45, v45, v27
v_sub_f32 v46, v46, v27
v_sub_f32 v47, v47, v27
v_mul_f32 v18, v18, v25
v_mul_f32 v19, v19, v25
s_mov_b32 s46, 0x41000000
s_mov_b32 s48, 0
s_mov_b32 s47, 1
s_branch L_rareret0
L_resc0:
s_nop 7
v_mul_f32 v128, v128, v24
v_mul_f32 v129, v129, v24
v_mul_f32 v130, v130, v24
v_mul_f32 v131, v131, v24
v_mul_f32 v132, v132, v25
v_mul_f32 v133, v133, v25
v_mul_f32 v134, v134, v25
v_mul_f32 v135, v135, v25
v_mul_f32 v136, v136, v24
v_mul_f32 v137, v137, v24
v_mul_f32 v138, v138, v24
v_mul_f32 v139, v139, v24
v_mul_f32 v140, v140, v25
v_mul_f32 v141, v141, v25
v_mul_f32 v142, v142, v25
v_mul_f32 v143, v143, v25
v_mul_f32 v144, v144, v24
v_mul_f32 v145, v145, v24
v_mul_f32 v146, v146, v24
v_mul_f32 v147, v147, v24
v_mul_f32 v148, v148, v25
v_mul_f32 v149, v149, v25
v_mul_f32 v150, v150, v25
v_mul_f32 v151, v151, v25
v_mul_f32 v152, v152, v24
v_mul_f32 v153, v153, v24
v_mul_f32 v154, v154, v24
v_mul_f32 v155, v155, v24
v_mul_f32 v156, v156, v25
v_mul_f32 v157, v157, v25
v_mul_f32 v158, v158, v25
v_mul_f32 v159, v159, v25
v_mul_f32 v160, v160, v24
v_mul_f32 v161, v161, v24
v_mul_f32 v162, v162, v24
v_mul_f32 v163, v163, v24
v_mul_f32 v164, v164, v25
v_mul_f32 v165, v165, v25
v_mul_f32 v166, v166, v25
v_mul_f32 v167, v167, v25
v_mul_f32 v168, v168, v24
v_mul_f32 v169, v169, v24
v_mul_f32 v170, v170, v24
v_mul_f32 v171, v171, v24
v_mul_f32 v172, v172, v25
v_mul_f32 v173, v173, v25
v_mul_f32 v174, v174, v25
v_mul_f32 v175, v175, v25
v_mul_f32 v176, v176, v24
v_mul_f32 v177, v177, v24
v_mul_f32 v178, v178, v24
v_mul_f32 v179, v179, v24
v_mul_f32 v180, v180, v25
v_mul_f32 v181, v181, v25
v_mul_f32 v182, v182, v25
v_mul_f32 v183, v183, v25
v_mul_f32 v184, v184, v24
v_mul_f32 v185, v185, v24
v_mul_f32 v186, v186, v24
v_mul_f32 v187, v187, v24
v_mul_f32 v188, v188, v25
v_mul_f32 v189, v189, v25
v_mul_f32 v190, v190, v25
v_mul_f32 v191, v191, v25
v_mul_f32 v192, v192, v24
v_mul_f32 v193, v193, v24
v_mul_f32 v194, v194, v24
v_mul_f32 v195, v195, v24
v_mul_f32 v196, v196, v25
v_mul_f32 v197, v197, v25
v_mul_f32 v198, v198, v25
v_mul_f32 v199, v199, v25
v_mul_f32 v200, v200, v24
v_mul_f32 v201, v201, v24
v_mul_f32 v202, v202, v24
v_mul_f32 v203, v203, v24
v_mul_f32 v204, v204, v25
v_mul_f32 v205, v205, v25
v_mul_f32 v206, v206, v25
v_mul_f32 v207, v207, v25
v_mul_f32 v208, v208, v24
v_mul_f32 v209, v209, v24
v_mul_f32 v210, v210, v24
v_mul_f32 v211, v211, v24
v_mul_f32 v212, v212, v25
v_mul_f32 v213, v213, v25
v_mul_f32 v214, v214, v25
v_mul_f32 v215, v215, v25
v_mul_f32 v216, v216, v24
v_mul_f32 v217, v217, v24
v_mul_f32 v218, v218, v24
v_mul_f32 v219, v219, v24
v_mul_f32 v220, v220, v25
v_mul_f32 v221, v221, v25
v_mul_f32 v222, v222, v25
v_mul_f32 v223, v223, v25
v_mul_f32 v224, v224, v24
v_mul_f32 v225, v225, v24
v_mul_f32 v226, v226, v24
v_mul_f32 v227, v227, v24
v_mul_f32 v228, v228, v25
v_mul_f32 v229, v229, v25
v_mul_f32 v230, v230, v25
v_mul_f32 v231, v231, v25
v_mul_f32 v232, v232, v24
v_mul_f32 v233, v233, v24
v_mul_f32 v234, v234, v24
v_mul_f32 v235, v235, v24
v_mul_f32 v236, v236, v25
v_mul_f32 v237, v237, v25
v_mul_f32 v238, v238, v25
v_mul_f32 v239, v239, v25
v_mul_f32 v240, v240, v24
v_mul_f32 v241, v241, v24
v_mul_f32 v242, v242, v24
v_mul_f32 v243, v243, v24
v_mul_f32 v244, v244, v25
v_mul_f32 v245, v245, v25
v_mul_f32 v246, v246, v25
v_mul_f32 v247, v247, v25
v_mul_f32 v248, v248, v24
v_mul_f32 v249, v249, v24
v_mul_f32 v250, v250, v24
v_mul_f32 v251, v251, v24
v_mul_f32 v252, v252, v25
v_mul_f32 v253, v253, v25
v_mul_f32 v254, v254, v25
v_mul_f32 v255, v255, v25
s_mov_b32 s47, 0
s_nop 1
s_branch L_rescret0
L_mask1:
s_nop 7
s_lshl_b32 s50, s45, 6
v_add_u32 v26, s50, v22
v_cmp_le_i32 s[56:57], 0, v26
v_cmp_le_i32 s[58:59], 1, v26
v_cmp_le_i32 s[60:61], 2, v26
v_cmp_le_i32 s[62:63], 3, v26
v_cndmask_b32 v96, v23, v96, s[56:57]
v_cmp_le_i32 s[56:57], -16, v26
v_cndmask_b32 v97, v23, v97, s[58:59]
v_cmp_le_i32 s[58:59], -15, v26
v_cndmask_b32 v98, v23, v98, s[60:61]
v_cmp_le_i32 s[60:61], -14, v26
v_cndmask_b32 v99, v23, v99, s[62:63]
v_cmp_le_i32 s[62:63], -13, v26
v_cndmask_b32 v100, v23, v100, s[56:57]
v_cmp_le_i32 s[56:57], 16, v26
v_cndmask_b32 v101, v23, v101, s[58:59]
v_cmp_le_i32 s[58:59], 17, v26
v_cndmask_b32 v102, v23, v102, s[60:61]
v_cmp_le_i32 s[60:61], 18, v26
v_cndmask_b32 v103, v23, v103, s[62:63]
v_cmp_le_i32 s[62:63], 19, v26
v_cndmask_b32 v104, v23, v104, s[56:57]
v_cmp_le_i32 s[56:57], 0, v26
v_cndmask_b32 v105, v23, v105, s[58:59]
v_cmp_le_i32 s[58:59], 1, v26
v_cndmask_b32 v106, v23, v106, s[60:61]
v_cmp_le_i32 s[60:61], 2, v26
v_cndmask_b32 v107, v23, v107, s[62:63]
v_cmp_le_i32 s[62:63], 3, v26
v_cndmask_b32 v108, v23, v108, s[56:57]
v_cmp_le_i32 s[56:57], 32, v26
v_cndmask_b32 v109, v23, v109, s[58:59]
v_cmp_le_i32 s[58:59], 33, v26
v_cndmask_b32 v110, v23, v110, s[60:61]
v_cmp_le_i32 s[60:61], 34, v26
v_cndmask_b32 v111, v23, v111, s[62:63]
v_cmp_le_i32 s[62:63], 35, v26
v_cndmask_b32 v112, v23, v112, s[56:57]
v_cmp_le_i32 s[56:57], 16, v26
v_cndmask_b32 v113, v23, v113, s[58:59]
v_cmp_le_i32 s[58:59], 17, v26
v_cndmask_b32 v114, v23, v114, s[60:61]
v_cmp_le_i32 s[60:61], 18, v26
v_cndmask_b32 v115, v23, v115, s[62:63]
v_cmp_le_i32 s[62:63], 19, v26
v_cndmask_b32 v116, v23, v116, s[56:57]
v_cmp_le_i32 s[56:57], 48, v26
v_cndmask_b32 v117, v23, v117, s[58:59]
v_cmp_le_i32 s[58:59], 49, v26
v_cndmask_b32 v118, v23, v118, s[60:61]
v_cmp_le_i32 s[60:61], 50, v26
v_cndmask_b32 v119, v23, v119, s[62:63]
v_cmp_le_i32 s[62:63], 51, v26
v_cndmask_b32 v120, v23, v120, s[56:57]
v_cmp_le_i32 s[56:57], 32, v26
v_cndmask_b32 v121, v23, v121, s[58:59]
v_cmp_le_i32 s[58:59], 33, v26
v_cndmask_b32 v122, v23, v122, s[60:61]
v_cmp_le_i32 s[60:61], 34, v26
v_cndmask_b32 v123, v23, v123, s[62:63]
v_cmp_le_i32 s[62:63], 35, v26
v_cndmask_b32 v124, v23, v124, s[56:57]
s_nop 0
v_cndmask_b32 v125, v23, v125, s[58:59]
s_nop 0
v_cndmask_b32 v126, v23, v126, s[60:61]
s_nop 0
v_cndmask_b32 v127, v23, v127, s[62:63]
s_branch L_maskret1
L_rare1:
v_max3_f32 v26, v96, v97, v98
v_max3_f32 v26, v26, v99, v104
v_max3_f32 v26, v26, v105, v106
v_max3_f32 v26, v26, v107, v112
v_max3_f32 v26, v26, v113, v114
v_max3_f32 v26, v26, v115, v120
v_max3_f32 v26, v26, v121, v122
v_max_f32 v26, v26, v123
v_max3_f32 v27, v100, v101, v102
v_max3_f32 v27, v27, v103, v108
v_max3_f32 v27, v27, v109, v110
v_max3_f32 v27, v27, v111, v116
v_max3_f32 v27, v27, v117, v118
v_max3_f32 v27, v27, v119, v124
v_max3_f32 v27, v27, v125, v126
v_max_f32 v27, v27, v127
s_nop 1
v_permlane32_swap_b32 v26, v27
v_max_f32 v26, v26, v27
v_mov_b32 v27, v26
s_nop 1
v_permlane16_swap_b32 v26, v27
v_max_f32 v26, v26, v27
v_mov_b32 v27, v26
s_nop 1
v_permlane32_swap_b32 v26, v27
v_max_f32 v26, s48, v26
v_max_f32 v28, 0, v26
v_exp_f32 v24, -v28
v_max_f32 v27, s48, v27
v_max_f32 v29, 0, v27
v_exp_f32 v25, -v29
v_sub_f32 v96, v96, v26
v_sub_f32 v97, v97, v26
v_sub_f32 v98, v98, v26
v_sub_f32 v99, v99, v26
v_sub_f32 v104, v104, v26
v_sub_f32 v105, v105, v26
v_sub_f32 v106, v106, v26
v_sub_f32 v107, v107, v26
v_sub_f32 v112, v112, v26
v_sub_f32 v113, v113, v26
v_sub_f32 v114, v114, v26
v_sub_f32 v115, v115, v26
v_sub_f32 v120, v120, v26
v_sub_f32 v121, v121, v26
v_sub_f32 v122, v122, v26
v_sub_f32 v123, v123, v26
v_sub_f32 v40, v40, v26
v_sub_f32 v41, v41, v26
v_sub_f32 v42, v42, v26
v_sub_f32 v43, v43, v26
v_mul_f32 v16, v16, v24
v_mul_f32 v17, v17, v24
v_sub_f32 v100, v100, v27
v_sub_f32 v101, v101, v27
v_sub_f32 v102, v102, v27
v_sub_f32 v103, v103, v27
v_sub_f32 v108, v108, v27
v_sub_f32 v109, v109, v27
v_sub_f32 v110, v110, v27
v_sub_f32 v111, v111, v27
v_sub_f32 v116, v116, v27
v_sub_f32 v117, v117, v27
v_sub_f32 v118, v118, v27
v_sub_f32 v119, v119, v27
v_sub_f32 v124, v124, v27
v_sub_f32 v125, v125, v27
v_sub_f32 v126, v126, v27
v_sub_f32 v127, v127, v27
v_sub_f32 v44, v44, v27
v_sub_f32 v45, v45, v27
v_sub_f32 v46, v46, v27
v_sub_f32 v47, v47, v27
v_mul_f32 v18, v18, v25
v_mul_f32 v19, v19, v25
s_mov_b32 s46, 0x41000000
s_mov_b32 s48, 0
s_mov_b32 s47, 1
s_branch L_rareret1
L_resc1:
s_nop 7
v_mul_f32 v128, v128, v24
v_mul_f32 v129, v129, v24
v_mul_f32 v130, v130, v24
v_mul_f32 v131, v131, v24
v_mul_f32 v132, v132, v25
v_mul_f32 v133, v133, v25
v_mul_f32 v134, v134, v25
v_mul_f32 v135, v135, v25
v_mul_f32 v136, v136, v24
v_mul_f32 v137, v137, v24
v_mul_f32 v138, v138, v24
v_mul_f32 v139, v139, v24
v_mul_f32 v140, v140, v25
v_mul_f32 v141, v141, v25
v_mul_f32 v142, v142, v25
v_mul_f32 v143, v143, v25
v_mul_f32 v144, v144, v24
v_mul_f32 v145, v145, v24
v_mul_f32 v146, v146, v24
v_mul_f32 v147, v147, v24
v_mul_f32 v148, v148, v25
v_mul_f32 v149, v149, v25
v_mul_f32 v150, v150, v25
v_mul_f32 v151, v151, v25
v_mul_f32 v152, v152, v24
v_mul_f32 v153, v153, v24
v_mul_f32 v154, v154, v24
v_mul_f32 v155, v155, v24
v_mul_f32 v156, v156, v25
v_mul_f32 v157, v157, v25
v_mul_f32 v158, v158, v25
v_mul_f32 v159, v159, v25
v_mul_f32 v160, v160, v24
v_mul_f32 v161, v161, v24
v_mul_f32 v162, v162, v24
v_mul_f32 v163, v163, v24
v_mul_f32 v164, v164, v25
v_mul_f32 v165, v165, v25
v_mul_f32 v166, v166, v25
v_mul_f32 v167, v167, v25
v_mul_f32 v168, v168, v24
v_mul_f32 v169, v169, v24
v_mul_f32 v170, v170, v24
v_mul_f32 v171, v171, v24
v_mul_f32 v172, v172, v25
v_mul_f32 v173, v173, v25
v_mul_f32 v174, v174, v25
v_mul_f32 v175, v175, v25
v_mul_f32 v176, v176, v24
v_mul_f32 v177, v177, v24
v_mul_f32 v178, v178, v24
v_mul_f32 v179, v179, v24
v_mul_f32 v180, v180, v25
v_mul_f32 v181, v181, v25
v_mul_f32 v182, v182, v25
v_mul_f32 v183, v183, v25
v_mul_f32 v184, v184, v24
v_mul_f32 v185, v185, v24
v_mul_f32 v186, v186, v24
v_mul_f32 v187, v187, v24
v_mul_f32 v188, v188, v25
v_mul_f32 v189, v189, v25
v_mul_f32 v190, v190, v25
v_mul_f32 v191, v191, v25
v_mul_f32 v192, v192, v24
v_mul_f32 v193, v193, v24
v_mul_f32 v194, v194, v24
v_mul_f32 v195, v195, v24
v_mul_f32 v196, v196, v25
v_mul_f32 v197, v197, v25
v_mul_f32 v198, v198, v25
v_mul_f32 v199, v199, v25
v_mul_f32 v200, v200, v24
v_mul_f32 v201, v201, v24
v_mul_f32 v202, v202, v24
v_mul_f32 v203, v203, v24
v_mul_f32 v204, v204, v25
v_mul_f32 v205, v205, v25
v_mul_f32 v206, v206, v25
v_mul_f32 v207, v207, v25
v_mul_f32 v208, v208, v24
v_mul_f32 v209, v209, v24
v_mul_f32 v210, v210, v24
v_mul_f32 v211, v211, v24
v_mul_f32 v212, v212, v25
v_mul_f32 v213, v213, v25
v_mul_f32 v214, v214, v25
v_mul_f32 v215, v215, v25
v_mul_f32 v216, v216, v24
v_mul_f32 v217, v217, v24
v_mul_f32 v218, v218, v24
v_mul_f32 v219, v219, v24
v_mul_f32 v220, v220, v25
v_mul_f32 v221, v221, v25
v_mul_f32 v222, v222, v25
v_mul_f32 v223, v223, v25
v_mul_f32 v224, v224, v24
v_mul_f32 v225, v225, v24
v_mul_f32 v226, v226, v24
v_mul_f32 v227, v227, v24
v_mul_f32 v228, v228, v25
v_mul_f32 v229, v229, v25
v_mul_f32 v230, v230, v25
v_mul_f32 v231, v231, v25
v_mul_f32 v232, v232, v24
v_mul_f32 v233, v233, v24
v_mul_f32 v234, v234, v24
v_mul_f32 v235, v235, v24
v_mul_f32 v236, v236, v25
v_mul_f32 v237, v237, v25
v_mul_f32 v238, v238, v25
v_mul_f32 v239, v239, v25
v_mul_f32 v240, v240, v24
v_mul_f32 v241, v241, v24
v_mul_f32 v242, v242, v24
v_mul_f32 v243, v243, v24
v_mul_f32 v244, v244, v25
v_mul_f32 v245, v245, v25
v_mul_f32 v246, v246, v25
v_mul_f32 v247, v247, v25
v_mul_f32 v248, v248, v24
v_mul_f32 v249, v249, v24
v_mul_f32 v250, v250, v24
v_mul_f32 v251, v251, v24
v_mul_f32 v252, v252, v25
v_mul_f32 v253, v253, v25
v_mul_f32 v254, v254, v25
v_mul_f32 v255, v255, v25
s_mov_b32 s47, 0
s_nop 1
s_branch L_rescret1
L_epi:
v_add_f32 v16, v16, v17
v_add_f32 v18, v18, v19
s_cmp_eq_u32 s74, 3
s_cselect_b32 s53, 0, 0x8000
L_kfchk:
s_waitcnt vmcnt(0)
v_min3_u32 v32, v32, v33, v34
v_min_u32 v32, v32, v35
v_cmp_eq_u32 vcc, 0, v32
s_sub_u32 s53, s53, 1
s_cbranch_scc1 L_kfok
s_cbranch_vccz L_kfok
s_sleep 8
s_mov_b32 s50, 79693824
buffer_load_dwordx4 v[32:35], v14, s[36:39], s50 offen sc1
s_branch L_kfchk
L_kfok:
v_readfirstlane_b32 s81, v36
s_mul_i32 s50, s34, 33792
s_add_u32 s50, s50, 83886080
s_cmp_eq_u32 s74, 2
s_cbranch_scc0 L_notpart
v_mov_b32 v26, v40
v_mov_b32 v27, v44
v_mov_b32 v28, v16
v_mov_b32 v29, v18
buffer_store_dwordx4 v[128:131], v14, s[36:39], s50 offen offset:0 sc1
buffer_store_dwordx4 v[132:135], v14, s[36:39], s50 offen offset:1024 sc1
buffer_store_dwordx4 v[136:139], v14, s[36:39], s50 offen offset:2048 sc1
buffer_store_dwordx4 v[140:143], v14, s[36:39], s50 offen offset:3072 sc1
s_add_u32 s50, s50, 4096
buffer_store_dwordx4 v[144:147], v14, s[36:39], s50 offen offset:0 sc1
buffer_store_dwordx4 v[148:151], v14, s[36:39], s50 offen offset:1024 sc1
buffer_store_dwordx4 v[152:155], v14, s[36:39], s50 offen offset:2048 sc1
buffer_store_dwordx4 v[156:159], v14, s[36:39], s50 offen offset:3072 sc1
s_add_u32 s50, s50, 4096
buffer_store_dwordx4 v[160:163], v14, s[36:39], s50 offen offset:0 sc1
buffer_store_dwordx4 v[164:167], v14, s[36:39], s50 offen offset:1024 sc1
buffer_store_dwordx4 v[168:171], v14, s[36:39], s50 offen offset:2048 sc1
buffer_store_dwordx4 v[172:175], v14, s[36:39], s50 offen offset:3072 sc1
s_add_u32 s50, s50, 4096
buffer_store_dwordx4 v[176:179], v14, s[36:39], s50 offen offset:0 sc1
buffer_store_dwordx4 v[180:183], v14, s[36:39], s50 offen offset:1024 sc1
buffer_store_dwordx4 v[184:187], v14, s[36:39], s50 offen offset:2048 sc1
buffer_store_dwordx4 v[188:191], v14, s[36:39], s50 offen offset:3072 sc1
s_add_u32 s50, s50, 4096
buffer_store_dwordx4 v[192:195], v14, s[36:39], s50 offen offset:0 sc1
buffer_store_dwordx4 v[196:199], v14, s[36:39], s50 offen offset:1024 sc1
buffer_store_dwordx4 v[200:203], v14, s[36:39], s50 offen offset:2048 sc1
buffer_store_dwordx4 v[204:207], v14, s[36:39], s50 offen offset:3072 sc1
s_add_u32 s50, s50, 4096
buffer_store_dwordx4 v[208:211], v14, s[36:39], s50 offen offset:0 sc1
buffer_store_dwordx4 v[212:215], v14, s[36:39], s50 offen offset:1024 sc1
buffer_store_dwordx4 v[216:219], v14, s[36:39], s50 offen offset:2048 sc1
buffer_store_dwordx4 v[220:223], v14, s[36:39], s50 offen offset:3072 sc1
s_add_u32 s50, s50, 4096
buffer_store_dwordx4 v[224:227], v14, s[36:39], s50 offen offset:0 sc1
buffer_store_dwordx4 v[228:231], v14, s[36:39], s50 offen offset:1024 sc1
buffer_store_dwordx4 v[232:235], v14, s[36:39], s50 offen offset:2048 sc1
buffer_store_dwordx4 v[236:239], v14, s[36:39], s50 offen offset:3072 sc1
s_add_u32 s50, s50, 4096
buffer_store_dwordx4 v[240:243], v14, s[36:39], s50 offen offset:0 sc1
buffer_store_dwordx4 v[244:247], v14, s[36:39], s50 offen offset:1024 sc1
buffer_store_dwordx4 v[248:251], v14, s[36:39], s50 offen offset:2048 sc1
buffer_store_dwordx4 v[252:255], v14, s[36:39], s50 offen offset:3072 sc1
s_add_u32 s50, s50, 4096
buffer_store_dwordx4 v[26:29], v14, s[36:39], s50 offen sc1
s_add_u32 s78, s77, 8192
s_branch L_sig
L_notpart:
s_cmp_eq_u32 s74, 0
s_cbranch_scc1 L_fin
s_cmp_lg_u32 s81, 0
s_cbranch_scc1 L_flgok
s_mov_b32 s53, 0x8000
v_mov_b32 v26, 0
L_pollP:
buffer_load_dword v27, v26, s[36:39], s77 offen sc1
s_waitcnt vmcnt(0)
v_readfirstlane_b32 s52, v27
s_sub_u32 s53, s53, 1
s_cselect_b32 s52, 1, s52
s_cmp_lg_u32 s52, 0
s_cbranch_scc1 L_pollokP
s_sleep 2
s_branch L_pollP
L_pollokP:
L_flgok:
s_cmp_eq_u32 s74, 1
s_cbranch_scc1 L_merge
s_lshl_b32 s40, s49, 11
s_mov_b32 s41, s40
s_add_u32 m0, s42, 0
s_nop 0
buffer_load_dwordx4 v10, s[24:27], s40 offen sc1 lds
buffer_load_dwordx4 v10, s[24:27], s40 offen offset:1024 sc1 lds
s_add_u32 m0, s42, 8192
s_nop 0
buffer_load_dwordx4 v11, s[24:27], s40 offen sc1 lds
buffer_load_dwordx4 v11, s[24:27], s40 offen offset:1024 sc1 lds
s_add_u32 m0, s42, 16384
s_nop 0
buffer_load_dwordx4 v12, s[24:27], s40 offen sc1 lds
buffer_load_dwordx4 v12, s[24:27], s40 offen offset:1024 sc1 lds
s_add_u32 m0, s42, 24576
s_nop 0
buffer_load_dwordx4 v13, s[24:27], s40 offen sc1 lds
buffer_load_dwordx4 v13, s[24:27], s40 offen offset:1024 sc1 lds
s_add_u32 s40, s40, 32768
s_lshl_b32 s50, s34, 14
buffer_load_dwordx4 a[0:3], v14, s[36:39], s50 offen offset:0 sc1
buffer_load_dwordx4 a[4:7], v14, s[36:39], s50 offen offset:1024 sc1
buffer_load_dwordx4 a[8:11], v14, s[36:39], s50 offen offset:2048 sc1
buffer_load_dwordx4 a[12:15], v14, s[36:39], s50 offen offset:3072 sc1
s_add_u32 s50, s50, 4096
buffer_load_dwordx4 a[16:19], v14, s[36:39], s50 offen offset:0 sc1
buffer_load_dwordx4 a[20:23], v14, s[36:39], s50 offen offset:1024 sc1
buffer_load_dwordx4 a[24:27], v14, s[36:39], s50 offen offset:2048 sc1
buffer_load_dwordx4 a[28:31], v14, s[36:39], s50 offen offset:3072 sc1
s_add_u32 s50, s50, 4096
buffer_load_dwordx4 a[32:35], v14, s[36:39], s50 offen offset:0 sc1
buffer_load_dwordx4 a[36:39], v14, s[36:39], s50 offen offset:1024 sc1
buffer_load_dwordx4 a[40:43], v14, s[36:39], s50 offen offset:2048 sc1
buffer_load_dwordx4 a[44:47], v14, s[36:39], s50 offen offset:3072 sc1
s_add_u32 s50, s50, 4096
buffer_load_dwordx4 a[48:51], v14, s[36:39], s50 offen offset:0 sc1
buffer_load_dwordx4 a[52:55], v14, s[36:39], s50 offen offset:1024 sc1
buffer_load_dwordx4 a[56:59], v14, s[36:39], s50 offen offset:2048 sc1
buffer_load_dwordx4 a[60:63], v14, s[36:39], s50 offen offset:3072 sc1
s_add_u32 m0, s42, 32768
s_nop 0
buffer_load_dwordx4 v10, s[24:27], s40 offen sc1 lds
buffer_load_dwordx4 v10, s[24:27], s40 offen offset:1024 sc1 lds
s_add_u32 m0, s42, 40960
s_nop 0
buffer_load_dwordx4 v11, s[24:27], s40 offen sc1 lds
buffer_load_dwordx4 v11, s[24:27], s40 offen offset:1024 sc1 lds
s_add_u32 m0, s42, 49152
s_nop 0
buffer_load_dwordx4 v12, s[24:27], s40 offen sc1 lds
buffer_load_dwordx4 v12, s[24:27], s40 offen offset:1024 sc1 lds
s_add_u32 m0, s42, 57344
s_nop 0
buffer_load_dwordx4 v13, s[24:27], s40 offen sc1 lds
buffer_load_dwordx4 v13, s[24:27], s40 offen offset:1024 sc1 lds
s_add_u32 s40, s40, 32768
s_branch L_fin
L_merge:
s_add_u32 s51, s50, 32768
buffer_load_dwordx4 v[26:29], v14, s[36:39], s51 offen sc1
buffer_load_dwordx4 v[48:51], v14, s[36:39], s50 offen offset:0 sc1
buffer_load_dwordx4 v[52:55], v14, s[36:39], s50 offen offset:1024 sc1
buffer_load_dwordx4 v[56:59], v14, s[36:39], s50 offen offset:2048 sc1
buffer_load_dwordx4 v[60:63], v14, s[36:39], s50 offen offset:3072 sc1
s_add_u32 s50, s50, 4096
buffer_load_dwordx4 v[64:67], v14, s[36:39], s50 offen offset:0 sc1
buffer_load_dwordx4 v[68:71], v14, s[36:39], s50 offen offset:1024 sc1
buffer_load_dwordx4 v[72:75], v14, s[36:39], s50 offen offset:2048 sc1
buffer_load_dwordx4 v[76:79], v14, s[36:39], s50 offen offset:3072 sc1
s_add_u32 s50, s50, 4096
buffer_load_dwordx4 v[80:83], v14, s[36:39], s50 offen offset:0 sc1
buffer_load_dwordx4 v[84:87], v14, s[36:39], s50 offen offset:1024 sc1
buffer_load_dwordx4 v[88:91], v14, s[36:39], s50 offen offset:2048 sc1
buffer_load_dwordx4 v[92:95], v14, s[36:39], s50 offen offset:3072 sc1
s_add_u32 s50, s50, 4096
buffer_load_dwordx4 v[96:99], v14, s[36:39], s50 offen offset:0 sc1
buffer_load_dwordx4 v[100:103], v14, s[36:39], s50 offen offset:1024 sc1
buffer_load_dwordx4 v[104:107], v14, s[36:39], s50 offen offset:2048 sc1
buffer_load_dwordx4 v[108:111], v14, s[36:39], s50 offen offset:3072 sc1
s_add_u32 s50, s50, 4096
buffer_load_dwordx4 v[112:115], v14, s[36:39], s50 offen offset:0 sc1
buffer_load_dwordx4 v[116:119], v14, s[36:39], s50 offen offset:1024 sc1
buffer_load_dwordx4 v[120:123], v14, s[36:39], s50 offen offset:2048 sc1
buffer_load_dwordx4 v[124:127], v14, s[36:39], s50 offen offset:3072 sc1
s_add_u32 s50, s50, 4096
buffer_load_dwordx4 a[0:3], v14, s[36:39], s50 offen offset:0 sc1
buffer_load_dwordx4 a[4:7], v14, s[36:39], s50 offen offset:1024 sc1
buffer_load_dwordx4 a[8:11], v14, s[36:39], s50 offen offset:2048 sc1
buffer_load_dwordx4 a[12:15], v14, s[36:39], s50 offen offset:3072 sc1
s_add_u32 s50, s50, 4096
buffer_load_dwordx4 a[16:19], v14, s[36:39], s50 offen offset:0 sc1
buffer_load_dwordx4 a[20:23], v14, s[36:39], s50 offen offset:1024 sc1
buffer_load_dwordx4 a[24:27], v14, s[36:39], s50 offen offset:2048 sc1
buffer_load_dwordx4 a[28:31], v14, s[36:39], s50 offen offset:3072 sc1
s_add_u32 s50, s50, 4096
buffer_load_dwordx4 a[32:35], v14, s[36:39], s50 offen offset:0 sc1
buffer_load_dwordx4 a[36:39], v14, s[36:39], s50 offen offset:1024 sc1
buffer_load_dwordx4 a[40:43], v14, s[36:39], s50 offen offset:2048 sc1
buffer_load_dwordx4 a[44:47], v14, s[36:39], s50 offen offset:3072 sc1
s_add_u32 s50, s50, 4096
s_waitcnt vmcnt(32)
v_min_f32 v34, v26, v40
v_sub_f32 v30, v34, v40
v_sub_f32 v32, v34, v26
v_exp_f32 v30, v30
v_exp_f32 v32, v32
s_nop 0
v_mul_f32 v16, v16, v30
v_fmac_f32 v16, v28, v32
v_min_f32 v34, v27, v44
v_sub_f32 v31, v34, v44
v_sub_f32 v33, v34, v27
v_exp_f32 v31, v31
v_exp_f32 v33, v33
s_nop 0
v_mul_f32 v18, v18, v31
v_fmac_f32 v18, v29, v33
v_mul_f32 v128, v128, v30
v_mul_f32 v129, v129, v30
v_mul_f32 v130, v130, v30
v_mul_f32 v131, v131, v30
v_mul_f32 v132, v132, v31
v_mul_f32 v133, v133, v31
v_mul_f32 v134, v134, v31
v_mul_f32 v135, v135, v31
v_mul_f32 v136, v136, v30
v_mul_f32 v137, v137, v30
v_mul_f32 v138, v138, v30
v_mul_f32 v139, v139, v30
v_mul_f32 v140, v140, v31
v_mul_f32 v141, v141, v31
v_mul_f32 v142, v142, v31
v_mul_f32 v143, v143, v31
v_mul_f32 v144, v144, v30
v_mul_f32 v145, v145, v30
v_mul_f32 v146, v146, v30
v_mul_f32 v147, v147, v30
v_mul_f32 v148, v148, v31
v_mul_f32 v149, v149, v31
v_mul_f32 v150, v150, v31
v_mul_f32 v151, v151, v31
v_mul_f32 v152, v152, v30
v_mul_f32 v153, v153, v30
v_mul_f32 v154, v154, v30
v_mul_f32 v155, v155, v30
v_mul_f32 v156, v156, v31
v_mul_f32 v157, v157, v31
v_mul_f32 v158, v158, v31
v_mul_f32 v159, v159, v31
v_mul_f32 v160, v160, v30
v_mul_f32 v161, v161, v30
v_mul_f32 v162, v162, v30
v_mul_f32 v163, v163, v30
v_mul_f32 v164, v164, v31
v_mul_f32 v165, v165, v31
v_mul_f32 v166, v166, v31
v_mul_f32 v167, v167, v31
v_mul_f32 v168, v168, v30
v_mul_f32 v169, v169, v30
v_mul_f32 v170, v170, v30
v_mul_f32 v171, v171, v30
v_mul_f32 v172, v172, v31
v_mul_f32 v173, v173, v31
v_mul_f32 v174, v174, v31
v_mul_f32 v175, v175, v31
v_mul_f32 v176, v176, v30
v_mul_f32 v177, v177, v30
v_mul_f32 v178, v178, v30
v_mul_f32 v179, v179, v30
v_mul_f32 v180, v180, v31
v_mul_f32 v181, v181, v31
v_mul_f32 v182, v182, v31
v_mul_f32 v183, v183, v31
v_mul_f32 v184, v184, v30
v_mul_f32 v185, v185, v30
v_mul_f32 v186, v186, v30
v_mul_f32 v187, v187, v30
v_mul_f32 v188, v188, v31
v_mul_f32 v189, v189, v31
v_mul_f32 v190, v190, v31
v_mul_f32 v191, v191, v31
v_mul_f32 v192, v192, v30
v_mul_f32 v193, v193, v30
v_mul_f32 v194, v194, v30
v_mul_f32 v195, v195, v30
v_mul_f32 v196, v196, v31
v_mul_f32 v197, v197, v31
v_mul_f32 v198, v198, v31
v_mul_f32 v199, v199, v31
v_mul_f32 v200, v200, v30
v_mul_f32 v201, v201, v30
v_mul_f32 v202, v202, v30
v_mul_f32 v203, v203, v30
v_mul_f32 v204, v204, v31
v_mul_f32 v205, v205, v31
v_mul_f32 v206, v206, v31
v_mul_f32 v207, v207, v31
v_mul_f32 v208, v208, v30
v_mul_f32 v209, v209, v30
v_mul_f32 v210, v210, v30
v_mul_f32 v211, v211, v30
v_mul_f32 v212, v212, v31
v_mul_f32 v213, v213, v31
v_mul_f32 v214, v214, v31
v_mul_f32 v215, v215, v31
v_mul_f32 v216, v216, v30
v_mul_f32 v217, v217, v30
v_mul_f32 v218, v218, v30
v_mul_f32 v219, v219, v30
v_mul_f32 v220, v220, v31
v_mul_f32 v221, v221, v31
v_mul_f32 v222, v222, v31
v_mul_f32 v223, v223, v31
v_mul_f32 v224, v224, v30
v_mul_f32 v225, v225, v30
v_mul_f32 v226, v226, v30
v_mul_f32 v227, v227, v30
v_mul_f32 v228, v228, v31
v_mul_f32 v229, v229, v31
v_mul_f32 v230, v230, v31
v_mul_f32 v231, v231, v31
v_mul_f32 v232, v232, v30
v_mul_f32 v233, v233, v30
v_mul_f32 v234, v234, v30
v_mul_f32 v235, v235, v30
v_mul_f32 v236, v236, v31
v_mul_f32 v237, v237, v31
v_mul_f32 v238, v238, v31
v_mul_f32 v239, v239, v31
v_mul_f32 v240, v240, v30
v_mul_f32 v241, v241, v30
v_mul_f32 v242, v242, v30
v_mul_f32 v243, v243, v30
v_mul_f32 v244, v244, v31
v_mul_f32 v245, v245, v31
v_mul_f32 v246, v246, v31
v_mul_f32 v247, v247, v31
v_mul_f32 v248, v248, v30
v_mul_f32 v249, v249, v30
v_mul_f32 v250, v250, v30
v_mul_f32 v251, v251, v30
v_mul_f32 v252, v252, v31
v_mul_f32 v253, v253, v31
v_mul_f32 v254, v254, v31
v_mul_f32 v255, v255, v31
s_waitcnt vmcnt(28)
v_fmac_f32 v128, v48, v32
v_fmac_f32 v129, v49, v32
v_fmac_f32 v130, v50, v32
v_fmac_f32 v131, v51, v32
v_fmac_f32 v132, v52, v33
v_fmac_f32 v133, v53, v33
v_fmac_f32 v134, v54, v33
v_fmac_f32 v135, v55, v33
v_fmac_f32 v136, v56, v32
v_fmac_f32 v137, v57, v32
v_fmac_f32 v138, v58, v32
v_fmac_f32 v139, v59, v32
v_fmac_f32 v140, v60, v33
v_fmac_f32 v141, v61, v33
v_fmac_f32 v142, v62, v33
v_fmac_f32 v143, v63, v33
s_waitcnt vmcnt(24)
v_fmac_f32 v144, v64, v32
v_fmac_f32 v145, v65, v32
v_fmac_f32 v146, v66, v32
v_fmac_f32 v147, v67, v32
v_fmac_f32 v148, v68, v33
v_fmac_f32 v149, v69, v33
v_fmac_f32 v150, v70, v33
v_fmac_f32 v151, v71, v33
v_fmac_f32 v152, v72, v32
v_fmac_f32 v153, v73, v32
v_fmac_f32 v154, v74, v32
v_fmac_f32 v155, v75, v32
v_fmac_f32 v156, v76, v33
v_fmac_f32 v157, v77, v33
v_fmac_f32 v158, v78, v33
v_fmac_f32 v159, v79, v33
s_waitcnt vmcnt(20)
v_fmac_f32 v160, v80, v32
v_fmac_f32 v161, v81, v32
v_fmac_f32 v162, v82, v32
v_fmac_f32 v163, v83, v32
v_fmac_f32 v164, v84, v33
v_fmac_f32 v165, v85, v33
v_fmac_f32 v166, v86, v33
v_fmac_f32 v167, v87, v33
v_fmac_f32 v168, v88, v32
v_fmac_f32 v169, v89, v32
v_fmac_f32 v170, v90, v32
v_fmac_f32 v171, v91, v32
v_fmac_f32 v172, v92, v33
v_fmac_f32 v173, v93, v33
v_fmac_f32 v174, v94, v33
v_fmac_f32 v175, v95, v33
s_waitcnt vmcnt(16)
v_fmac_f32 v176, v96, v32
v_fmac_f32 v177, v97, v32
v_fmac_f32 v178, v98, v32
v_fmac_f32 v179, v99, v32
v_fmac_f32 v180, v100, v33
v_fmac_f32 v181, v101, v33
v_fmac_f32 v182, v102, v33
v_fmac_f32 v183, v103, v33
v_fmac_f32 v184, v104, v32
v_fmac_f32 v185, v105, v32
v_fmac_f32 v186, v106, v32
v_fmac_f32 v187, v107, v32
v_fmac_f32 v188, v108, v33
v_fmac_f32 v189, v109, v33
v_fmac_f32 v190, v110, v33
v_fmac_f32 v191, v111, v33
s_waitcnt vmcnt(12)
v_fmac_f32 v192, v112, v32
v_fmac_f32 v193, v113, v32
v_fmac_f32 v194, v114, v32
v_fmac_f32 v195, v115, v32
v_fmac_f32 v196, v116, v33
v_fmac_f32 v197, v117, v33
v_fmac_f32 v198, v118, v33
v_fmac_f32 v199, v119, v33
v_fmac_f32 v200, v120, v32
v_fmac_f32 v201, v121, v32
v_fmac_f32 v202, v122, v32
v_fmac_f32 v203, v123, v32
v_fmac_f32 v204, v124, v33
v_fmac_f32 v205, v125, v33
v_fmac_f32 v206, v126, v33
v_fmac_f32 v207, v127, v33
s_waitcnt vmcnt(8)
v_accvgpr_read_b32 v48, a0
v_accvgpr_read_b32 v49, a1
v_accvgpr_read_b32 v50, a2
v_accvgpr_read_b32 v51, a3
v_fmac_f32 v208, v48, v32
v_fmac_f32 v209, v49, v32
v_fmac_f32 v210, v50, v32
v_fmac_f32 v211, v51, v32
v_accvgpr_read_b32 v52, a4
v_accvgpr_read_b32 v53, a5
v_accvgpr_read_b32 v54, a6
v_accvgpr_read_b32 v55, a7
v_fmac_f32 v212, v52, v33
v_fmac_f32 v213, v53, v33
v_fmac_f32 v214, v54, v33
v_fmac_f32 v215, v55, v33
v_accvgpr_read_b32 v56, a8
v_accvgpr_read_b32 v57, a9
v_accvgpr_read_b32 v58, a10
v_accvgpr_read_b32 v59, a11
v_fmac_f32 v216, v56, v32
v_fmac_f32 v217, v57, v32
v_fmac_f32 v218, v58, v32
v_fmac_f32 v219, v59, v32
v_accvgpr_read_b32 v60, a12
v_accvgpr_read_b32 v61, a13
v_accvgpr_read_b32 v62, a14
v_accvgpr_read_b32 v63, a15
v_fmac_f32 v220, v60, v33
v_fmac_f32 v221, v61, v33
v_fmac_f32 v222, v62, v33
v_fmac_f32 v223, v63, v33
s_waitcnt vmcnt(4)
v_accvgpr_read_b32 v64, a16
v_accvgpr_read_b32 v65, a17
v_accvgpr_read_b32 v66, a18
v_accvgpr_read_b32 v67, a19
v_fmac_f32 v224, v64, v32
v_fmac_f32 v225, v65, v32
v_fmac_f32 v226, v66, v32
v_fmac_f32 v227, v67, v32
v_accvgpr_read_b32 v68, a20
v_accvgpr_read_b32 v69, a21
v_accvgpr_read_b32 v70, a22
v_accvgpr_read_b32 v71, a23
v_fmac_f32 v228, v68, v33
v_fmac_f32 v229, v69, v33
v_fmac_f32 v230, v70, v33
v_fmac_f32 v231, v71, v33
v_accvgpr_read_b32 v72, a24
v_accvgpr_read_b32 v73, a25
v_accvgpr_read_b32 v74, a26
v_accvgpr_read_b32 v75, a27
v_fmac_f32 v232, v72, v32
v_fmac_f32 v233, v73, v32
v_fmac_f32 v234, v74, v32
v_fmac_f32 v235, v75, v32
v_accvgpr_read_b32 v76, a28
v_accvgpr_read_b32 v77, a29
v_accvgpr_read_b32 v78, a30
v_accvgpr_read_b32 v79, a31
v_fmac_f32 v236, v76, v33
v_fmac_f32 v237, v77, v33
v_fmac_f32 v238, v78, v33
v_fmac_f32 v239, v79, v33
s_waitcnt vmcnt(0)
v_accvgpr_read_b32 v80, a32
v_accvgpr_read_b32 v81, a33
v_accvgpr_read_b32 v82, a34
v_accvgpr_read_b32 v83, a35
v_fmac_f32 v240, v80, v32
v_fmac_f32 v241, v81, v32
v_fmac_f32 v242, v82, v32
v_fmac_f32 v243, v83, v32
v_accvgpr_read_b32 v84, a36
v_accvgpr_read_b32 v85, a37
v_accvgpr_read_b32 v86, a38
v_accvgpr_read_b32 v87, a39
v_fmac_f32 v244, v84, v33
v_fmac_f32 v245, v85, v33
v_fmac_f32 v246, v86, v33
v_fmac_f32 v247, v87, v33
v_accvgpr_read_b32 v88, a40
v_accvgpr_read_b32 v89, a41
v_accvgpr_read_b32 v90, a42
v_accvgpr_read_b32 v91, a43
v_fmac_f32 v248, v88, v32
v_fmac_f32 v249, v89, v32
v_fmac_f32 v250, v90, v32
v_fmac_f32 v251, v91, v32
v_accvgpr_read_b32 v92, a44
v_accvgpr_read_b32 v93, a45
v_accvgpr_read_b32 v94, a46
v_accvgpr_read_b32 v95, a47
v_fmac_f32 v252, v92, v33
v_fmac_f32 v253, v93, v33
v_fmac_f32 v254, v94, v33
v_fmac_f32 v255, v95, v33
L_fin:
v_mov_b32 v26, v16
v_mov_b32 v27, v18
s_nop 1
v_permlane32_swap_b32 v26, v27
v_add_f32 v26, v26, v27
v_mov_b32 v27, v26
s_nop 1
v_permlane16_swap_b32 v26, v27
v_add_f32 v26, v26, v27
v_mov_b32 v27, v26
s_nop 1
v_permlane32_swap_b32 v26, v27
v_rcp_f32 v26, v26
v_rcp_f32 v27, v27
s_lshl_b32 s50, s69, 3
s_lshl_b32 s51, s49, 1
s_add_u32 s50, s50, s51
s_lshl_b32 s50, s50, 17
s_lshl_b32 s51, s68, 13
s_add_u32 s50, s50, s51
s_add_u32 s50, s50, 50331648
s_lshl_b32 s78, s69, 4
s_add_u32 s78, s78, s68
s_lshl_b32 s78, s78, 2
s_add_u32 s78, s78, 79694848
v_mul_f32 v128, v128, v26
v_mul_f32 v129, v129, v26
v_mul_f32 v130, v130, v26
v_mul_f32 v131, v131, v26
v_mul_f32 v136, v136, v26
v_mul_f32 v137, v137, v26
v_mul_f32 v138, v138, v26
v_mul_f32 v139, v139, v26
v_cvt_pk_bf16_f32 v64, v128, v129
v_cvt_pk_bf16_f32 v65, v130, v131
v_cvt_pk_bf16_f32 v66, v136, v137
v_cvt_pk_bf16_f32 v67, v138, v139
s_add_u32 s51, s50, 0
buffer_store_dwordx4 v[64:67], v14, s[36:39], s51 offen offset:0 sc1
v_mul_f32 v144, v144, v26
v_mul_f32 v145, v145, v26
v_mul_f32 v146, v146, v26
v_mul_f32 v147, v147, v26
v_mul_f32 v152, v152, v26
v_mul_f32 v153, v153, v26
v_mul_f32 v154, v154, v26
v_mul_f32 v155, v155, v26
v_cvt_pk_bf16_f32 v68, v144, v145
v_cvt_pk_bf16_f32 v69, v146, v147
v_cvt_pk_bf16_f32 v70, v152, v153
v_cvt_pk_bf16_f32 v71, v154, v155
s_add_u32 s51, s50, 0
buffer_store_dwordx4 v[68:71], v14, s[36:39], s51 offen offset:1024 sc1
v_mul_f32 v160, v160, v26
v_mul_f32 v161, v161, v26
v_mul_f32 v162, v162, v26
v_mul_f32 v163, v163, v26
v_mul_f32 v168, v168, v26
v_mul_f32 v169, v169, v26
v_mul_f32 v170, v170, v26
v_mul_f32 v171, v171, v26
v_cvt_pk_bf16_f32 v72, v160, v161
v_cvt_pk_bf16_f32 v73, v162, v163
v_cvt_pk_bf16_f32 v74, v168, v169
v_cvt_pk_bf16_f32 v75, v170, v171
s_add_u32 s51, s50, 0
buffer_store_dwordx4 v[72:75], v14, s[36:39], s51 offen offset:2048 sc1
v_mul_f32 v176, v176, v26
v_mul_f32 v177, v177, v26
v_mul_f32 v178, v178, v26
v_mul_f32 v179, v179, v26
v_mul_f32 v184, v184, v26
v_mul_f32 v185, v185, v26
v_mul_f32 v186, v186, v26
v_mul_f32 v187, v187, v26
v_cvt_pk_bf16_f32 v76, v176, v177
v_cvt_pk_bf16_f32 v77, v178, v179
v_cvt_pk_bf16_f32 v78, v184, v185
v_cvt_pk_bf16_f32 v79, v186, v187
s_add_u32 s51, s50, 0
buffer_store_dwordx4 v[76:79], v14, s[36:39], s51 offen offset:3072 sc1
v_mul_f32 v192, v192, v26
v_mul_f32 v193, v193, v26
v_mul_f32 v194, v194, v26
v_mul_f32 v195, v195, v26
v_mul_f32 v200, v200, v26
v_mul_f32 v201, v201, v26
v_mul_f32 v202, v202, v26
v_mul_f32 v203, v203, v26
v_cvt_pk_bf16_f32 v80, v192, v193
v_cvt_pk_bf16_f32 v81, v194, v195
v_cvt_pk_bf16_f32 v82, v200, v201
v_cvt_pk_bf16_f32 v83, v202, v203
s_add_u32 s51, s50, 4096
buffer_store_dwordx4 v[80:83], v14, s[36:39], s51 offen offset:0 sc1
v_mul_f32 v208, v208, v26
v_mul_f32 v209, v209, v26
v_mul_f32 v210, v210, v26
v_mul_f32 v211, v211, v26
v_mul_f32 v216, v216, v26
v_mul_f32 v217, v217, v26
v_mul_f32 v218, v218, v26
v_mul_f32 v219, v219, v26
v_cvt_pk_bf16_f32 v84, v208, v209
v_cvt_pk_bf16_f32 v85, v210, v211
v_cvt_pk_bf16_f32 v86, v216, v217
v_cvt_pk_bf16_f32 v87, v218, v219
s_add_u32 s51, s50, 4096
buffer_store_dwordx4 v[84:87], v14, s[36:39], s51 offen offset:1024 sc1
v_mul_f32 v224, v224, v26
v_mul_f32 v225, v225, v26
v_mul_f32 v226, v226, v26
v_mul_f32 v227, v227, v26
v_mul_f32 v232, v232, v26
v_mul_f32 v233, v233, v26
v_mul_f32 v234, v234, v26
v_mul_f32 v235, v235, v26
v_cvt_pk_bf16_f32 v88, v224, v225
v_cvt_pk_bf16_f32 v89, v226, v227
v_cvt_pk_bf16_f32 v90, v232, v233
v_cvt_pk_bf16_f32 v91, v234, v235
s_add_u32 s51, s50, 4096
buffer_store_dwordx4 v[88:91], v14, s[36:39], s51 offen offset:2048 sc1
v_mul_f32 v240, v240, v26
v_mul_f32 v241, v241, v26
v_mul_f32 v242, v242, v26
v_mul_f32 v243, v243, v26
v_mul_f32 v248, v248, v26
v_mul_f32 v249, v249, v26
v_mul_f32 v250, v250, v26
v_mul_f32 v251, v251, v26
v_cvt_pk_bf16_f32 v92, v240, v241
v_cvt_pk_bf16_f32 v93, v242, v243
v_cvt_pk_bf16_f32 v94, v248, v249
v_cvt_pk_bf16_f32 v95, v250, v251
s_add_u32 s51, s50, 4096
buffer_store_dwordx4 v[92:95], v14, s[36:39], s51 offen offset:3072 sc1
v_mul_f32 v132, v132, v27
v_mul_f32 v133, v133, v27
v_mul_f32 v134, v134, v27
v_mul_f32 v135, v135, v27
v_mul_f32 v140, v140, v27
v_mul_f32 v141, v141, v27
v_mul_f32 v142, v142, v27
v_mul_f32 v143, v143, v27
v_cvt_pk_bf16_f32 v64, v132, v133
v_cvt_pk_bf16_f32 v65, v134, v135
v_cvt_pk_bf16_f32 v66, v140, v141
v_cvt_pk_bf16_f32 v67, v142, v143
s_add_u32 s51, s50, 131072
buffer_store_dwordx4 v[64:67], v14, s[36:39], s51 offen offset:0 sc1
v_mul_f32 v148, v148, v27
v_mul_f32 v149, v149, v27
v_mul_f32 v150, v150, v27
v_mul_f32 v151, v151, v27
v_mul_f32 v156, v156, v27
v_mul_f32 v157, v157, v27
v_mul_f32 v158, v158, v27
v_mul_f32 v159, v159, v27
v_cvt_pk_bf16_f32 v68, v148, v149
v_cvt_pk_bf16_f32 v69, v150, v151
v_cvt_pk_bf16_f32 v70, v156, v157
v_cvt_pk_bf16_f32 v71, v158, v159
s_add_u32 s51, s50, 131072
buffer_store_dwordx4 v[68:71], v14, s[36:39], s51 offen offset:1024 sc1
v_mul_f32 v164, v164, v27
v_mul_f32 v165, v165, v27
v_mul_f32 v166, v166, v27
v_mul_f32 v167, v167, v27
v_mul_f32 v172, v172, v27
v_mul_f32 v173, v173, v27
v_mul_f32 v174, v174, v27
v_mul_f32 v175, v175, v27
v_cvt_pk_bf16_f32 v72, v164, v165
v_cvt_pk_bf16_f32 v73, v166, v167
v_cvt_pk_bf16_f32 v74, v172, v173
v_cvt_pk_bf16_f32 v75, v174, v175
s_add_u32 s51, s50, 131072
buffer_store_dwordx4 v[72:75], v14, s[36:39], s51 offen offset:2048 sc1
v_mul_f32 v180, v180, v27
v_mul_f32 v181, v181, v27
v_mul_f32 v182, v182, v27
v_mul_f32 v183, v183, v27
v_mul_f32 v188, v188, v27
v_mul_f32 v189, v189, v27
v_mul_f32 v190, v190, v27
v_mul_f32 v191, v191, v27
v_cvt_pk_bf16_f32 v76, v180, v181
v_cvt_pk_bf16_f32 v77, v182, v183
v_cvt_pk_bf16_f32 v78, v188, v189
v_cvt_pk_bf16_f32 v79, v190, v191
s_add_u32 s51, s50, 131072
buffer_store_dwordx4 v[76:79], v14, s[36:39], s51 offen offset:3072 sc1
v_mul_f32 v196, v196, v27
v_mul_f32 v197, v197, v27
v_mul_f32 v198, v198, v27
v_mul_f32 v199, v199, v27
v_mul_f32 v204, v204, v27
v_mul_f32 v205, v205, v27
v_mul_f32 v206, v206, v27
v_mul_f32 v207, v207, v27
v_cvt_pk_bf16_f32 v80, v196, v197
v_cvt_pk_bf16_f32 v81, v198, v199
v_cvt_pk_bf16_f32 v82, v204, v205
v_cvt_pk_bf16_f32 v83, v206, v207
s_add_u32 s51, s50, 135168
buffer_store_dwordx4 v[80:83], v14, s[36:39], s51 offen offset:0 sc1
v_mul_f32 v212, v212, v27
v_mul_f32 v213, v213, v27
v_mul_f32 v214, v214, v27
v_mul_f32 v215, v215, v27
v_mul_f32 v220, v220, v27
v_mul_f32 v221, v221, v27
v_mul_f32 v222, v222, v27
v_mul_f32 v223, v223, v27
v_cvt_pk_bf16_f32 v84, v212, v213
v_cvt_pk_bf16_f32 v85, v214, v215
v_cvt_pk_bf16_f32 v86, v220, v221
v_cvt_pk_bf16_f32 v87, v222, v223
s_add_u32 s51, s50, 135168
buffer_store_dwordx4 v[84:87], v14, s[36:39], s51 offen offset:1024 sc1
v_mul_f32 v228, v228, v27
v_mul_f32 v229, v229, v27
v_mul_f32 v230, v230, v27
v_mul_f32 v231, v231, v27
v_mul_f32 v236, v236, v27
v_mul_f32 v237, v237, v27
v_mul_f32 v238, v238, v27
v_mul_f32 v239, v239, v27
v_cvt_pk_bf16_f32 v88, v228, v229
v_cvt_pk_bf16_f32 v89, v230, v231
v_cvt_pk_bf16_f32 v90, v236, v237
v_cvt_pk_bf16_f32 v91, v238, v239
s_add_u32 s51, s50, 135168
buffer_store_dwordx4 v[88:91], v14, s[36:39], s51 offen offset:2048 sc1
v_mul_f32 v244, v244, v27
v_mul_f32 v245, v245, v27
v_mul_f32 v246, v246, v27
v_mul_f32 v247, v247, v27
v_mul_f32 v252, v252, v27
v_mul_f32 v253, v253, v27
v_mul_f32 v254, v254, v27
v_mul_f32 v255, v255, v27
v_cvt_pk_bf16_f32 v92, v244, v245
v_cvt_pk_bf16_f32 v93, v246, v247
v_cvt_pk_bf16_f32 v94, v252, v253
v_cvt_pk_bf16_f32 v95, v254, v255
s_add_u32 s51, s50, 135168
buffer_store_dwordx4 v[92:95], v14, s[36:39], s51 offen offset:3072 sc1
s_cmp_eq_u32 s74, 3
s_cbranch_scc1 L_s2
L_sig:
s_waitcnt vmcnt(0)
s_barrier
v_mov_b32 v27, 1
s_cmp_eq_u32 s49, 0
s_cselect_b32 exec_lo, 1, 0
s_mov_b32 exec_hi, 0
buffer_store_dword v27, off, s[36:39], s78 sc1
s_mov_b64 exec, -1
s_branch L_done
L_s2:
s_mov_b32 s74, 2
s_mov_b32 s70, 0
s_mov_b32 s71, s75
s_mov_b32 s69, 1000
s_branch L_seg2
L_done:
)ASM"
  :: "s"(c), "s"(wuk), "s"(wuv), "s"(ws), "s"(blockIdx.x), "v"(threadIdx.x), "s"((unsigned)(size_t)lds), "s"(x), "s"(wq), "s"(kin), "s"(vin), "s"(shk), "s"(wo), "s"(ssk) : "v8", "v9", "v10", "v11", "v12", "v13", "v14", "v15", "v16", "v17", "v18", "v19", "v20", "v21", "v22", "v23", "v24", "v25", "v26", "v27", "v28", "v29", "v30", "v31", "v32", "v33", "v34", "v35", "v36", "v37", "v38", "v39", "v40", "v41", "v42", "v43", "v44", "v45", "v46", "v47", "v48", "v49", "v50", "v51", "v52", "v53", "v54", "v55", "v56", "v57", "v58", "v59", "v60", "v61", "v62", "v63", "v64", "v65", "v66", "v67", "v68", "v69", "v70", "v71", "v72", "v73", "v74", "v75", "v76", "v77", "v78", "v79", "v80", "v81", "v82", "v83", "v84", "v85", "v86", "v87", "v88", "v89", "v90", "v91", "v92", "v93", "v94", "v95", "v96", "v97", "v98", "v99", "v100", "v101", "v102", "v103", "v104", "v105", "v106", "v107", "v108", "v109", "v110", "v111", "v112", "v113", "v114", "v115", "v116", "v117", "v118", "v119", "v120", "v121", "v122", "v123", "v124", "v125", "v126", "v127", "v128", "v129", "v130", "v131", "v132", "v133", "v134", "v135", "v136", "v137", "v138", "v139", "v140", "v141", "v142", "v143", "v144", "v145", "v146", "v147", "v148", "v149", "v150", "v151", "v152", "v153", "v154", "v155", "v156", "v157", "v158", "v159", "v160", "v161", "v162", "v163", "v164", "v165", "v166", "v167", "v168", "v169", "v170", "v171", "v172", "v173", "v174", "v175", "v176", "v177", "v178", "v179", "v180", "v181", "v182", "v183", "v184", "v185", "v186", "v187", "v188", "v189", "v190", "v191", "v192", "v193", "v194", "v195", "v196", "v197", "v198", "v199", "v200", "v201", "v202", "v203", "v204", "v205", "v206", "v207", "v208", "v209", "v210", "v211", "v212", "v213", "v214", "v215", "v216", "v217", "v218", "v219", "v220", "v221", "v222", "v223", "v224", "v225", "v226", "v227", "v228", "v229", "v230", "v231", "v232", "v233", "v234", "v235", "v236", "v237", "v238", "v239", "v240", "v241", "v242", "v243", "v244", "v245", "v246", "v247", "v248", "v249", "v250", "v251", "v252", "v253", "v254", "v255", "a0", "a1", "a2", "a3", "a4", "a5", "a6", "a7", "a8", "a9", "a10", "a11", "a12", "a13", "a14", "a15", "a16", "a17", "a18", "a19", "a20", "a21", "a22", "a23", "a24", "a25", "a26", "a27", "a28", "a29", "a30", "a31", "a32", "a33", "a34", "a35", "a36", "a37", "a38", "a39", "a40", "a41", "a42", "a43", "a44", "a45", "a46", "a47", "a48", "a49", "a50", "a51", "a52", "a53", "a54", "a55", "a56", "a57", "a58", "a59", "a60", "a61", "a62", "a63", "a64", "a65", "a66", "a67", "a68", "a69", "a70", "a71", "a72", "a73", "a74", "a75", "a76", "a77", "a78", "a79", "a80", "a81", "a82", "a83", "a84", "a85", "a86", "a87", "a88", "a89", "a90", "a91", "a92", "a93", "a94", "a95", "a96", "a97", "a98", "a99", "a100", "a101", "a102", "a103", "a104", "a105", "a106", "a107", "a108", "a109", "a110", "a111", "a112", "a113", "a114", "a115", "a116", "a117", "a118", "a119", "a120", "a121", "a122", "a123", "a124", "a125", "a126", "a127", "a128", "a129", "a130", "a131", "a132", "a133", "a134", "a135", "a136", "a137", "a138", "a139", "a140", "a141", "a142", "a143", "a144", "a145", "a146", "a147", "a148", "a149", "a150", "a151", "a152", "a153", "a154", "a155", "a156", "a157", "a158", "a159", "a160", "a161", "a162", "a163", "a164", "a165", "a166", "a167", "a168", "a169", "a170", "a171", "a172", "a173", "a174", "a175", "a176", "a177", "a178", "a179", "a180", "a181", "a182", "a183", "a184", "a185", "a186", "a187", "a188", "a189", "a190", "a191", "a192", "a193", "a194", "a195", "a196", "a197", "a198", "a199", "a200", "a201", "a202", "a203", "a204", "a205", "a206", "a207", "a208", "a209", "a210", "a211", "a212", "a213", "a214", "a215", "a216", "a217", "a218", "a219", "a220", "a221", "a222", "a223", "a224", "a225", "a226", "a227", "a228", "a229", "a230", "a231", "a232", "a233", "a234", "a235", "a236", "a237", "a238", "a239", "a240", "a241", "a242", "a243", "a244", "a245", "a246", "a247", "a248", "a249", "a250", "a251", "a252", "a253", "a254", "a255", "s24", "s25", "s26", "s27", "s28", "s29", "s30", "s31", "s34", "s35", "s36", "s37", "s38", "s39", "s40", "s41", "s42", "s43", "s44", "s45", "s46", "s47", "s48", "s49", "s50", "s51", "s52", "s53", "s54", "s55", "s56", "s57", "s58", "s59", "s60", "s61", "s62", "s63", "s64", "s65", "s66", "s67", "s68", "s69", "s70", "s71", "s72", "s73", "s74", "s75", "s76", "s77", "s78", "s79", "s80", "s81", "s82", "s83", "vcc", "scc", "m0", "memory");
  const int idx = blockIdx.x >> 3, pr = idx >> 1;
  if (pr >= 8 && pr < 15) conv_job((2 * (blockIdx.x & 7) + (idx & 1)) * 7 + (pr - 8), lds, ssk, ssd, ws);
  otile_job(blockIdx.x, lds, x, gw, ws, out);
}

__global__ __launch_bounds__(256) void kz_flags(u32x4* f, const char* c, const char* wuk, const char* wuv,
                                                const char* shk, const char* ssd, const char* gw) {
  u32x4 z = {0u, 0u, 0u, 0u};
  if (blockIdx.x < 5) f[blockIdx.x * 256 + threadIdx.x] = z;
  const unsigned b = blockIdx.x, t = threadIdx.x;
  unsigned d1;
  const char* pw = ((b & 1) ? wuv : wuk) + (size_t)(b >> 1) * 24576u;
  const unsigned tt = t < 56u ? t : 0u;
  const char* pc = c + (size_t)(8u * b + tt / 7u) * 2048u + (tt % 7u) * 128u;
  unsigned d0;
  pw += (t < 192u ? t : 0u) * 128u;
  if (t >= 192u && t < 220u) pw = shk + (t - 192u) * 128u;
  if (t >= 64u && t < 71u) pc = ssd + (t - 64u) * 128u;
  if (t >= 128u && t < 135u) pc = gw + (t - 128u) * 128u;
  asm volatile("global_load_dword %0, %2, off\n global_load_dword %1, %3, off"
               : "=&v"(d0), "=&v"(d1) : "v"(pw), "v"(pc) : "memory");
  asm volatile("s_getpc_b64 s[24:25]\n"
               "L_kz0:\n"
               "s_mov_b32 s26, L_kz0-L_kf0\n"
               "s_sub_u32 s24, s24, s26\n"
               "s_subb_u32 s25, s25, 0\n"
               "s_and_b32 s25, s25, 0xffff\n"
               "s_cmp_lt_u32 s26, 0x14000\n"
               "s_cselect_b32 s26, s26, 0\n"
               "s_mov_b32 s27, 0x20000\n"
               "v_lshlrev_b32 v8, 4, %0\n"
               "s_mov_b32 s28, 4096\n"
               "s_mov_b32 s29, 8192\n"
               "buffer_load_dwordx4 v[12:15], v8, s[24:27], 0 offen\n"
               "buffer_load_dwordx4 v[16:19], v8, s[24:27], s28 offen\n"
               "buffer_load_dwordx4 v[20:23], v8, s[24:27], s29 offen\n"
               "s_waitcnt vmcnt(0)\n"
               :: "v"(threadIdx.x) : "s24", "s25", "s26", "s27", "s28", "s29", "v8", "v12", "v13", "v14", "v15", "v16", "v17", "v18", "v19",
                  "v20", "v21", "v22", "v23", "scc", "memory");
  asm volatile("" :: "v"(d0), "v"(d1));
}
extern "C" void kernel_launch(void* const* d_in, const int* in_sizes, int n_in,
                              void* d_out, int out_size, void* d_ws, size_t ws_size,
                              hipStream_t stream) {
  kz_flags<<<256, 256, 0, stream>>>((u32x4*)((char*)d_ws + WS_FLAG), (const char*)d_in[7], (const char*)d_in[9], (const char*)d_in[10],
                                    (const char*)d_in[3], (const char*)d_in[5], (const char*)d_in[12]);
  kf_main<<<256, 256, 0, stream>>>(d_in[7], d_in[9], d_in[10], (char*)d_ws, (const u16*)d_in[0], (const u16*)d_in[8],
                                  (const u16*)d_in[1], (const u16*)d_in[2], (const u16*)d_in[3], (const u16*)d_in[11], (const u16*)d_in[12],
                                  (const u16*)d_in[4], (const u16*)d_in[5], (u16*)d_out);
}
```
